```python
import torch

def _softmax(x, axis=-1):
    return torch.softmax(x, dim=axis)

def _gelu(x):
    return torch.nn.functional.gelu(x, approximate='tanh')

def _silu(x):
    return torch.nn.functional.silu(x)

def _sigmoid(x):
    return torch.sigmoid(x)

def _layernorm(x, axis=-1, eps=1e-05):
    m = x.mean(dim=axis, keepdim=True)
    v = x.var(dim=axis, keepdim=True, unbiased=False)
    return (x - m) / torch.sqrt(v + eps)

def _scatter_add(tgt, idx, src, axis=0):
    return tgt.clone().index_add_(axis, idx, src)

def _segment_sum(src, seg, n):
    out = torch.zeros((n,) + src.shape[1:], dtype=src.dtype)
    return out.index_add_(0, seg, src)

def _pad(x, pw):
    flat = tuple((v for pair in reversed(pw) for v in pair))
    return torch.nn.functional.pad(x, flat)
kernel_name = 'gated_mix__memory_aug_recurrent_mem_tr__glu_gated_swiglu__prompt'

def setup_inputs():

    _w = lambda a: (lambda t: t.to(torch.bfloat16) if t.is_floating_point() else t)(torch.as_tensor(a))

    def _A_setup():
        B, S, Dm = (1, 2048, 640)
        H, Dh = (32, 128)
        M, seg = (8, 32)
        seg = min(seg, S)
        S = S // seg * seg
        return {'x': _w((lambda *s: torch.randn(*s, dtype=torch.bfloat16) * 0.05)(B, S, Dm)), 'mem0': _w((lambda *s: torch.randn(*s, dtype=torch.bfloat16) * 0.8)(B, M, Dm)), 'Wq': _w((lambda *s: torch.randn(*s, dtype=torch.bfloat16) * 1.6)(Dm, H, Dh) * Dm ** (-0.5)), 'Wk': _w((lambda *s: torch.randn(*s, dtype=torch.bfloat16) * 1.6)(Dm, H, Dh) * Dm ** (-0.5)), 'Wv': _w((lambda *s: torch.randn(*s, dtype=torch.bfloat16) * 0.8)(Dm, H, Dh) * Dm ** (-0.5)), 'Wo': _w((lambda *s: torch.randn(*s, dtype=torch.bfloat16) * 0.8)(H, Dh, Dm) * (H * Dh) ** (-0.5)), 'seg_len': seg}
    _A_in = _A_setup()

    def _B_setup():
        B, S, D, F = (1, 2048, 640, 1728)
        return {'x': _w((lambda *s: torch.randn(*s, dtype=torch.bfloat16) * 0.05)(B, S, D)), 'w1': _w((lambda *s: torch.randn(*s, dtype=torch.bfloat16) * 3.2)(D, F) * (2.0 / D) ** 0.5), 'w3': _w((lambda *s: torch.randn(*s, dtype=torch.bfloat16) * 3.2)(D, F) * (2.0 / D) ** 0.5), 'w2': _w((lambda *s: torch.randn(*s, dtype=torch.bfloat16) * 3.2)(F, D) * (2.0 / F) ** 0.5)}
    _B_in = _B_setup()
    return {'A_x': _A_in['x'], 'A_mem0': _A_in['mem0'], 'A_Wq': _A_in['Wq'], 'A_Wk': _A_in['Wk'], 'A_Wv': _A_in['Wv'], 'A_Wo': _A_in['Wo'], 'A_seg_len': _A_in['seg_len'], 'B_x': _B_in['x'], 'B_w1': _B_in['w1'], 'B_w3': _B_in['w3'], 'B_w2': _B_in['w2'], 'gate_w': _w((lambda *s: torch.randn(*s, dtype=torch.bfloat16) * 0.05)(640) * 16.0)}

def reference(A_x, A_mem0, A_Wq, A_Wk, A_Wv, A_Wo, A_seg_len, B_x, B_w1, B_w3, B_w2, gate_w):

    def _A_ref(x, mem0, Wq, Wk, Wv, Wo, seg_len):
        B, S, Dm = x.shape
        H, Dh = (Wq.shape[1], Wq.shape[2])
        M = mem0.shape[1]
        scale = Dh ** (-0.5)
        n_seg = S // seg_len
        L = M + seg_len
        idx = torch.arange(L)
        mask = (idx[:, None] < M) | (idx[:, None] >= idx[None, :])
        mem = mem0
        outs = []
        for s in range(n_seg):
            xs = x[:, s * seg_len:(s + 1) * seg_len, :].clone()
            h = torch.concatenate([mem, xs], axis=1)
            q = torch.einsum('btd,dhe->bhte', h, Wq)
            k = torch.einsum('btd,dhe->bhte', h, Wk)
            v = torch.einsum('btd,dhe->bhte', h, Wv)
            scores = torch.einsum('bhqe,bhke->bhqk', q, k) * scale
            scores = torch.where(mask[None, None], scores, -1000000000.0)
            p = _softmax(scores, axis=-1)
            o = torch.einsum('bhqk,bhke->bhqe', p, v)
            o = torch.einsum('bhte,hed->btd', o, Wo)
            mem = o[:, :M, :].clone()
            outs.append(o[:, M:, :])
        return torch.concatenate(outs, axis=1)

    def _B_ref(x, w1, w3, w2):
        gate = torch.einsum('bsd,df->bsf', x, w1)
        up = torch.einsum('bsd,df->bsf', x, w3)
        h = _silu(gate) * up
        out = torch.einsum('bsf,fd->bsd', h, w2)
        return out
    x = A_x
    g = _sigmoid((x * gate_w).sum(-1, keepdims=True))
    a = _A_ref(x, A_mem0, A_Wq, A_Wk, A_Wv, A_Wo, A_seg_len)
    b = _B_ref(x, B_w1, B_w3, B_w2)
    return g * a + (1 - g) * b
```

```cpp
#include <hip/hip_runtime.h>
#include <hip/hip_bf16.h>
#include <stdint.h>

namespace nv {
typedef unsigned short bf;
__device__ __forceinline__ float b2f(bf b) { return __uint_as_float(((unsigned)b) << 16); }
__device__ __forceinline__ bf f2b(float f) { unsigned u = __float_as_uint(f); u += 0x7FFFu + ((u >> 16) & 1u); return (bf)(u >> 16); }
constexpr int S = 2048, DM = 640, H = 32, DH = 128, M = 8, SEG = 32, NSEG = 64, F = 1728, HD = 4096, L = 40;

__global__ void gemm(const bf* A, int lda, const bf* B, int ldb, bf* C, int ldc, int Mr, int N, int K) {
  __shared__ float As[16][17], Bs[16][17];
  int tx = threadIdx.x & 15, ty = threadIdx.x >> 4;
  int m = blockIdx.y * 16 + ty, n = blockIdx.x * 16 + tx;
  float acc = 0.f;
  for (int k0 = 0; k0 < K; k0 += 16) {
    As[ty][tx] = (m < Mr) ? b2f(A[(size_t)m * lda + k0 + tx]) : 0.f;
    Bs[ty][tx] = b2f(B[(size_t)(k0 + ty) * ldb + n]);
    __syncthreads();
    for (int k = 0; k < 16; ++k) acc += As[ty][k] * Bs[k][tx];
    __syncthreads();
  }
  if (m < Mr) C[(size_t)m * ldc + n] = f2b(acc);
}
__global__ void attn(const bf* qm, const bf* km, const bf* vm, const bf* qx, const bf* kx, const bf* vx, bf* om, bf* ox, int do_mem, int do_x) {
  int h = blockIdx.x, s = blockIdx.y, t = threadIdx.x;
  if (t >= L) return;
  if (t < M && !do_mem) return;
  if (t >= M && !do_x) return;
  const bf* kmS = km + (size_t)s * M * HD; const bf* vmS = vm + (size_t)s * M * HD; const bf* qmS = qm + (size_t)s * M * HD;
  const bf* kxS = kx + (size_t)s * SEG * HD; const bf* vxS = vx + (size_t)s * SEG * HD; const bf* qxS = qx + (size_t)s * SEG * HD;
  const bf* q = (t < M) ? qmS + (size_t)t * HD + h * DH : qxS + (size_t)(t - M) * HD + h * DH;
  float sc[L]; float mx = -1e30f;
  const float scale = b2f(f2b(0.08838834764831845f)) * 0.f + 0.08838834764831845f;
  for (int j = 0; j < L; ++j) {
    const bf* k = (j < M) ? kmS + (size_t)j * HD + h * DH : kxS + (size_t)(j - M) * HD + h * DH;
    float a = 0.f;
    for (int e = 0; e < DH; ++e) a += b2f(q[e]) * b2f(k[e]);
    a = b2f(f2b(a)); a = b2f(f2b(a * scale));
    bool ok = (t < M) || (t >= j);
    sc[j] = ok ? a : -1e9f; mx = fmaxf(mx, sc[j]);
  }
  float sum = 0.f;
  for (int j = 0; j < L; ++j) { sc[j] = expf(sc[j] - mx); sum += sc[j]; }
  for (int j = 0; j < L; ++j) sc[j] = b2f(f2b(sc[j] / sum));
  bf* o = (t < M) ? om + (size_t)s * M * HD + (size_t)t * HD + h * DH : ox + (size_t)s * SEG * HD + (size_t)(t - M) * HD + h * DH;
  for (int e = 0; e < DH; ++e) {
    float a = 0.f;
    for (int j = 0; j < L; ++j) { const bf* v = (j < M) ? vmS + (size_t)j * HD + h * DH : vxS + (size_t)(j - M) * HD + h * DH; a += sc[j] * b2f(v[e]); }
    o[e] = f2b(a);
  }
}
__global__ void swiglu(const bf* g, const bf* u, bf* hh, int n) {
  int i = blockIdx.x * 256 + threadIdx.x; if (i >= n) return;
  float x = b2f(g[i]); float s = b2f(f2b(x / (1.f + expf(-x)))); hh[i] = f2b(s * b2f(u[i]));
}
__global__ void fin(const bf* x, const bf* gw, const bf* a, const bf* b, bf* out) {
  int r = blockIdx.x; __shared__ float red[64];
  float acc = 0.f;
  for (int d = threadIdx.x; d < DM; d += 64) acc += b2f(f2b(b2f(x[r * DM + d]) * b2f(gw[d])));
  red[threadIdx.x] = acc; __syncthreads();
  float s = 0.f; for (int i = 0; i < 64; ++i) s += red[i];
  s = b2f(f2b(s)); float g = b2f(f2b(1.f / (1.f + expf(-s)))); float omg = b2f(f2b(1.f - g));
  for (int d = threadIdx.x; d < DM; d += 64) {
    float t1 = b2f(f2b(g * b2f(a[r * DM + d]))), t2 = b2f(f2b(omg * b2f(b[r * DM + d])));
    out[r * DM + d] = f2b(t1 + t2);
  }
}
struct WS { bf *qx, *kx, *vx, *ox, *qm, *km, *vm, *om, *mem, *a, *g, *u, *hh, *b; };
inline WS carve(void* ws) {
  WS w; bf* p = (bf*)ws;
  w.qx = p; p += (size_t)S * HD; w.kx = p; p += (size_t)S * HD; w.vx = p; p += (size_t)S * HD; w.ox = p; p += (size_t)S * HD;
  w.qm = p; p += (size_t)NSEG * M * HD; w.km = p; p += (size_t)NSEG * M * HD; w.vm = p; p += (size_t)NSEG * M * HD; w.om = p; p += (size_t)NSEG * M * HD;
  w.mem = p; p += (size_t)(NSEG + 1) * M * DM; w.a = p; p += (size_t)S * DM; w.g = p; p += (size_t)S * F; w.u = p; p += (size_t)S * F; w.hh = p; p += (size_t)S * F; w.b = p; p += (size_t)S * DM;
  return w;
}
inline void run(void* const* d_in, void* d_out, void* d_ws, hipStream_t st) {
  const bf* x = (const bf*)d_in[0]; const bf* mem0 = (const bf*)d_in[1]; const bf* Wq = (const bf*)d_in[2]; const bf* Wk = (const bf*)d_in[3];
  const bf* Wv = (const bf*)d_in[4]; const bf* Wo = (const bf*)d_in[5]; const bf* w1 = (const bf*)d_in[8]; const bf* w3 = (const bf*)d_in[9];
  const bf* w2 = (const bf*)d_in[10]; const bf* gw = (const bf*)d_in[11];
  WS w = carve(d_ws);
  gemm<<<dim3(HD / 16, S / 16), 256, 0, st>>>(x, DM, Wq, HD, w.qx, HD, S, HD, DM);
  gemm<<<dim3(HD / 16, S / 16), 256, 0, st>>>(x, DM, Wk, HD, w.kx, HD, S, HD, DM);
  gemm<<<dim3(HD / 16, S / 16), 256, 0, st>>>(x, DM, Wv, HD, w.vx, HD, S, HD, DM);
  hipMemcpyAsync(w.mem, mem0, M * DM * 2, hipMemcpyDeviceToDevice, st);
  for (int s = 0; s < NSEG; ++s) {
    const bf* mm = w.mem + (size_t)s * M * DM;
    gemm<<<dim3(HD / 16, 1), 256, 0, st>>>(mm, DM, Wq, HD, w.qm + (size_t)s * M * HD, HD, M, HD, DM);
    gemm<<<dim3(HD / 16, 1), 256, 0, st>>>(mm, DM, Wk, HD, w.km + (size_t)s * M * HD, HD, M, HD, DM);
    gemm<<<dim3(HD / 16, 1), 256, 0, st>>>(mm, DM, Wv, HD, w.vm + (size_t)s * M * HD, HD, M, HD, DM);
    attn<<<dim3(H, 1), 64, 0, st>>>(w.qm + (size_t)s * M * HD, w.km + (size_t)s * M * HD, w.vm + (size_t)s * M * HD,
                                    w.qx + (size_t)s * SEG * HD, w.kx + (size_t)s * SEG * HD, w.vx + (size_t)s * SEG * HD,
                                    w.om + (size_t)s * M * HD, w.ox + (size_t)s * SEG * HD, 1, 0);
    gemm<<<dim3(DM / 16, 1), 256, 0, st>>>(w.om + (size_t)s * M * HD, HD, Wo, DM, w.mem + (size_t)(s + 1) * M * DM, DM, M, DM, HD);
  }
  attn<<<dim3(H, NSEG), 64, 0, st>>>(w.qm, w.km, w.vm, w.qx, w.kx, w.vx, w.om, w.ox, 0, 1);
  gemm<<<dim3(DM / 16, S / 16), 256, 0, st>>>(w.ox, HD, Wo, DM, w.a, DM, S, DM, HD);
  gemm<<<dim3(F / 16, S / 16), 256, 0, st>>>(x, DM, w1, F, w.g, F, S, F, DM);
  gemm<<<dim3(F / 16, S / 16), 256, 0, st>>>(x, DM, w3, F, w.u, F, S, F, DM);
  swiglu<<<(S * F + 255) / 256, 256, 0, st>>>(w.g, w.u, w.hh, S * F);
  gemm<<<dim3(DM / 16, S / 16), 256, 0, st>>>(w.hh, F, w2, DM, w.b, DM, S, DM, F);
  fin<<<S, 64, 0, st>>>(x, gw, w.a, w.b, (bf*)d_out);
}
}
extern "C" void kernel_launch(void* const* d_in, const int* in_sizes, int n_in, void* d_out, int out_size, void* d_ws, size_t ws_size, hipStream_t stream) {
  nv::run(d_in, d_out, d_ws, stream);
}
```

```cpp
#include <hip/hip_runtime.h>
#include <stdint.h>
#define P_AN 1
#define P_PC 15
#define P_P0 10
#define PD_O 12
#define PD_H 12
#define P_YS 1
#define P_BW 512
#define P_DH 3065
#define P_DY 1525
#define P_DR 1300
#define P_FH 6
#define P_FY (500 + (0 << 16))
#define P_DF 300
#define P_MASK 0
#define P_FORCE -1
#define P_AD 0
#define ASM_H \
  "s_cmp_lg_u32 s84, 0\n" \
  "s_cselect_b64 vcc, -1, 0\n" \
  "v_mov_b32 v80, v112\n" \
  "v_mov_b32 v81, v113\n" \
  "v_mov_b32 v82, v114\n" \
  "v_mov_b32 v83, v115\n" \
  "s_waitcnt vmcnt(28)\n" \
  "s_cbranch_vccnz HV0_%=\n" \
  "ds_read_b64_tr_b16 a[0:1], v82 offset:0\n" \
  "ds_read_b64_tr_b16 a[2:3], v82 offset:1024\n" \
  "ds_read_b64_tr_b16 a[4:5], v82 offset:2048\n" \
  "ds_read_b64_tr_b16 a[6:7], v82 offset:3072\n" \
  "ds_read_b64_tr_b16 a[8:9], v83 offset:0\n" \
  "ds_read_b64_tr_b16 a[10:11], v83 offset:1024\n" \
  "ds_read_b64_tr_b16 a[12:13], v83 offset:2048\n" \
  "ds_read_b64_tr_b16 a[14:15], v83 offset:3072\n" \
  "s_branch HW0_%=\n" \
  "HV0_%=:\n" \
  "ds_read_b64_tr_b16 a[48:49], v82 offset:0\n" \
  "ds_read_b64_tr_b16 a[50:51], v82 offset:1024\n" \
  "ds_read_b64_tr_b16 a[52:53], v82 offset:2048\n" \
  "ds_read_b64_tr_b16 a[54:55], v82 offset:3072\n" \
  "ds_read_b64_tr_b16 a[56:57], v83 offset:0\n" \
  "ds_read_b64_tr_b16 a[58:59], v83 offset:1024\n" \
  "ds_read_b64_tr_b16 a[60:61], v83 offset:2048\n" \
  "ds_read_b64_tr_b16 a[62:63], v83 offset:3072\n" \
  "HW0_%=:\n" \
  "s_waitcnt lgkmcnt(0)\n" \
  "s_add_u32 m0, s34, 0\n" \
  "s_nop 0\n" \
  "buffer_load_dwordx4 v108, s[20:23], s32 offen lds\n" \
  "buffer_load_dwordx4 v109, s[20:23], s32 offen offset:1024 lds\n" \
  "buffer_load_dwordx4 v110, s[20:23], s32 offen offset:2048 lds\n" \
  "buffer_load_dwordx4 v111, s[20:23], s32 offen offset:3072 lds\n" \
  "s_waitcnt vmcnt(28)\n" \
  "s_cbranch_vccnz HV1_%=\n" \
  "ds_read_b64_tr_b16 a[16:17], v82 offset:4096\n" \
  "ds_read_b64_tr_b16 a[18:19], v82 offset:5120\n" \
  "ds_read_b64_tr_b16 a[20:21], v82 offset:6144\n" \
  "ds_read_b64_tr_b16 a[22:23], v82 offset:7168\n" \
  "ds_read_b64_tr_b16 a[24:25], v83 offset:4096\n" \
  "ds_read_b64_tr_b16 a[26:27], v83 offset:5120\n" \
  "ds_read_b64_tr_b16 a[28:29], v83 offset:6144\n" \
  "ds_read_b64_tr_b16 a[30:31], v83 offset:7168\n" \
  "s_branch HW1_%=\n" \
  "HV1_%=:\n" \
  "ds_read_b64_tr_b16 a[64:65], v82 offset:4096\n" \
  "ds_read_b64_tr_b16 a[66:67], v82 offset:5120\n" \
  "ds_read_b64_tr_b16 a[68:69], v82 offset:6144\n" \
  "ds_read_b64_tr_b16 a[70:71], v82 offset:7168\n" \
  "ds_read_b64_tr_b16 a[72:73], v83 offset:4096\n" \
  "ds_read_b64_tr_b16 a[74:75], v83 offset:5120\n" \
  "ds_read_b64_tr_b16 a[76:77], v83 offset:6144\n" \
  "ds_read_b64_tr_b16 a[78:79], v83 offset:7168\n" \
  "HW1_%=:\n" \
  "s_waitcnt lgkmcnt(0)\n" \
  "s_add_u32 m0, s34, 4096\n" \
  "s_nop 0\n" \
  "buffer_load_dwordx4 v108, s[20:23], s33 offen lds\n" \
  "buffer_load_dwordx4 v109, s[20:23], s33 offen offset:1024 lds\n" \
  "buffer_load_dwordx4 v110, s[20:23], s33 offen offset:2048 lds\n" \
  "buffer_load_dwordx4 v111, s[20:23], s33 offen offset:3072 lds\n" \
  "s_waitcnt vmcnt(28)\n" \
  "ds_read_b64_tr_b16 a[32:33], v82 offset:8192\n" \
  "ds_read_b64_tr_b16 a[34:35], v82 offset:9216\n" \
  "ds_read_b64_tr_b16 a[36:37], v82 offset:10240\n" \
  "ds_read_b64_tr_b16 a[38:39], v82 offset:11264\n" \
  "ds_read_b64_tr_b16 a[40:41], v83 offset:8192\n" \
  "ds_read_b64_tr_b16 a[42:43], v83 offset:9216\n" \
  "ds_read_b64_tr_b16 a[44:45], v83 offset:10240\n" \
  "ds_read_b64_tr_b16 a[46:47], v83 offset:11264\n" \
  "s_waitcnt lgkmcnt(0)\n" \
  "s_add_u32 m0, s34, 8192\n" \
  "s_nop 0\n" \
  "buffer_load_dwordx4 v104, s[12:15], s29 offen lds\n" \
  "buffer_load_dwordx4 v105, s[12:15], s29 offen offset:1024 lds\n" \
  "buffer_load_dwordx4 v106, s[12:15], s29 offen offset:2048 lds\n" \
  "buffer_load_dwordx4 v107, s[12:15], s29 offen offset:3072 lds\n" \
  "s_waitcnt vmcnt(28)\n" \
  "s_cbranch_vccnz HV3_%=\n" \
  "ds_read_b64_tr_b16 a[48:49], v82 offset:12288\n" \
  "ds_read_b64_tr_b16 a[50:51], v82 offset:13312\n" \
  "ds_read_b64_tr_b16 a[52:53], v82 offset:14336\n" \
  "ds_read_b64_tr_b16 a[54:55], v82 offset:15360\n" \
  "ds_read_b64_tr_b16 a[56:57], v83 offset:12288\n" \
  "ds_read_b64_tr_b16 a[58:59], v83 offset:13312\n" \
  "ds_read_b64_tr_b16 a[60:61], v83 offset:14336\n" \
  "ds_read_b64_tr_b16 a[62:63], v83 offset:15360\n" \
  "s_branch HW3_%=\n" \
  "HV3_%=:\n" \
  "ds_read_b64_tr_b16 a[0:1], v82 offset:12288\n" \
  "ds_read_b64_tr_b16 a[2:3], v82 offset:13312\n" \
  "ds_read_b64_tr_b16 a[4:5], v82 offset:14336\n" \
  "ds_read_b64_tr_b16 a[6:7], v82 offset:15360\n" \
  "ds_read_b64_tr_b16 a[8:9], v83 offset:12288\n" \
  "ds_read_b64_tr_b16 a[10:11], v83 offset:13312\n" \
  "ds_read_b64_tr_b16 a[12:13], v83 offset:14336\n" \
  "ds_read_b64_tr_b16 a[14:15], v83 offset:15360\n" \
  "HW3_%=:\n" \
  "s_waitcnt lgkmcnt(0)\n" \
  "s_add_u32 m0, s34, 12288\n" \
  "s_nop 0\n" \
  "buffer_load_dwordx4 v104, s[12:15], s30 offen lds\n" \
  "buffer_load_dwordx4 v105, s[12:15], s30 offen offset:1024 lds\n" \
  "buffer_load_dwordx4 v106, s[12:15], s30 offen offset:2048 lds\n" \
  "buffer_load_dwordx4 v107, s[12:15], s30 offen offset:3072 lds\n" \
  "s_waitcnt vmcnt(28)\n" \
  "s_cbranch_vccnz HV4_%=\n" \
  "ds_read_b64_tr_b16 a[64:65], v82 offset:16384\n" \
  "ds_read_b64_tr_b16 a[66:67], v82 offset:17408\n" \
  "ds_read_b64_tr_b16 a[68:69], v82 offset:18432\n" \
  "ds_read_b64_tr_b16 a[70:71], v82 offset:19456\n" \
  "ds_read_b64_tr_b16 a[72:73], v83 offset:16384\n" \
  "ds_read_b64_tr_b16 a[74:75], v83 offset:17408\n" \
  "ds_read_b64_tr_b16 a[76:77], v83 offset:18432\n" \
  "ds_read_b64_tr_b16 a[78:79], v83 offset:19456\n" \
  "s_branch HW4_%=\n" \
  "HV4_%=:\n" \
  "ds_read_b64_tr_b16 a[16:17], v82 offset:16384\n" \
  "ds_read_b64_tr_b16 a[18:19], v82 offset:17408\n" \
  "ds_read_b64_tr_b16 a[20:21], v82 offset:18432\n" \
  "ds_read_b64_tr_b16 a[22:23], v82 offset:19456\n" \
  "ds_read_b64_tr_b16 a[24:25], v83 offset:16384\n" \
  "ds_read_b64_tr_b16 a[26:27], v83 offset:17408\n" \
  "ds_read_b64_tr_b16 a[28:29], v83 offset:18432\n" \
  "ds_read_b64_tr_b16 a[30:31], v83 offset:19456\n" \
  "HW4_%=:\n" \
  "s_waitcnt lgkmcnt(0)\n" \
  "s_add_u32 m0, s34, 16384\n" \
  "s_nop 0\n" \
  "buffer_load_dwordx4 v104, s[12:15], s31 offen lds\n" \
  "buffer_load_dwordx4 v105, s[12:15], s31 offen offset:1024 lds\n" \
  "buffer_load_dwordx4 v106, s[12:15], s31 offen offset:2048 lds\n" \
  "buffer_load_dwordx4 v107, s[12:15], s31 offen offset:3072 lds\n" \
  "s_waitcnt vmcnt(28)\n" \
  "s_cbranch_vccnz HV5_%=\n" \
  "ds_read_b64_tr_b16 a[80:81], v82 offset:20480\n" \
  "ds_read_b64_tr_b16 a[82:83], v82 offset:21504\n" \
  "ds_read_b64_tr_b16 a[84:85], v82 offset:22528\n" \
  "ds_read_b64_tr_b16 a[86:87], v82 offset:23552\n" \
  "ds_read_b64_tr_b16 a[88:89], v83 offset:20480\n" \
  "ds_read_b64_tr_b16 a[90:91], v83 offset:21504\n" \
  "ds_read_b64_tr_b16 a[92:93], v83 offset:22528\n" \
  "ds_read_b64_tr_b16 a[94:95], v83 offset:23552\n" \
  "s_branch HW5_%=\n" \
  "HV5_%=:\n" \
  "ds_read_b64_tr_b16 a[128:129], v82 offset:20480\n" \
  "ds_read_b64_tr_b16 a[130:131], v82 offset:21504\n" \
  "ds_read_b64_tr_b16 a[132:133], v82 offset:22528\n" \
  "ds_read_b64_tr_b16 a[134:135], v82 offset:23552\n" \
  "ds_read_b64_tr_b16 a[136:137], v83 offset:20480\n" \
  "ds_read_b64_tr_b16 a[138:139], v83 offset:21504\n" \
  "ds_read_b64_tr_b16 a[140:141], v83 offset:22528\n" \
  "ds_read_b64_tr_b16 a[142:143], v83 offset:23552\n" \
  "HW5_%=:\n" \
  "s_waitcnt lgkmcnt(0)\n" \
  "s_add_u32 m0, s34, 20480\n" \
  "s_nop 0\n" \
  "buffer_load_dwordx4 v104, s[12:15], s32 offen lds\n" \
  "buffer_load_dwordx4 v105, s[12:15], s32 offen offset:1024 lds\n" \
  "buffer_load_dwordx4 v106, s[12:15], s32 offen offset:2048 lds\n" \
  "buffer_load_dwordx4 v107, s[12:15], s32 offen offset:3072 lds\n" \
  "s_waitcnt vmcnt(28)\n" \
  "s_cbranch_vccnz HV6_%=\n" \
  "ds_read_b64_tr_b16 a[96:97], v82 offset:24576\n" \
  "ds_read_b64_tr_b16 a[98:99], v82 offset:25600\n" \
  "ds_read_b64_tr_b16 a[100:101], v82 offset:26624\n" \
  "ds_read_b64_tr_b16 a[102:103], v82 offset:27648\n" \
  "ds_read_b64_tr_b16 a[104:105], v83 offset:24576\n" \
  "ds_read_b64_tr_b16 a[106:107], v83 offset:25600\n" \
  "ds_read_b64_tr_b16 a[108:109], v83 offset:26624\n" \
  "ds_read_b64_tr_b16 a[110:111], v83 offset:27648\n" \
  "s_branch HW6_%=\n" \
  "HV6_%=:\n" \
  "ds_read_b64_tr_b16 a[144:145], v82 offset:24576\n" \
  "ds_read_b64_tr_b16 a[146:147], v82 offset:25600\n" \
  "ds_read_b64_tr_b16 a[148:149], v82 offset:26624\n" \
  "ds_read_b64_tr_b16 a[150:151], v82 offset:27648\n" \
  "ds_read_b64_tr_b16 a[152:153], v83 offset:24576\n" \
  "ds_read_b64_tr_b16 a[154:155], v83 offset:25600\n" \
  "ds_read_b64_tr_b16 a[156:157], v83 offset:26624\n" \
  "ds_read_b64_tr_b16 a[158:159], v83 offset:27648\n" \
  "HW6_%=:\n" \
  "s_waitcnt lgkmcnt(0)\n" \
  "s_add_u32 m0, s34, 24576\n" \
  "s_nop 0\n" \
  "buffer_load_dwordx4 v104, s[12:15], s33 offen lds\n" \
  "buffer_load_dwordx4 v105, s[12:15], s33 offen offset:1024 lds\n" \
  "buffer_load_dwordx4 v106, s[12:15], s33 offen offset:2048 lds\n" \
  "buffer_load_dwordx4 v107, s[12:15], s33 offen offset:3072 lds\n" \
  "s_waitcnt vmcnt(28)\n" \
  "ds_read_b64_tr_b16 a[112:113], v82 offset:28672\n" \
  "ds_read_b64_tr_b16 a[114:115], v82 offset:29696\n" \
  "ds_read_b64_tr_b16 a[116:117], v82 offset:30720\n" \
  "ds_read_b64_tr_b16 a[118:119], v82 offset:31744\n" \
  "ds_read_b64_tr_b16 a[120:121], v83 offset:28672\n" \
  "ds_read_b64_tr_b16 a[122:123], v83 offset:29696\n" \
  "ds_read_b64_tr_b16 a[124:125], v83 offset:30720\n" \
  "ds_read_b64_tr_b16 a[126:127], v83 offset:31744\n" \
  "s_waitcnt lgkmcnt(0)\n" \
  "s_add_u32 m0, s34, 28672\n" \
  "s_nop 0\n" \
  "buffer_load_dwordx4 v104, s[16:19], s26 offen lds\n" \
  "buffer_load_dwordx4 v105, s[16:19], s26 offen offset:1024 lds\n" \
  "buffer_load_dwordx4 v106, s[16:19], s26 offen offset:2048 lds\n" \
  "buffer_load_dwordx4 v107, s[16:19], s26 offen offset:3072 lds\n" \
  "s_waitcnt vmcnt(28)\n" \
  "s_cbranch_vccnz HV8_%=\n" \
  "ds_read_b64_tr_b16 a[128:129], v82 offset:0\n" \
  "ds_read_b64_tr_b16 a[130:131], v82 offset:1024\n" \
  "ds_read_b64_tr_b16 a[132:133], v82 offset:2048\n" \
  "ds_read_b64_tr_b16 a[134:135], v82 offset:3072\n" \
  "ds_read_b64_tr_b16 a[136:137], v83 offset:0\n" \
  "ds_read_b64_tr_b16 a[138:139], v83 offset:1024\n" \
  "ds_read_b64_tr_b16 a[140:141], v83 offset:2048\n" \
  "ds_read_b64_tr_b16 a[142:143], v83 offset:3072\n" \
  "s_branch HW8_%=\n" \
  "HV8_%=:\n" \
  "ds_read_b64_tr_b16 a[80:81], v82 offset:0\n" \
  "ds_read_b64_tr_b16 a[82:83], v82 offset:1024\n" \
  "ds_read_b64_tr_b16 a[84:85], v82 offset:2048\n" \
  "ds_read_b64_tr_b16 a[86:87], v82 offset:3072\n" \
  "ds_read_b64_tr_b16 a[88:89], v83 offset:0\n" \
  "ds_read_b64_tr_b16 a[90:91], v83 offset:1024\n" \
  "ds_read_b64_tr_b16 a[92:93], v83 offset:2048\n" \
  "ds_read_b64_tr_b16 a[94:95], v83 offset:3072\n" \
  "HW8_%=:\n" \
  "s_waitcnt lgkmcnt(0)\n" \
  "s_add_u32 m0, s34, 0\n" \
  "s_nop 0\n" \
  "buffer_load_dwordx4 v104, s[16:19], s30 offen lds\n" \
  "buffer_load_dwordx4 v105, s[16:19], s30 offen offset:1024 lds\n" \
  "buffer_load_dwordx4 v106, s[16:19], s30 offen offset:2048 lds\n" \
  "buffer_load_dwordx4 v107, s[16:19], s30 offen offset:3072 lds\n" \
  "s_waitcnt vmcnt(28)\n" \
  "s_cbranch_vccnz HV9_%=\n" \
  "ds_read_b64_tr_b16 a[144:145], v82 offset:4096\n" \
  "ds_read_b64_tr_b16 a[146:147], v82 offset:5120\n" \
  "ds_read_b64_tr_b16 a[148:149], v82 offset:6144\n" \
  "ds_read_b64_tr_b16 a[150:151], v82 offset:7168\n" \
  "ds_read_b64_tr_b16 a[152:153], v83 offset:4096\n" \
  "ds_read_b64_tr_b16 a[154:155], v83 offset:5120\n" \
  "ds_read_b64_tr_b16 a[156:157], v83 offset:6144\n" \
  "ds_read_b64_tr_b16 a[158:159], v83 offset:7168\n" \
  "s_branch HW9_%=\n" \
  "HV9_%=:\n" \
  "ds_read_b64_tr_b16 a[96:97], v82 offset:4096\n" \
  "ds_read_b64_tr_b16 a[98:99], v82 offset:5120\n" \
  "ds_read_b64_tr_b16 a[100:101], v82 offset:6144\n" \
  "ds_read_b64_tr_b16 a[102:103], v82 offset:7168\n" \
  "ds_read_b64_tr_b16 a[104:105], v83 offset:4096\n" \
  "ds_read_b64_tr_b16 a[106:107], v83 offset:5120\n" \
  "ds_read_b64_tr_b16 a[108:109], v83 offset:6144\n" \
  "ds_read_b64_tr_b16 a[110:111], v83 offset:7168\n" \
  "HW9_%=:\n" \
  "s_waitcnt lgkmcnt(0)\n" \
  "s_add_u32 m0, s34, 4096\n" \
  "s_nop 0\n" \
  "buffer_load_dwordx4 v104, s[16:19], s31 offen lds\n" \
  "buffer_load_dwordx4 v105, s[16:19], s31 offen offset:1024 lds\n" \
  "buffer_load_dwordx4 v106, s[16:19], s31 offen offset:2048 lds\n" \
  "buffer_load_dwordx4 v107, s[16:19], s31 offen offset:3072 lds\n" \
  "s_waitcnt vmcnt(28)\n" \
  "s_cbranch_vccnz HV10_%=\n" \
  "ds_read_b64_tr_b16 a[160:161], v80 offset:8192\n" \
  "ds_read_b64_tr_b16 a[162:163], v80 offset:9216\n" \
  "ds_read_b64_tr_b16 a[164:165], v80 offset:10240\n" \
  "ds_read_b64_tr_b16 a[166:167], v80 offset:11264\n" \
  "ds_read_b64_tr_b16 a[168:169], v81 offset:8192\n" \
  "ds_read_b64_tr_b16 a[170:171], v81 offset:9216\n" \
  "ds_read_b64_tr_b16 a[172:173], v81 offset:10240\n" \
  "ds_read_b64_tr_b16 a[174:175], v81 offset:11264\n" \
  "s_branch HW10_%=\n" \
  "HV10_%=:\n" \
  "ds_read_b64_tr_b16 a[208:209], v80 offset:8192\n" \
  "ds_read_b64_tr_b16 a[210:211], v80 offset:9216\n" \
  "ds_read_b64_tr_b16 a[212:213], v80 offset:10240\n" \
  "ds_read_b64_tr_b16 a[214:215], v80 offset:11264\n" \
  "ds_read_b64_tr_b16 a[216:217], v81 offset:8192\n" \
  "ds_read_b64_tr_b16 a[218:219], v81 offset:9216\n" \
  "ds_read_b64_tr_b16 a[220:221], v81 offset:10240\n" \
  "ds_read_b64_tr_b16 a[222:223], v81 offset:11264\n" \
  "HW10_%=:\n" \
  "s_waitcnt lgkmcnt(0)\n" \
  "s_add_u32 m0, s34, 8192\n" \
  "s_nop 0\n" \
  "buffer_load_dwordx4 v104, s[16:19], s33 offen lds\n" \
  "buffer_load_dwordx4 v105, s[16:19], s33 offen offset:1024 lds\n" \
  "buffer_load_dwordx4 v106, s[16:19], s33 offen offset:2048 lds\n" \
  "buffer_load_dwordx4 v107, s[16:19], s33 offen offset:3072 lds\n" \
  "s_waitcnt vmcnt(28)\n" \
  "s_cbranch_vccnz HV11_%=\n" \
  "ds_read_b64_tr_b16 a[176:177], v80 offset:12288\n" \
  "ds_read_b64_tr_b16 a[178:179], v80 offset:13312\n" \
  "ds_read_b64_tr_b16 a[180:181], v80 offset:14336\n" \
  "ds_read_b64_tr_b16 a[182:183], v80 offset:15360\n" \
  "ds_read_b64_tr_b16 a[184:185], v81 offset:12288\n" \
  "ds_read_b64_tr_b16 a[186:187], v81 offset:13312\n" \
  "ds_read_b64_tr_b16 a[188:189], v81 offset:14336\n" \
  "ds_read_b64_tr_b16 a[190:191], v81 offset:15360\n" \
  "s_branch HW11_%=\n" \
  "HV11_%=:\n" \
  "ds_read_b64_tr_b16 a[224:225], v80 offset:12288\n" \
  "ds_read_b64_tr_b16 a[226:227], v80 offset:13312\n" \
  "ds_read_b64_tr_b16 a[228:229], v80 offset:14336\n" \
  "ds_read_b64_tr_b16 a[230:231], v80 offset:15360\n" \
  "ds_read_b64_tr_b16 a[232:233], v81 offset:12288\n" \
  "ds_read_b64_tr_b16 a[234:235], v81 offset:13312\n" \
  "ds_read_b64_tr_b16 a[236:237], v81 offset:14336\n" \
  "ds_read_b64_tr_b16 a[238:239], v81 offset:15360\n" \
  "HW11_%=:\n" \
  "s_waitcnt lgkmcnt(0)\n" \
  "s_cmp_lg_u32 s84, 0\n" \
  "s_cselect_b32 s46, s29, s32\n" \
  "s_add_u32 m0, s34, 12288\n" \
  "s_nop 0\n" \
  "buffer_load_dwordx4 v104, s[16:19], s46 offen lds\n" \
  "buffer_load_dwordx4 v105, s[16:19], s46 offen offset:1024 lds\n" \
  "buffer_load_dwordx4 v106, s[16:19], s46 offen offset:2048 lds\n" \
  "buffer_load_dwordx4 v107, s[16:19], s46 offen offset:3072 lds\n" \
  "s_waitcnt vmcnt(28)\n" \
  "ds_read_b64_tr_b16 a[192:193], v80 offset:16384\n" \
  "ds_read_b64_tr_b16 a[194:195], v80 offset:17408\n" \
  "ds_read_b64_tr_b16 a[196:197], v80 offset:18432\n" \
  "ds_read_b64_tr_b16 a[198:199], v80 offset:19456\n" \
  "ds_read_b64_tr_b16 a[200:201], v81 offset:16384\n" \
  "ds_read_b64_tr_b16 a[202:203], v81 offset:17408\n" \
  "ds_read_b64_tr_b16 a[204:205], v81 offset:18432\n" \
  "ds_read_b64_tr_b16 a[206:207], v81 offset:19456\n" \
  "s_waitcnt lgkmcnt(0)\n" \
  "s_cmp_lg_u32 s84, 0\n" \
  "s_cselect_b32 s46, s25, s28\n" \
  "s_add_u32 m0, s34, 16384\n" \
  "s_nop 0\n" \
  "buffer_load_dwordx4 v104, s[12:15], s46 offen lds\n" \
  "buffer_load_dwordx4 v105, s[12:15], s46 offen offset:1024 lds\n" \
  "buffer_load_dwordx4 v106, s[12:15], s46 offen offset:2048 lds\n" \
  "buffer_load_dwordx4 v107, s[12:15], s46 offen offset:3072 lds\n" \
  "s_waitcnt vmcnt(28)\n" \
  "s_cbranch_vccnz HV13_%=\n" \
  "ds_read_b64_tr_b16 a[208:209], v80 offset:20480\n" \
  "ds_read_b64_tr_b16 a[210:211], v80 offset:21504\n" \
  "ds_read_b64_tr_b16 a[212:213], v80 offset:22528\n" \
  "ds_read_b64_tr_b16 a[214:215], v80 offset:23552\n" \
  "ds_read_b64_tr_b16 a[216:217], v81 offset:20480\n" \
  "ds_read_b64_tr_b16 a[218:219], v81 offset:21504\n" \
  "ds_read_b64_tr_b16 a[220:221], v81 offset:22528\n" \
  "ds_read_b64_tr_b16 a[222:223], v81 offset:23552\n" \
  "s_branch HW13_%=\n" \
  "HV13_%=:\n" \
  "ds_read_b64_tr_b16 a[160:161], v80 offset:20480\n" \
  "ds_read_b64_tr_b16 a[162:163], v80 offset:21504\n" \
  "ds_read_b64_tr_b16 a[164:165], v80 offset:22528\n" \
  "ds_read_b64_tr_b16 a[166:167], v80 offset:23552\n" \
  "ds_read_b64_tr_b16 a[168:169], v81 offset:20480\n" \
  "ds_read_b64_tr_b16 a[170:171], v81 offset:21504\n" \
  "ds_read_b64_tr_b16 a[172:173], v81 offset:22528\n" \
  "ds_read_b64_tr_b16 a[174:175], v81 offset:23552\n" \
  "HW13_%=:\n" \
  "s_waitcnt lgkmcnt(0)\n" \
  "s_cmp_lg_u32 s84, 0\n" \
  "s_cselect_b32 s46, s24, s27\n" \
  "s_add_u32 m0, s34, 20480\n" \
  "s_nop 0\n" \
  "buffer_load_dwordx4 v104, s[12:15], s46 offen lds\n" \
  "buffer_load_dwordx4 v105, s[12:15], s46 offen offset:1024 lds\n" \
  "buffer_load_dwordx4 v106, s[12:15], s46 offen offset:2048 lds\n" \
  "buffer_load_dwordx4 v107, s[12:15], s46 offen offset:3072 lds\n" \
  "s_waitcnt vmcnt(28)\n" \
  "s_cbranch_vccnz HV14_%=\n" \
  "ds_read_b64_tr_b16 a[224:225], v80 offset:24576\n" \
  "ds_read_b64_tr_b16 a[226:227], v80 offset:25600\n" \
  "ds_read_b64_tr_b16 a[228:229], v80 offset:26624\n" \
  "ds_read_b64_tr_b16 a[230:231], v80 offset:27648\n" \
  "ds_read_b64_tr_b16 a[232:233], v81 offset:24576\n" \
  "ds_read_b64_tr_b16 a[234:235], v81 offset:25600\n" \
  "ds_read_b64_tr_b16 a[236:237], v81 offset:26624\n" \
  "ds_read_b64_tr_b16 a[238:239], v81 offset:27648\n" \
  "s_branch HW14_%=\n" \
  "HV14_%=:\n" \
  "ds_read_b64_tr_b16 a[176:177], v80 offset:24576\n" \
  "ds_read_b64_tr_b16 a[178:179], v80 offset:25600\n" \
  "ds_read_b64_tr_b16 a[180:181], v80 offset:26624\n" \
  "ds_read_b64_tr_b16 a[182:183], v80 offset:27648\n" \
  "ds_read_b64_tr_b16 a[184:185], v81 offset:24576\n" \
  "ds_read_b64_tr_b16 a[186:187], v81 offset:25600\n" \
  "ds_read_b64_tr_b16 a[188:189], v81 offset:26624\n" \
  "ds_read_b64_tr_b16 a[190:191], v81 offset:27648\n" \
  "HW14_%=:\n" \
  "s_waitcnt lgkmcnt(0)\n" \
  "s_cmp_lg_u32 s84, 0\n" \
  "s_cselect_b32 s46, s27, s24\n" \
  "s_add_u32 m0, s34, 24576\n" \
  "s_nop 0\n" \
  "buffer_load_dwordx4 v104, s[12:15], s46 offen lds\n" \
  "buffer_load_dwordx4 v105, s[12:15], s46 offen offset:1024 lds\n" \
  "buffer_load_dwordx4 v106, s[12:15], s46 offen offset:2048 lds\n" \
  "buffer_load_dwordx4 v107, s[12:15], s46 offen offset:3072 lds\n" \
  "s_waitcnt vmcnt(28)\n" \
  "ds_read_b64_tr_b16 a[240:241], v80 offset:28672\n" \
  "ds_read_b64_tr_b16 a[242:243], v80 offset:29696\n" \
  "ds_read_b64_tr_b16 a[244:245], v80 offset:30720\n" \
  "ds_read_b64_tr_b16 a[246:247], v80 offset:31744\n" \
  "ds_read_b64_tr_b16 a[248:249], v81 offset:28672\n" \
  "ds_read_b64_tr_b16 a[250:251], v81 offset:29696\n" \
  "ds_read_b64_tr_b16 a[252:253], v81 offset:30720\n" \
  "ds_read_b64_tr_b16 a[254:255], v81 offset:31744\n" \
  "s_waitcnt lgkmcnt(0)\n" \
  "s_cmp_lg_u32 s84, 0\n" \
  "s_cselect_b32 s46, s28, s25\n" \
  "s_add_u32 m0, s34, 28672\n" \
  "s_nop 0\n" \
  "buffer_load_dwordx4 v104, s[12:15], s46 offen lds\n" \
  "buffer_load_dwordx4 v105, s[12:15], s46 offen offset:1024 lds\n" \
  "buffer_load_dwordx4 v106, s[12:15], s46 offen offset:2048 lds\n" \
  "buffer_load_dwordx4 v107, s[12:15], s46 offen offset:3072 lds\n" \
  "s_waitcnt vmcnt(28)\n" \
  "s_cbranch_vccnz HV16_%=\n" \
  "ds_read_b64_tr_b16 v[160:161], v80 offset:0\n" \
  "ds_read_b64_tr_b16 v[162:163], v80 offset:1024\n" \
  "ds_read_b64_tr_b16 v[164:165], v80 offset:2048\n" \
  "ds_read_b64_tr_b16 v[166:167], v80 offset:3072\n" \
  "ds_read_b64_tr_b16 v[168:169], v81 offset:0\n" \
  "ds_read_b64_tr_b16 v[170:171], v81 offset:1024\n" \
  "ds_read_b64_tr_b16 v[172:173], v81 offset:2048\n" \
  "ds_read_b64_tr_b16 v[174:175], v81 offset:3072\n" \
  "s_branch HW16_%=\n" \
  "HV16_%=:\n" \
  "ds_read_b64_tr_b16 v[192:193], v80 offset:0\n" \
  "ds_read_b64_tr_b16 v[194:195], v80 offset:1024\n" \
  "ds_read_b64_tr_b16 v[196:197], v80 offset:2048\n" \
  "ds_read_b64_tr_b16 v[198:199], v80 offset:3072\n" \
  "ds_read_b64_tr_b16 v[200:201], v81 offset:0\n" \
  "ds_read_b64_tr_b16 v[202:203], v81 offset:1024\n" \
  "ds_read_b64_tr_b16 v[204:205], v81 offset:2048\n" \
  "ds_read_b64_tr_b16 v[206:207], v81 offset:3072\n" \
  "HW16_%=:\n" \
  "s_waitcnt lgkmcnt(0)\n" \
  "s_cmp_lg_u32 s84, 0\n" \
  "s_cselect_b32 s46, s32, s29\n" \
  "s_add_u32 m0, s34, 0\n" \
  "s_nop 0\n" \
  "buffer_load_dwordx4 v104, s[16:19], s46 offen lds\n" \
  "buffer_load_dwordx4 v105, s[16:19], s46 offen offset:1024 lds\n" \
  "buffer_load_dwordx4 v106, s[16:19], s46 offen offset:2048 lds\n" \
  "buffer_load_dwordx4 v107, s[16:19], s46 offen offset:3072 lds\n" \
  "s_waitcnt vmcnt(28)\n" \
  "ds_read_b64_tr_b16 v[176:177], v80 offset:4096\n" \
  "ds_read_b64_tr_b16 v[178:179], v80 offset:5120\n" \
  "ds_read_b64_tr_b16 v[180:181], v80 offset:6144\n" \
  "ds_read_b64_tr_b16 v[182:183], v80 offset:7168\n" \
  "ds_read_b64_tr_b16 v[184:185], v81 offset:4096\n" \
  "ds_read_b64_tr_b16 v[186:187], v81 offset:5120\n" \
  "ds_read_b64_tr_b16 v[188:189], v81 offset:6144\n" \
  "ds_read_b64_tr_b16 v[190:191], v81 offset:7168\n" \
  "s_waitcnt lgkmcnt(0)\n" \
  "s_add_u32 m0, s34, 4096\n" \
  "s_nop 0\n" \
  "buffer_load_dwordx4 v104, s[12:15], s26 offen lds\n" \
  "buffer_load_dwordx4 v105, s[12:15], s26 offen offset:1024 lds\n" \
  "buffer_load_dwordx4 v106, s[12:15], s26 offen offset:2048 lds\n" \
  "buffer_load_dwordx4 v107, s[12:15], s26 offen offset:3072 lds\n" \
  "s_waitcnt vmcnt(28)\n" \
  "s_cbranch_vccnz HV18_%=\n" \
  "ds_read_b64_tr_b16 v[192:193], v80 offset:8192\n" \
  "ds_read_b64_tr_b16 v[194:195], v80 offset:9216\n" \
  "ds_read_b64_tr_b16 v[196:197], v80 offset:10240\n" \
  "ds_read_b64_tr_b16 v[198:199], v80 offset:11264\n" \
  "ds_read_b64_tr_b16 v[200:201], v81 offset:8192\n" \
  "ds_read_b64_tr_b16 v[202:203], v81 offset:9216\n" \
  "ds_read_b64_tr_b16 v[204:205], v81 offset:10240\n" \
  "ds_read_b64_tr_b16 v[206:207], v81 offset:11264\n" \
  "s_branch HW18_%=\n" \
  "HV18_%=:\n" \
  "ds_read_b64_tr_b16 v[160:161], v80 offset:8192\n" \
  "ds_read_b64_tr_b16 v[162:163], v80 offset:9216\n" \
  "ds_read_b64_tr_b16 v[164:165], v80 offset:10240\n" \
  "ds_read_b64_tr_b16 v[166:167], v80 offset:11264\n" \
  "ds_read_b64_tr_b16 v[168:169], v81 offset:8192\n" \
  "ds_read_b64_tr_b16 v[170:171], v81 offset:9216\n" \
  "ds_read_b64_tr_b16 v[172:173], v81 offset:10240\n" \
  "ds_read_b64_tr_b16 v[174:175], v81 offset:11264\n" \
  "HW18_%=:\n" \
  "s_waitcnt lgkmcnt(0)\n" \
  "s_cmp_lg_u32 s84, 0\n" \
  "s_cselect_b32 s46, s27, s24\n" \
  "s_add_u32 m0, s34, 8192\n" \
  "s_nop 0\n" \
  "buffer_load_dwordx4 v104, s[16:19], s46 offen lds\n" \
  "buffer_load_dwordx4 v105, s[16:19], s46 offen offset:1024 lds\n" \
  "buffer_load_dwordx4 v106, s[16:19], s46 offen offset:2048 lds\n" \
  "buffer_load_dwordx4 v107, s[16:19], s46 offen offset:3072 lds\n" \
  "s_waitcnt vmcnt(28)\n" \
  "ds_read_b64_tr_b16 v[208:209], v80 offset:12288\n" \
  "ds_read_b64_tr_b16 v[210:211], v80 offset:13312\n" \
  "ds_read_b64_tr_b16 v[212:213], v80 offset:14336\n" \
  "ds_read_b64_tr_b16 v[214:215], v80 offset:15360\n" \
  "ds_read_b64_tr_b16 v[216:217], v81 offset:12288\n" \
  "ds_read_b64_tr_b16 v[218:219], v81 offset:13312\n" \
  "ds_read_b64_tr_b16 v[220:221], v81 offset:14336\n" \
  "ds_read_b64_tr_b16 v[222:223], v81 offset:15360\n" \
  "s_waitcnt lgkmcnt(0)\n" \
  "s_cmp_lg_u32 s84, 0\n" \
  "s_cselect_b32 s46, s24, s27\n" \
  "s_add_u32 m0, s34, 12288\n" \
  "s_nop 0\n" \
  "buffer_load_dwordx4 v104, s[16:19], s46 offen lds\n" \
  "buffer_load_dwordx4 v105, s[16:19], s46 offen offset:1024 lds\n" \
  "buffer_load_dwordx4 v106, s[16:19], s46 offen offset:2048 lds\n" \
  "buffer_load_dwordx4 v107, s[16:19], s46 offen offset:3072 lds\n" \
  "s_waitcnt vmcnt(28)\n" \
  "ds_read_b64_tr_b16 v[224:225], v80 offset:16384\n" \
  "ds_read_b64_tr_b16 v[226:227], v80 offset:17408\n" \
  "ds_read_b64_tr_b16 v[228:229], v80 offset:18432\n" \
  "ds_read_b64_tr_b16 v[230:231], v80 offset:19456\n" \
  "ds_read_b64_tr_b16 v[232:233], v81 offset:16384\n" \
  "ds_read_b64_tr_b16 v[234:235], v81 offset:17408\n" \
  "ds_read_b64_tr_b16 v[236:237], v81 offset:18432\n" \
  "ds_read_b64_tr_b16 v[238:239], v81 offset:19456\n" \
  "s_waitcnt lgkmcnt(0)\n" \
  "s_cmp_lg_u32 s84, 0\n" \
  "s_cselect_b32 s46, s28, s25\n" \
  "s_add_u32 m0, s34, 16384\n" \
  "s_nop 0\n" \
  "buffer_load_dwordx4 v104, s[16:19], s46 offen lds\n" \
  "buffer_load_dwordx4 v105, s[16:19], s46 offen offset:1024 lds\n" \
  "buffer_load_dwordx4 v106, s[16:19], s46 offen offset:2048 lds\n" \
  "buffer_load_dwordx4 v107, s[16:19], s46 offen offset:3072 lds\n" \
  "s_waitcnt vmcnt(28)\n" \
  "ds_read_b64_tr_b16 v[240:241], v80 offset:20480\n" \
  "ds_read_b64_tr_b16 v[242:243], v80 offset:21504\n" \
  "ds_read_b64_tr_b16 v[244:245], v80 offset:22528\n" \
  "ds_read_b64_tr_b16 v[246:247], v80 offset:23552\n" \
  "ds_read_b64_tr_b16 v[248:249], v81 offset:20480\n" \
  "ds_read_b64_tr_b16 v[250:251], v81 offset:21504\n" \
  "ds_read_b64_tr_b16 v[252:253], v81 offset:22528\n" \
  "ds_read_b64_tr_b16 v[254:255], v81 offset:23552\n" \
  "s_waitcnt lgkmcnt(0)\n" \
  "s_cmp_lg_u32 s84, 0\n" \
  "s_cselect_b32 s46, s25, s28\n" \
  "s_add_u32 m0, s34, 20480\n" \
  "s_nop 0\n" \
  "buffer_load_dwordx4 v104, s[16:19], s46 offen lds\n" \
  "buffer_load_dwordx4 v105, s[16:19], s46 offen offset:1024 lds\n" \
  "buffer_load_dwordx4 v106, s[16:19], s46 offen offset:2048 lds\n" \
  "buffer_load_dwordx4 v107, s[16:19], s46 offen offset:3072 lds\n" \
  "global_load_dwordx4 v[0:3], v116, s[50:51]\n" \
  "global_load_dwordx4 v[4:7], v116, s[50:51] offset:128\n" \
  "global_load_dwordx4 v[8:11], v117, s[50:51]\n" \
  "buffer_load_dwordx2 v[72:73], off, s[8:11], s82 offset:4 sc1\n" \
  "s_waitcnt vmcnt(32)\n" \
  "ds_read_b64_tr_b16 v[12:13], v80 offset:24576\n" \
  "ds_read_b64_tr_b16 v[14:15], v80 offset:25600\n" \
  "ds_read_b64_tr_b16 v[16:17], v80 offset:26624\n" \
  "ds_read_b64_tr_b16 v[18:19], v80 offset:27648\n" \
  "ds_read_b64_tr_b16 v[20:21], v81 offset:24576\n" \
  "ds_read_b64_tr_b16 v[22:23], v81 offset:25600\n" \
  "ds_read_b64_tr_b16 v[24:25], v81 offset:26624\n" \
  "ds_read_b64_tr_b16 v[26:27], v81 offset:27648\n" \
  "s_waitcnt lgkmcnt(0)\n" \
  "ds_write_b128 v85, v[12:15] offset:24576\n" \
  "ds_write_b128 v85, v[16:19] offset:25600\n" \
  "ds_write_b128 v85, v[20:23] offset:26624\n" \
  "ds_write_b128 v85, v[24:27] offset:27648\n" \
  "s_waitcnt vmcnt(28)\n" \
  "ds_read_b64_tr_b16 v[104:105], v80 offset:28672\n" \
  "ds_read_b64_tr_b16 v[106:107], v80 offset:29696\n" \
  "ds_read_b64_tr_b16 v[108:109], v80 offset:30720\n" \
  "ds_read_b64_tr_b16 v[110:111], v80 offset:31744\n" \
  "ds_read_b64_tr_b16 v[112:113], v81 offset:28672\n" \
  "ds_read_b64_tr_b16 v[114:115], v81 offset:29696\n" \
  "ds_read_b64_tr_b16 v[116:117], v81 offset:30720\n" \
  "ds_read_b64_tr_b16 v[118:119], v81 offset:31744\n" \
  "s_waitcnt lgkmcnt(0)\n" \
  "ds_write_b128 v85, v[104:107] offset:28672\n" \
  "ds_write_b128 v85, v[108:111] offset:29696\n" \
  "ds_write_b128 v85, v[112:115] offset:30720\n" \
  "ds_write_b128 v85, v[116:119] offset:31744\n" \
  "s_waitcnt vmcnt(24)\n" \
  "ds_read_b64_tr_b16 v[44:45], v80 offset:0\n" \
  "ds_read_b64_tr_b16 v[46:47], v80 offset:1024\n" \
  "ds_read_b64_tr_b16 v[48:49], v80 offset:2048\n" \
  "ds_read_b64_tr_b16 v[50:51], v80 offset:3072\n" \
  "ds_read_b64_tr_b16 v[52:53], v81 offset:0\n" \
  "ds_read_b64_tr_b16 v[54:55], v81 offset:1024\n" \
  "ds_read_b64_tr_b16 v[56:57], v81 offset:2048\n" \
  "ds_read_b64_tr_b16 v[58:59], v81 offset:3072\n" \
  "s_waitcnt lgkmcnt(0)\n" \
  "ds_write_b128 v85, v[44:47] offset:0\n" \
  "ds_write_b128 v85, v[48:51] offset:1024\n" \
  "ds_write_b128 v85, v[52:55] offset:2048\n" \
  "ds_write_b128 v85, v[56:59] offset:3072\n" \
  "s_waitcnt vmcnt(20)\n" \
  "ds_read_b64_tr_b16 v[136:137], v80 offset:4096\n" \
  "ds_read_b64_tr_b16 v[138:139], v80 offset:5120\n" \
  "ds_read_b64_tr_b16 v[140:141], v80 offset:6144\n" \
  "ds_read_b64_tr_b16 v[142:143], v80 offset:7168\n" \
  "ds_read_b64_tr_b16 v[144:145], v81 offset:4096\n" \
  "ds_read_b64_tr_b16 v[146:147], v81 offset:5120\n" \
  "ds_read_b64_tr_b16 v[148:149], v81 offset:6144\n" \
  "ds_read_b64_tr_b16 v[150:151], v81 offset:7168\n" \
  "s_waitcnt lgkmcnt(0)\n" \
  "buffer_load_dword v74, off, s[8:11], s80 offset:4 sc1\n" \
  "buffer_load_dword v75, off, s[8:11], s81 offset:4 sc1\n" \
  "ds_write_b128 v85, v[136:139] offset:4096\n" \
  "ds_write_b128 v85, v[140:143] offset:5120\n" \
  "ds_write_b128 v85, v[144:147] offset:6144\n" \
  "ds_write_b128 v85, v[148:151] offset:7168\n" \
  "s_waitcnt vmcnt(18)\n" \
  "ds_read_b64_tr_b16 v[28:29], v80 offset:8192\n" \
  "ds_read_b64_tr_b16 v[30:31], v80 offset:9216\n" \
  "ds_read_b64_tr_b16 v[32:33], v80 offset:10240\n" \
  "ds_read_b64_tr_b16 v[34:35], v80 offset:11264\n" \
  "ds_read_b64_tr_b16 v[36:37], v81 offset:8192\n" \
  "ds_read_b64_tr_b16 v[38:39], v81 offset:9216\n" \
  "ds_read_b64_tr_b16 v[40:41], v81 offset:10240\n" \
  "ds_read_b64_tr_b16 v[42:43], v81 offset:11264\n" \
  "s_waitcnt lgkmcnt(0)\n" \
  "ds_write_b128 v85, v[28:31] offset:8192\n" \
  "ds_write_b128 v85, v[32:35] offset:9216\n" \
  "ds_write_b128 v85, v[36:39] offset:10240\n" \
  "ds_write_b128 v85, v[40:43] offset:11264\n" \
  "s_waitcnt vmcnt(14)\n" \
  "ds_read_b64_tr_b16 v[152:153], v80 offset:12288\n" \
  "ds_read_b64_tr_b16 v[154:155], v80 offset:13312\n" \
  "ds_read_b64_tr_b16 v[156:157], v80 offset:14336\n" \
  "ds_read_b64_tr_b16 v[158:159], v80 offset:15360\n" \
  "ds_read_b64_tr_b16 v[60:61], v81 offset:12288\n" \
  "ds_read_b64_tr_b16 v[62:63], v81 offset:13312\n" \
  "ds_read_b64_tr_b16 v[64:65], v81 offset:14336\n" \
  "ds_read_b64_tr_b16 v[66:67], v81 offset:15360\n" \
  "s_waitcnt lgkmcnt(0)\n" \
  "ds_write_b128 v85, v[152:155] offset:12288\n" \
  "ds_write_b128 v85, v[156:159] offset:13312\n" \
  "ds_write_b128 v85, v[60:63] offset:14336\n" \
  "ds_write_b128 v85, v[64:67] offset:15360\n" \
  "s_waitcnt vmcnt(10)\n" \
  "ds_read_b64_tr_b16 v[120:121], v80 offset:16384\n" \
  "ds_read_b64_tr_b16 v[122:123], v80 offset:17408\n" \
  "ds_read_b64_tr_b16 v[124:125], v80 offset:18432\n" \
  "ds_read_b64_tr_b16 v[126:127], v80 offset:19456\n" \
  "ds_read_b64_tr_b16 v[128:129], v81 offset:16384\n" \
  "ds_read_b64_tr_b16 v[130:131], v81 offset:17408\n" \
  "ds_read_b64_tr_b16 v[132:133], v81 offset:18432\n" \
  "ds_read_b64_tr_b16 v[134:135], v81 offset:19456\n" \
  "s_waitcnt lgkmcnt(0)\n" \
  "ds_write_b128 v85, v[120:123] offset:16384\n" \
  "ds_write_b128 v85, v[124:127] offset:17408\n" \
  "ds_write_b128 v85, v[128:131] offset:18432\n" \
  "ds_write_b128 v85, v[132:135] offset:19456\n" \
  "s_waitcnt vmcnt(6)\n" \
  "ds_read_b64_tr_b16 v[44:45], v80 offset:20480\n" \
  "ds_read_b64_tr_b16 v[46:47], v80 offset:21504\n" \
  "ds_read_b64_tr_b16 v[48:49], v80 offset:22528\n" \
  "ds_read_b64_tr_b16 v[50:51], v80 offset:23552\n" \
  "ds_read_b64_tr_b16 v[52:53], v81 offset:20480\n" \
  "ds_read_b64_tr_b16 v[54:55], v81 offset:21504\n" \
  "ds_read_b64_tr_b16 v[56:57], v81 offset:22528\n" \
  "ds_read_b64_tr_b16 v[58:59], v81 offset:23552\n" \
  "s_waitcnt lgkmcnt(0)\n" \
  "ds_write_b128 v85, v[44:47] offset:20480\n" \
  "ds_write_b128 v85, v[48:51] offset:21504\n" \
  "ds_write_b128 v85, v[52:55] offset:22528\n" \
  "ds_write_b128 v85, v[56:59] offset:23552\n" \
  "s_branch HAS_%=\n" \
  "HAR_%=:\n" \
  "buffer_load_dwordx2 v[72:73], off, s[8:11], s82 offset:4 sc1\n" \
  "HAS_%=:\n" \
  "s_waitcnt vmcnt(0)\n" \
  "v_readfirstlane_b32 s46, v72\n" \
  "v_readfirstlane_b32 s47, v73\n" \
  "s_nop 3\n" \
  "s_or_b32 s46, s46, 0x3fff\n" \
  "s_add_u32 s46, s46, 1\n" \
  "s_xor_b32 s46, s46, s47\n" \
  "s_andn2_b32 s47, s46, 0x2000\n" \
  "s_cbranch_scc0 HAT_%=\n" \
  "s_sleep 2\n" \
  "s_sub_u32 s44, s44, 1\n" \
  "s_cbranch_scc0 HAR_%=\n" \
  "s_mov_b32 s44, 0\n" \
  "HAT_%=:\n" \
  "s_and_b32 s46, s46, 0x2000\n" \
  "s_add_u32 s37, s37, s46\n" \
  "s_add_u32 s38, s38, s46\n" \
  "s_add_u32 s39, s39, s46\n" \
  "v_add_u32 v93, s46, v93\n" \
  "s_add_u32 s57, s57, s46\n" \
  "s_mov_b32 s55, s37\n" \
  "v_xor_b32 v74, v73, v74\n" \
  "v_xor_b32 v75, v73, v75\n" \
  "v_or_b32 v74, v74, v75\n" \
  "v_cmp_gt_u32 vcc, 0x4000, v74\n" \
  "s_cbranch_vccnz HUG_%=\n" \
  "HU_%=:\n" \
  "buffer_load_dword v76, off, s[8:11], s80 sc1\n" \
  "buffer_load_dword v77, off, s[8:11], s81 sc1\n" \
  "s_waitcnt vmcnt(0)\n" \
  "v_min_i32 v76, v76, v77\n" \
  "v_cmp_le_i32 vcc, s36, v76\n" \
  "s_cbranch_vccnz HUG_%=\n" \
  "s_sleep 2\n" \
  "s_sub_u32 s44, s44, 1\n" \
  "s_cbranch_scc0 HU_%=\n" \
  "s_mov_b32 s44, 0\n" \
  "HUG_%=:\n" \
  "s_mov_b32 s84, -1\n" \
  "s_waitcnt lgkmcnt(0)\n" \
  "buffer_load_dwordx4 v[68:71], v85, s[8:11], s40 offen sc1\n" \
  "buffer_load_dwordx2 v[72:73], v85, s[8:11], s40 offen offset:1032 sc1\n" \
  "buffer_load_dwordx2 v[74:75], v85, s[8:11], s40 offen offset:1024 sc1\n" \
  "buffer_load_dwordx4 v[76:79], v85, s[8:11], s45 offen sc1\n" \
  "buffer_load_dwordx4 v[80:83], v85, s[8:11], s45 offen offset:1024 sc1\n" \
  "v_mov_b32 v44, 0\n" \
  "v_mov_b32 v45, 0\n" \
  "v_mov_b32 v46, 0\n" \
  "v_mov_b32 v47, 0\n" \
  "v_mov_b32 v48, 0\n" \
  "v_mov_b32 v49, 0\n" \
  "v_mov_b32 v50, 0\n" \
  "v_mov_b32 v51, 0\n" \
  "v_mov_b32 v52, 0\n" \
  "v_mov_b32 v53, 0\n" \
  "v_mov_b32 v54, 0\n" \
  "v_mov_b32 v55, 0\n" \
  "v_mov_b32 v56, 0\n" \
  "v_mov_b32 v57, 0\n" \
  "v_mov_b32 v58, 0\n" \
  "v_mov_b32 v59, 0\n" \
  "v_mov_b32 v60, 0\n" \
  "v_mov_b32 v61, 0\n" \
  "v_mov_b32 v62, 0\n" \
  "v_mov_b32 v63, 0\n" \
  "v_mov_b32 v64, 0\n" \
  "v_mov_b32 v65, 0\n" \
  "v_mov_b32 v66, 0\n" \
  "v_mov_b32 v67, 0\n" \
  "s_getpc_b64 s[74:75]\n" \
  "HIP_%=:\n" \
  "s_add_u32 s74, s74, HIE_%=-HIP_%=\n" \
  "s_addc_u32 s75, s75, 0\n" \
  "s_memtime  s[72:73]\n" \
  "s_memrealtime s[70:71]\n" \
  "s_nop 2\n" \
  "s_memrealtime s[66:67]\n" \
  "s_nop 2\n" \
  "s_memrealtime s[60:61]\n" \
  "s_nop 2\n" \
  "s_memrealtime s[62:63]\n" \
  "s_nop 2\n" \
  "s_memrealtime s[64:65]\n" \
  "s_nop 2\n" \
  "s_memrealtime s[50:51]\n" \
  "s_waitcnt lgkmcnt(0)\n" \
  "s_add_u32 s66, s66, s60\n" \
  "s_add_u32 s62, s62, s64\n" \
  "s_add_u32 s66, s66, s50\n" \
  "s_mul_i32 s60, s70, 5\n" \
  "s_add_u32 s66, s66, s62\n" \
  "s_sub_u32 s66, s66, s60\n" \
  "s_mul_i32 s66, s66, 245\n" \
  "s_lshr_b32 s66, s66, 6\n" \
  "s_mul_i32 s68, s70, 23\n" \
  "s_add_u32 s68, s68, s66\n" \
  "s_sub_u32 s68, s68, s72\n" \
  "s_sub_u32 s59, s72, 0x10000\n" \
  "ds_write_b128 v85, v[0:3] offset:32768\n" \
  "v_smfmac_f32_16x16x64_bf16 v[44:47], v[0:3], v[12:19], v84\n" \
  "v_smfmac_f32_16x16x64_bf16 v[48:51], v[0:3], v[20:27], v84\n" \
  "v_smfmac_f32_16x16x64_bf16 v[52:55], v[0:3], v[28:35], v84\n" \
  "v_smfmac_f32_16x16x64_bf16 v[56:59], v[0:3], v[36:43], v84\n" \
  "ds_write_b128 v85, v[4:7] offset:33792\n" \
  "v_smfmac_f32_16x16x64_bf16 v[44:47], v[4:7], v[104:111], v84\n" \
  "v_smfmac_f32_16x16x64_bf16 v[48:51], v[4:7], v[112:119], v84\n" \
  "v_smfmac_f32_16x16x64_bf16 v[52:55], v[4:7], v[120:127], v84\n" \
  "v_smfmac_f32_16x16x64_bf16 v[56:59], v[4:7], v[128:135], v84\n" \
  "ds_write_b128 v85, v[8:11] offset:34816\n" \
  "v_smfmac_f32_16x16x64_bf16 v[44:47], v[8:11], v[136:143], v84\n" \
  "v_smfmac_f32_16x16x64_bf16 v[48:51], v[8:11], v[144:151], v84\n" \
  "v_smfmac_f32_16x16x64_bf16 v[52:55], v[8:11], a[240:247], v84\n" \
  "v_smfmac_f32_16x16x64_bf16 v[56:59], v[8:11], a[248:255], v84\n" \
  "v_smfmac_f32_16x16x64_bf16 v[60:63], v[0:3], a[0:7], v84\n" \
  "s_branch HL2_%=\n" \
  "HL_%=:\n" \
  "s_waitcnt vmcnt(2)\n" \
  "v_cmp_lt_u32 vcc, s43, v0\n" \
  "ds_write_b128 v85, v[0:3] offset:32768\n" \
  "s_cbranch_vccnz HT_%=\n" \
  "v_smfmac_f32_16x16x64_bf16 v[44:47], v[0:3], v[12:19], v84\n" \
  "v_smfmac_f32_16x16x64_bf16 v[48:51], v[0:3], v[20:27], v84\n" \
  "v_smfmac_f32_16x16x64_bf16 v[52:55], v[0:3], v[28:35], v84\n" \
  "v_smfmac_f32_16x16x64_bf16 v[56:59], v[0:3], v[36:43], v84\n" \
  "s_waitcnt vmcnt(1)\n" \
  "ds_write_b128 v85, v[4:7] offset:33792\n" \
  "v_smfmac_f32_16x16x64_bf16 v[44:47], v[4:7], v[104:111], v84\n" \
  "v_smfmac_f32_16x16x64_bf16 v[48:51], v[4:7], v[112:119], v84\n" \
  "v_smfmac_f32_16x16x64_bf16 v[52:55], v[4:7], v[120:127], v84\n" \
  "v_smfmac_f32_16x16x64_bf16 v[56:59], v[4:7], v[128:135], v84\n" \
  "s_waitcnt vmcnt(0)\n" \
  "ds_write_b128 v85, v[8:11] offset:34816\n" \
  "v_smfmac_f32_16x16x64_bf16 v[44:47], v[8:11], v[136:143], v84\n" \
  "v_max3_u32 v40, v0, v4, v8\n" \
  "v_smfmac_f32_16x16x64_bf16 v[48:51], v[8:11], v[144:151], v84\n" \
  "v_cmp_lt_u32 vcc, s43, v40\n" \
  "v_smfmac_f32_16x16x64_bf16 v[52:55], v[8:11], a[240:247], v84\n" \
  "v_smfmac_f32_16x16x64_bf16 v[56:59], v[8:11], a[248:255], v84\n" \
  "v_smfmac_f32_16x16x64_bf16 v[60:63], v[0:3], a[0:7], v84\n" \
  "s_cbranch_vccnz HR_%=\n" \
  "HL2_%=:\n" \
  "s_waitcnt lgkmcnt(0)\n" \
  "s_barrier\n" \
  "s_memtime  s[66:67]\n" \
  "ds_read_b128 v[12:15], v86 offset:32768\n" \
  "v_smfmac_f32_16x16x64_bf16 v[64:67], v[0:3], a[8:15], v84\n" \
  "ds_read_b128 v[16:19], v86 offset:33792\n" \
  "v_smfmac_f32_16x16x64_bf16 v[60:63], v[4:7], a[16:23], v84\n" \
  "ds_read_b128 v[20:23], v87 offset:32768\n" \
  "v_smfmac_f32_16x16x64_bf16 v[64:67], v[4:7], a[24:31], v84\n" \
  "ds_read_b128 v[24:27], v87 offset:33792\n" \
  "v_smfmac_f32_16x16x64_bf16 v[60:63], v[8:11], a[32:39], v84\n" \
  "ds_read_b128 v[28:31], v87 offset:34816\n" \
  "s_waitcnt lgkmcnt(4)\n" \
  "v_smfmac_f32_16x16x64_bf16 v[44:47], v[12:15], v[240:247], v84\n" \
  "ds_read_b128 v[32:35], v88 offset:32768\n" \
  "s_add_u32 s69, s66, s68\n" \
  "s_and_b32 s69, s69, 0xffffff\n" \
  "v_smfmac_f32_16x16x64_bf16 v[48:51], v[12:15], v[248:255], v84\n" \
  "ds_read_b128 v[36:39], v88 offset:33792\n" \
  "s_or_b32 s69, s69, s56\n" \
  "v_mov_b32 v41, s69\n" \
  "s_waitcnt lgkmcnt(4)\n" \
  "v_smfmac_f32_16x16x64_bf16 v[44:47], v[16:19], v[224:231], v84\n" \
  "buffer_store_dword v41, v93, s[8:11], 0 offen sc1\n" \
  "ds_read_b128 v[104:107], v85 offset:14336\n" \
  "v_smfmac_f32_16x16x64_bf16 v[48:51], v[16:19], v[232:239], v84\n" \
  "ds_read_b128 v[108:111], v85 offset:15360\n" \
  "v_smfmac_f32_16x16x64_bf16 v[44:47], v[20:23], a[160:167], v84\n" \
  "ds_read_b128 v[112:115], v85 offset:20480\n" \
  "v_smfmac_f32_16x16x64_bf16 v[48:51], v[20:23], a[168:175], v84\n" \
  "s_waitcnt lgkmcnt(3)\n" \
  "v_smfmac_f32_16x16x64_bf16 v[44:47], v[24:27], a[176:183], v84\n" \
  "ds_read_b128 v[116:119], v85 offset:21504\n" \
  "v_smfmac_f32_16x16x64_bf16 v[48:51], v[24:27], a[184:191], v84\n" \
  "ds_read_b128 v[120:123], v85 offset:22528\n" \
  "v_smfmac_f32_16x16x64_bf16 v[44:47], v[28:31], a[192:199], v84\n" \
  "ds_read_b128 v[124:127], v85 offset:23552\n" \
  "v_smfmac_f32_16x16x64_bf16 v[44:47], v[32:35], a[208:215], v84\n" \
  "ds_read_b128 v[128:131], v85\n" \
  "v_smfmac_f32_16x16x64_bf16 v[44:47], v[36:39], a[224:231], v84\n" \
  "ds_read_b128 v[132:135], v85 offset:1024\n" \
  "v_smfmac_f32_16x16x64_bf16 v[48:51], v[28:31], a[200:207], v84\n" \
  "ds_read_b128 v[136:139], v85 offset:2048\n" \
  "v_smfmac_f32_16x16x64_bf16 v[48:51], v[32:35], a[216:223], v84\n" \
  "ds_read_b128 v[140:143], v85 offset:3072\n" \
  "v_cvt_pk_bf16_f32 v144, v44, v45\n" \
  "v_smfmac_f32_16x16x64_bf16 v[48:51], v[36:39], a[232:239], v84\n" \
  "v_cvt_pk_bf16_f32 v145, v46, v47\n" \
  "v_smfmac_f32_16x16x64_bf16 v[52:55], v[24:27], v[160:167], v84\n" \
  "v_smfmac_f32_16x16x64_bf16 v[56:59], v[24:27], v[168:175], v84\n" \
  "v_cvt_pk_bf16_f32 v146, v48, v49\n" \
  "v_smfmac_f32_16x16x64_bf16 v[52:55], v[28:31], v[176:183], v84\n" \
  "v_cvt_pk_bf16_f32 v147, v50, v51\n" \
  "v_smfmac_f32_16x16x64_bf16 v[56:59], v[28:31], v[184:191], v84\n" \
  "v_mfma_f32_16x16x32_bf16 v[148:151], v[144:147], v[96:99], 0\n" \
  "v_smfmac_f32_16x16x64_bf16 v[52:55], v[32:35], v[208:215], v84\n" \
  "v_smfmac_f32_16x16x64_bf16 v[56:59], v[32:35], v[216:223], v84\n" \
  "v_mul_f32 v148, 0x3e0293ee, v148\n" \
  "v_mul_f32 v149, 0x3e0293ee, v149\n" \
  "v_smfmac_f32_16x16x64_bf16 v[52:55], v[36:39], v[192:199], v84\n" \
  "v_mul_f32 v150, 0x3e0293ee, v150\n" \
  "v_mul_f32 v151, 0x3e0293ee, v151\n" \
  "v_smfmac_f32_16x16x64_bf16 v[56:59], v[36:39], v[200:207], v84\n" \
  "v_cvt_pk_bf16_f32 v40, v148, v149\n" \
  "v_cvt_pk_bf16_f32 v41, v150, v151\n" \
  "v_smfmac_f32_16x16x64_bf16 v[52:55], v[12:15], v[152:159], v84\n" \
  "ds_write_b64 v89, v[40:41]\n" \
  "s_waitcnt vmcnt(1)\n" \
  "v_mov_b32_dpp v42, v40 row_ror:8 row_mask:0xf bank_mask:0xf\n" \
  "v_mov_b32_dpp v43, v41 row_ror:8 row_mask:0xf bank_mask:0xf\n" \
  "s_waitcnt lgkmcnt(7)\n" \
  "v_smfmac_f32_16x16x64_bf16 v[52:55], v[16:19], v[112:119], v84\n" \
  "v_mfma_f32_16x16x32_bf16 v[144:147], v[68:71], v[40:43], 0\n" \
  "v_mfma_f32_16x16x32_bf16 v[148:151], v[72:75], v[40:43], 0\n" \
  "s_waitcnt lgkmcnt(1)\n" \
  "v_smfmac_f32_16x16x64_bf16 v[52:55], v[20:23], v[128:135], v84\n" \
  "v_smfmac_f32_16x16x64_bf16 v[56:59], v[12:15], v[104:111], v84\n" \
  "v_cndmask_b32 v144, v144, v148, s[52:53]\n" \
  "v_cndmask_b32 v145, v145, v149, s[52:53]\n" \
  "v_smfmac_f32_16x16x64_bf16 v[56:59], v[16:19], v[120:127], v84\n" \
  "v_cndmask_b32 v146, v146, v150, s[52:53]\n" \
  "v_cndmask_b32 v147, v147, v151, s[52:53]\n" \
  "v_smfmac_f32_16x16x64_bf16 v[56:59], v[20:23], v[136:143], v84\n" \
  "ds_write_b128 v85, v[144:147] offset:35840\n" \
  "v_cvt_pk_bf16_f32 v44, v52, v53\n" \
  "v_smfmac_f32_16x16x64_bf16 v[64:67], v[8:11], a[40:47], v84\n" \
  "v_cvt_pk_bf16_f32 v45, v54, v55\n" \
  "v_smfmac_f32_16x16x64_bf16 v[60:63], v[12:15], a[48:55], v84\n" \
  "v_cvt_pk_bf16_f32 v46, v56, v57\n" \
  "v_cvt_pk_bf16_f32 v47, v58, v59\n" \
  "v_smfmac_f32_16x16x64_bf16 v[64:67], v[12:15], a[56:63], v84\n" \
  "s_waitcnt lgkmcnt(0)\n" \
  "s_barrier\n" \
  "v_mfma_f32_16x16x32_bf16 v[48:51], v[44:47], v[96:99], 0\n" \
  "ds_read_b128 v[136:139], v86 offset:35840\n" \
  "v_smfmac_f32_16x16x64_bf16 v[60:63], v[16:19], a[64:71], v84\n" \
  "ds_read_b128 v[140:143], v87 offset:35840\n" \
  "v_smfmac_f32_16x16x64_bf16 v[64:67], v[16:19], a[72:79], v84\n" \
  "ds_read_b128 v[68:71], v88 offset:35840\n" \
  "v_cvt_pk_bf16_f32 v40, v48, v49\n" \
  "v_cvt_pk_bf16_f32 v41, v50, v51\n" \
  "v_smfmac_f32_16x16x64_bf16 v[60:63], v[20:23], a[80:87], v84\n" \
  "ds_write_b64 v89, v[40:41] offset:2048\n" \
  "v_smfmac_f32_16x16x64_bf16 v[64:67], v[20:23], a[88:95], v84\n" \
  "ds_read_b128 v[104:107], v90\n" \
  "ds_read_b128 v[108:111], v90 offset:512\n" \
  "s_waitcnt lgkmcnt(3)\n" \
  "v_add_f32 v144, v144, v136\n" \
  "v_add_f32 v145, v145, v137\n" \
  "v_smfmac_f32_16x16x64_bf16 v[60:63], v[24:27], a[96:103], v84\n" \
  "ds_read_b128 v[112:115], v90 offset:1024\n" \
  "ds_read_b128 v[116:119], v90 offset:1536\n" \
  "v_add_f32 v146, v146, v138\n" \
  "v_add_f32 v147, v147, v139\n" \
  "v_add_f32 v140, v140, v68\n" \
  "v_add_f32 v141, v141, v69\n" \
  "s_waitcnt lgkmcnt(4)\n" \
  "s_barrier\n" \
  "v_smfmac_f32_16x16x64_bf16 v[64:67], v[24:27], a[104:111], v84\n" \
  "ds_read_b128 v[120:123], v90 offset:2048\n" \
  "ds_read_b128 v[124:127], v90 offset:2560\n" \
  "v_add_f32 v142, v142, v70\n" \
  "v_add_f32 v143, v143, v71\n" \
  "v_smfmac_f32_16x16x64_bf16 v[60:63], v[28:31], a[112:119], v84\n" \
  "ds_read_b128 v[128:131], v90 offset:3072\n" \
  "ds_read_b128 v[132:135], v90 offset:3584\n" \
  "v_add_f32 v44, v144, v140\n" \
  "v_add_f32 v45, v145, v141\n" \
  "v_smfmac_f32_16x16x64_bf16 v[64:67], v[28:31], a[120:127], v84\n" \
  "v_add_f32 v46, v146, v142\n" \
  "v_add_f32 v47, v147, v143\n" \
  "s_waitcnt lgkmcnt(2)\n" \
  "v_mfma_f32_16x16x32_bf16 v[148:151], v[120:123], v[104:107], 0\n" \
  "v_exp_f32 v48, v44\n" \
  "v_mfma_f32_16x16x32_bf16 v[148:151], v[124:127], v[108:111], v[148:151]\n" \
  "s_waitcnt lgkmcnt(0)\n" \
  "v_exp_f32 v49, v45\n" \
  "v_mfma_f32_16x16x32_bf16 v[148:151], v[128:131], v[112:115], v[148:151]\n" \
  "v_exp_f32 v50, v46\n" \
  "v_mfma_f32_16x16x32_bf16 v[148:151], v[132:135], v[116:119], v[148:151]\n" \
  "v_exp_f32 v51, v47\n" \
  "v_smfmac_f32_16x16x64_bf16 v[60:63], v[32:35], a[128:135], v84\n" \
  "v_cvt_pk_bf16_f32 v72, v48, v49\n" \
  "v_smfmac_f32_16x16x64_bf16 v[64:67], v[32:35], a[136:143], v84\n" \
  "v_cvt_pk_bf16_f32 v73, v50, v51\n" \
  "v_smfmac_f32_16x16x64_bf16 v[60:63], v[36:39], a[144:151], v84\n" \
  "v_mov_b32_dpp v74, v72 row_ror:8 row_mask:0xf bank_mask:0xf\n" \
  "v_smfmac_f32_16x16x64_bf16 v[64:67], v[36:39], a[152:159], v84\n" \
  "v_mov_b32_dpp v75, v73 row_ror:8 row_mask:0xf bank_mask:0xf\n" \
  "v_exp_f32 v52, v148\n" \
  "v_mfma_f32_16x16x32_bf16 v[152:155], v[100:103], v[72:75], 0\n" \
  "v_exp_f32 v53, v149\n" \
  "v_mfma_f32_16x16x32_bf16 v[156:159], v[76:79], v[72:75], 0\n" \
  "v_exp_f32 v54, v150\n" \
  "v_mfma_f32_16x16x32_bf16 v[136:139], v[80:83], v[72:75], 0\n" \
  "v_exp_f32 v55, v151\n" \
  "v_cvt_pk_bf16_f32 v56, v52, v53\n" \
  "v_cvt_pk_bf16_f32 v0, v60, v61\n" \
  "v_cvt_pk_bf16_f32 v57, v54, v55\n" \
  "v_cvt_pk_bf16_f32 v1, v62, v63\n" \
  "HM_%=:\n" \
  "v_cvt_pk_bf16_f32 v2, v64, v65\n" \
  "v_mfma_f32_16x16x16_bf16 v[152:155], v[94:95], v[56:57], v[152:155]\n" \
  "v_cvt_pk_bf16_f32 v3, v66, v67\n" \
  "v_mfma_f32_16x16x16_bf16 v[156:159], v[0:1], v[56:57], v[156:159]\n" \
  "v_mfma_f32_16x16x16_bf16 v[136:139], v[2:3], v[56:57], v[136:139]\n" \
  "v_rcp_f32 v58, v152\n" \
  "v_subrev_u32 v59, 0x30800000, v152\n" \
  "v_cmp_lt_u32 vcc, s49, v59\n" \
  "s_nop 0\n" \
  "v_mul_f32 v156, v156, v58\n" \
  "v_mul_f32 v157, v157, v58\n" \
  "v_mul_f32 v158, v158, v58\n" \
  "v_mul_f32 v159, v159, v58\n" \
  "v_mul_f32_dpp v156, v136, v58 row_ror:8 row_mask:0xf bank_mask:0xc\n" \
  "v_mul_f32_dpp v157, v137, v58 row_ror:8 row_mask:0xf bank_mask:0xc\n" \
  "s_cmp_eq_u64 vcc, 0\n" \
  "v_mul_f32_dpp v158, v138, v58 row_ror:8 row_mask:0xf bank_mask:0xc\n" \
  "s_cselect_b32 s47, s55, s91\n" \
  "v_mul_f32_dpp v159, v139, v58 row_ror:8 row_mask:0xf bank_mask:0xc\n" \
  "v_cvt_pk_bf16_f32 v4, v156, v157\n" \
  "v_cvt_pk_bf16_f32 v5, v158, v159\n" \
  "buffer_store_dwordx2 v[4:5], v91, s[8:11], s47 offen sc1\n" \
  "s_cbranch_scc0 HF_%=\n" \
  "HS_%=:\n" \
  "s_mov_b32 s49, 0x19000000\n" \
  "s_add_u32 s40, s40, 0x40000\n" \
  "s_add_u32 s45, s45, 0x40000\n" \
  "HX_%=:\n" \
  "v_mov_b32 v128, -1\n" \
  "v_mov_b32 v129, -1\n" \
  "v_permlane32_swap_b32 v60, v64\n" \
  "v_permlane32_swap_b32 v61, v65\n" \
  "v_permlane32_swap_b32 v62, v66\n" \
  "v_permlane32_swap_b32 v63, v67\n" \
  "v_add_f32 v60, v60, v64\n" \
  "v_add_f32 v61, v61, v65\n" \
  "v_add_f32 v62, v62, v66\n" \
  "v_add_f32 v63, v63, v67\n" \
  "v_cvt_pk_bf16_f32 v124, v60, v61\n" \
  "v_cvt_pk_bf16_f32 v125, v62, v63\n" \
  "v_lshrrev_b32 v126, 1, v85\n" \
  "buffer_store_dwordx2 v[124:125], v126, s[8:11], s48 offen sc1\n" \
  "buffer_store_dwordx2 v[40:41], v89, s[8:11], s41 offen sc1\n" \
  "buffer_store_dwordx2 v[128:129], v91, s[8:11], s39 offen sc1\n" \
  "s_cmp_eq_u32 s36, 63\n" \
  "s_cbranch_scc1 HE_%=\n" \
  "s_add_u32 s78, s36, 3\n" \
  "s_cmp_lt_i32 s78, s84\n" \
  "s_cbranch_scc1 HN_%=\n" \
  "buffer_load_dword v5, off, s[8:11], s80 sc1\n" \
  "buffer_load_dword v6, off, s[8:11], s81 sc1\n" \
  "HN_%=:\n" \
  "v_mbcnt_lo_u32_b32 v8, -1, 0\n" \
  "v_lshrrev_b32 v9, 3, v8\n" \
  "v_and_b32 v8, 7, v8\n" \
  "v_lshlrev_b32 v8, 10, v8\n" \
  "v_lshl_add_u32 v8, v9, 14, v8\n" \
  "buffer_load_dword v0, v8, s[8:11], s57 offen sc1\n" \
  "s_nop 1\n" \
  "ds_read_b128 v[12:15], v85 offset:24576\n" \
  "ds_read_b128 v[16:19], v85 offset:25600\n" \
  "ds_read_b128 v[20:23], v85 offset:26624\n" \
  "ds_read_b128 v[24:27], v85 offset:27648\n" \
  "ds_read_b128 v[28:31], v85 offset:8192\n" \
  "ds_read_b128 v[32:35], v85 offset:9216\n" \
  "ds_read_b128 v[36:39], v85 offset:10240\n" \
  "ds_read_b128 v[40:43], v85 offset:11264\n" \
  "ds_read_b128 v[104:107], v85 offset:28672\n" \
  "ds_read_b128 v[108:111], v85 offset:29696\n" \
  "ds_read_b128 v[112:115], v85 offset:30720\n" \
  "ds_read_b128 v[116:119], v85 offset:31744\n" \
  "ds_read_b128 v[120:123], v85 offset:16384\n" \
  "ds_read_b128 v[124:127], v85 offset:17408\n" \
  "ds_read_b128 v[128:131], v85 offset:18432\n" \
  "ds_read_b128 v[132:135], v85 offset:19456\n" \
  "ds_read_b128 v[136:139], v85 offset:4096\n" \
  "ds_read_b128 v[140:143], v85 offset:5120\n" \
  "ds_read_b128 v[144:147], v85 offset:6144\n" \
  "ds_read_b128 v[148:151], v85 offset:7168\n" \
  "ds_read_b128 v[152:155], v85 offset:12288\n" \
  "ds_read_b128 v[156:159], v85 offset:13312\n" \
  "v_mov_b32 v44, 0\n" \
  "v_mov_b32 v45, 0\n" \
  "v_mov_b32 v46, 0\n" \
  "v_mov_b32 v47, 0\n" \
  "v_mov_b32 v48, 0\n" \
  "v_mov_b32 v49, 0\n" \
  "v_mov_b32 v50, 0\n" \
  "v_mov_b32 v51, 0\n" \
  "v_mov_b32 v52, 0\n" \
  "v_mov_b32 v53, 0\n" \
  "v_mov_b32 v54, 0\n" \
  "v_mov_b32 v55, 0\n" \
  "v_mov_b32 v56, 0\n" \
  "v_mov_b32 v57, 0\n" \
  "v_mov_b32 v58, 0\n" \
  "v_mov_b32 v59, 0\n" \
  "v_mov_b32 v60, 0\n" \
  "v_mov_b32 v61, 0\n" \
  "v_mov_b32 v62, 0\n" \
  "v_mov_b32 v63, 0\n" \
  "v_mov_b32 v64, 0\n" \
  "v_mov_b32 v65, 0\n" \
  "v_mov_b32 v66, 0\n" \
  "v_mov_b32 v67, 0\n" \
  "s_add_u32 s41, s41, 0x20000\n" \
  "s_add_u32 s48, s48, 0x20000\n" \
  "s_mov_b32 s46, s39\n" \
  "s_mov_b32 s39, s37\n" \
  "s_mov_b32 s37, s38\n" \
  "s_mov_b32 s38, s46\n" \
  "s_add_u32 s88, s37, s85\n" \
  "s_add_u32 s89, s37, s86\n" \
  "s_add_u32 s90, s37, s87\n" \
  "s_cmp_eq_u32 s36, 62\n" \
  "s_cselect_b32 s55, s91, s37\n" \
  "s_cmp_eq_u32 s36, 0\n" \
  "s_cselect_b32 s43, -2, s43\n" \
  "s_waitcnt lgkmcnt(0)\n" \
  "s_cmp_lt_i32 s36, s84\n" \
  "s_cbranch_scc1 HWG_%=\n" \
  "HW_%=:\n" \
  "buffer_load_dword v1, off, s[8:11], s80 sc1\n" \
  "buffer_load_dword v2, off, s[8:11], s81 sc1\n" \
  "s_waitcnt vmcnt(0)\n" \
  "v_min_i32 v1, v1, v2\n" \
  "v_cmp_lt_i32 vcc, s36, v1\n" \
  "s_cbranch_vccnz HWG_%=\n" \
  "s_sleep 2\n" \
  "s_sub_u32 s44, s44, 1\n" \
  "s_cbranch_scc0 HW_%=\n" \
  "s_mov_b32 s44, 0\n" \
  "HWG_%=:\n" \
  "buffer_load_dwordx4 v[68:71], v85, s[8:11], s40 offen sc1\n" \
  "buffer_load_dwordx2 v[72:73], v85, s[8:11], s40 offen offset:1032 sc1\n" \
  "buffer_load_dwordx2 v[74:75], v85, s[8:11], s40 offen offset:1024 sc1\n" \
  "buffer_load_dwordx4 v[76:79], v85, s[8:11], s45 offen sc1\n" \
  "buffer_load_dwordx4 v[80:83], v85, s[8:11], s45 offen offset:1024 sc1\n" \
  "v_mov_b32 v1, s69\n" \
  "s_mov_b32 exec_hi, 0\n" \
  "buffer_store_dword v1, v93, s[8:11], 0 offen sc1\n" \
  "s_mov_b32 exec_hi, -1\n" \
  "s_memtime  s[72:73]\n" \
  "s_memrealtime s[70:71]\n" \
  "s_nop 2\n" \
  "s_memrealtime s[66:67]\n" \
  "s_nop 2\n" \
  "s_memrealtime s[60:61]\n" \
  "s_nop 2\n" \
  "s_memrealtime s[62:63]\n" \
  "s_nop 2\n" \
  "s_memrealtime s[64:65]\n" \
  "s_nop 2\n" \
  "s_memrealtime s[50:51]\n" \
  "s_waitcnt lgkmcnt(0)\n" \
  "s_add_u32 s66, s66, s60\n" \
  "s_add_u32 s62, s62, s64\n" \
  "s_add_u32 s66, s66, s50\n" \
  "s_mul_i32 s60, s70, 5\n" \
  "s_add_u32 s66, s66, s62\n" \
  "s_sub_u32 s66, s66, s60\n" \
  "s_mul_i32 s66, s66, 245\n" \
  "s_lshr_b32 s66, s66, 6\n" \
  "s_mul_i32 s68, s70, 23\n" \
  "s_add_u32 s68, s68, s66\n" \
  "s_sub_u32 s68, s68, s72\n" \
  "s_cmp_lt_i32 s78, s84\n" \
  "s_cbranch_scc1 HO_%=\n" \
  "s_waitcnt vmcnt(7)\n" \
  "v_min_i32 v5, v5, v6\n" \
  "s_nop 0\n" \
  "v_readfirstlane_b32 s84, v5\n" \
  "HO_%=:\n" \
  "s_mov_b32 exec_hi, 0\n" \
  "s_mov_b32 s46, 0x2000\n" \
  "s_waitcnt vmcnt(6)\n" \
  "HC_%=:\n" \
  "v_cmp_gt_i32 vcc, s56, v0\n" \
  "s_cbranch_vccz HB_%=\n" \
  "buffer_load_dword v0, v8, s[8:11], s57 offen sc1\n" \
  "s_waitcnt vmcnt(0)\n" \
  "s_sub_u32 s46, s46, 1\n" \
  "s_cbranch_scc0 HC_%=\n" \
  "HB_%=:\n" \
  "s_add_u32 s56, s56, 0x1000000\n" \
  "s_cmp_lt_u32 s36, 60\n" \
  "s_cbranch_scc1 HK_%=\n" \
  "v_mov_b32 v1, s56\n" \
  "v_xor_b32 v2, 0x2000, v93\n" \
  "buffer_store_dword v1, v2, s[8:11], 0 offen sc1\n" \
  "HK_%=:\n" \
  "s_nop 0\n" \
  "v_cmp_le_i32 vcc, s56, v0\n" \
  "s_add_u32 s69, s72, s68\n" \
  "v_subrev_u32 v0, s69, v0\n" \
  "v_lshlrev_b32 v0, 8, v0\n" \
  "s_nop 1\n" \
  "v_max_i32_dpp v0, v0, v0 row_shr:1 row_mask:0xf bank_mask:0xf\n" \
  "s_nop 1\n" \
  "v_max_i32_dpp v0, v0, v0 row_shr:2 row_mask:0xf bank_mask:0xf\n" \
  "s_nop 1\n" \
  "v_max_i32_dpp v0, v0, v0 row_shr:4 row_mask:0xf bank_mask:0xf\n" \
  "s_nop 1\n" \
  "v_max_i32_dpp v0, v0, v0 row_shr:8 row_mask:0xf bank_mask:0xf\n" \
  "s_nop 1\n" \
  "v_readlane_b32 s46, v0, 15\n" \
  "v_readlane_b32 s47, v0, 31\n" \
  "s_nop 3\n" \
  "s_max_i32 s46, s46, s47\n" \
  "s_nop 3\n" \
  "s_ashr_i32 s46, s46, 8\n" \
  "s_min_i32 s46, s46, 0x2000\n" \
  "s_add_u32 s46, s46, s72\n" \
  "s_sub_u32 s47, s46, s59\n" \
  "s_sub_u32 s47, s47, 1300\n" \
  "s_cmp_lt_u32 s47, 1000\n" \
  "s_cselect_b32 s78, s94, 0\n" \
  "s_cmp_lt_i32 s47, 0\n" \
  "s_cselect_b32 s78, s95, s78\n" \
  "s_add_u32 s35, s35, s78\n" \
  "s_max_i32 s35, s35, s92\n" \
  "s_min_i32 s35, s35, s93\n" \
  "s_cmp_gt_i32 s47, 0\n" \
  "s_cselect_b32 s47, s96, 0\n" \
  "s_add_u32 s46, s46, s47\n" \
  "s_add_u32 s46, s46, s35\n" \
  "s_mov_b32 s59, s46\n" \
  "s_mov_b32 exec_hi, -1\n" \
  "s_cbranch_vccnz HJ_%=\n" \
  "HI_%=:\n" \
  "s_memtime  s[70:71]\n" \
  "v_mov_b32 v84, v84\n" \
  "v_mov_b32 v84, v84\n" \
  "s_waitcnt lgkmcnt(0)\n" \
  "s_sub_u32 s70, s46, s70\n" \
  "s_cmp_gt_i32 s70, 224\n" \
  "s_cbranch_scc1 HI_%=\n" \
  "s_sub_u32 s70, s70, 96\n" \
  "s_max_i32 s70, s70, 0\n" \
  "s_and_b32 s70, s70, -4\n" \
  "s_sub_u32 s70, s74, s70\n" \
  "s_subb_u32 s71, s75, 0\n" \
  "s_setpc_b64 s[70:71]\n" \
  "v_mov_b32 v84, v84\n" \
  "v_mov_b32 v84, v84\n" \
  "v_mov_b32 v84, v84\n" \
  "v_mov_b32 v84, v84\n" \
  "v_mov_b32 v84, v84\n" \
  "v_mov_b32 v84, v84\n" \
  "v_mov_b32 v84, v84\n" \
  "v_mov_b32 v84, v84\n" \
  "v_mov_b32 v84, v84\n" \
  "v_mov_b32 v84, v84\n" \
  "v_mov_b32 v84, v84\n" \
  "v_mov_b32 v84, v84\n" \
  "v_mov_b32 v84, v84\n" \
  "v_mov_b32 v84, v84\n" \
  "v_mov_b32 v84, v84\n" \
  "v_mov_b32 v84, v84\n" \
  "v_mov_b32 v84, v84\n" \
  "v_mov_b32 v84, v84\n" \
  "v_mov_b32 v84, v84\n" \
  "v_mov_b32 v84, v84\n" \
  "v_mov_b32 v84, v84\n" \
  "v_mov_b32 v84, v84\n" \
  "v_mov_b32 v84, v84\n" \
  "v_mov_b32 v84, v84\n" \
  "v_mov_b32 v84, v84\n" \
  "v_mov_b32 v84, v84\n" \
  "v_mov_b32 v84, v84\n" \
  "v_mov_b32 v84, v84\n" \
  "v_mov_b32 v84, v84\n" \
  "v_mov_b32 v84, v84\n" \
  "v_mov_b32 v84, v84\n" \
  "v_mov_b32 v84, v84\n" \
  "HIE_%=:\n" \
  "HJ_%=:\n" \
  "s_add_u32 s36, s36, 1\n" \
  "buffer_load_dwordx4 v[0:3], v92, s[8:11], s88 offen offset:0 sc1\n" \
  "v_mov_b32 v84, v84\n" \
  "s_nop 7\n" \
  "buffer_load_dwordx4 v[4:7], v92, s[8:11], s89 offen offset:1024 sc1\n" \
  "v_mov_b32 v84, v84\n" \
  "s_nop 7\n" \
  "buffer_load_dwordx4 v[8:11], v92, s[8:11], s90 offen offset:2048 sc1\n" \
  "HQ0_%=:\n" \
  "s_mov_b32 s46, s54\n" \
  "HQ_%=:\n" \
  "v_mov_b32 v84, v84\n" \
  "s_sub_u32 s46, s46, 1\n" \
  "v_mov_b32 v84, v84\n" \
  "s_cbranch_scc0 HQ_%=\n" \
  "s_cmp_lg_u32 s36, 63\n" \
  "s_cbranch_scc1 HL_%=\n" \
  "HZ_%=:\n" \
  "s_waitcnt vmcnt(2)\n" \
  "v_max3_u32 v44, v0, v1, v2\n" \
  "s_waitcnt vmcnt(1)\n" \
  "v_max3_u32 v44, v44, v3, v4\n" \
  "v_max3_u32 v44, v44, v5, v6\n" \
  "s_waitcnt vmcnt(0)\n" \
  "v_max3_u32 v44, v44, v7, v8\n" \
  "v_max3_u32 v44, v44, v9, v10\n" \
  "v_max_u32 v44, v44, v11\n" \
  "v_cmp_lt_u32 vcc, s43, v44\n" \
  "s_cbranch_vccnz HY_%=\n" \
  "ds_write_b128 v85, v[0:3] offset:32768\n" \
  "ds_write_b128 v85, v[4:7] offset:33792\n" \
  "ds_write_b128 v85, v[8:11] offset:34816\n" \
  "v_smfmac_f32_16x16x64_bf16 v[52:55], v[0:3], v[28:35], v84\n" \
  "v_smfmac_f32_16x16x64_bf16 v[56:59], v[0:3], v[36:43], v84\n" \
  "v_smfmac_f32_16x16x64_bf16 v[52:55], v[4:7], v[120:127], v84\n" \
  "v_smfmac_f32_16x16x64_bf16 v[56:59], v[4:7], v[128:135], v84\n" \
  "v_smfmac_f32_16x16x64_bf16 v[52:55], v[8:11], a[240:247], v84\n" \
  "v_smfmac_f32_16x16x64_bf16 v[56:59], v[8:11], a[248:255], v84\n" \
  "s_waitcnt lgkmcnt(0)\n" \
  "s_barrier\n" \
  "ds_read_b128 v[12:15], v86 offset:32768\n" \
  "ds_read_b128 v[16:19], v86 offset:33792\n" \
  "ds_read_b128 v[20:23], v87 offset:32768\n" \
  "ds_read_b128 v[24:27], v87 offset:33792\n" \
  "ds_read_b128 v[28:31], v87 offset:34816\n" \
  "ds_read_b128 v[32:35], v88 offset:32768\n" \
  "ds_read_b128 v[36:39], v88 offset:33792\n" \
  "v_smfmac_f32_16x16x64_bf16 v[60:63], v[0:3], a[0:7], v84\n" \
  "v_smfmac_f32_16x16x64_bf16 v[64:67], v[0:3], a[8:15], v84\n" \
  "v_smfmac_f32_16x16x64_bf16 v[60:63], v[4:7], a[16:23], v84\n" \
  "v_smfmac_f32_16x16x64_bf16 v[64:67], v[4:7], a[24:31], v84\n" \
  "v_smfmac_f32_16x16x64_bf16 v[60:63], v[8:11], a[32:39], v84\n" \
  "v_smfmac_f32_16x16x64_bf16 v[64:67], v[8:11], a[40:47], v84\n" \
  "ds_read_b128 v[104:107], v85 offset:14336\n" \
  "ds_read_b128 v[108:111], v85 offset:15360\n" \
  "ds_read_b128 v[112:115], v85 offset:20480\n" \
  "ds_read_b128 v[116:119], v85 offset:21504\n" \
  "ds_read_b128 v[120:123], v85 offset:22528\n" \
  "ds_read_b128 v[124:127], v85 offset:23552\n" \
  "ds_read_b128 v[128:131], v85\n" \
  "ds_read_b128 v[132:135], v85 offset:1024\n" \
  "ds_read_b128 v[136:139], v85 offset:2048\n" \
  "ds_read_b128 v[140:143], v85 offset:3072\n" \
  "s_waitcnt lgkmcnt(13)\n" \
  "v_smfmac_f32_16x16x64_bf16 v[52:55], v[24:27], v[160:167], v84\n" \
  "v_smfmac_f32_16x16x64_bf16 v[56:59], v[24:27], v[168:175], v84\n" \
  "s_waitcnt lgkmcnt(12)\n" \
  "v_smfmac_f32_16x16x64_bf16 v[52:55], v[28:31], v[176:183], v84\n" \
  "v_smfmac_f32_16x16x64_bf16 v[56:59], v[28:31], v[184:191], v84\n" \
  "s_waitcnt lgkmcnt(11)\n" \
  "v_smfmac_f32_16x16x64_bf16 v[52:55], v[32:35], v[208:215], v84\n" \
  "v_smfmac_f32_16x16x64_bf16 v[56:59], v[32:35], v[216:223], v84\n" \
  "s_waitcnt lgkmcnt(10)\n" \
  "v_smfmac_f32_16x16x64_bf16 v[52:55], v[36:39], v[192:199], v84\n" \
  "v_smfmac_f32_16x16x64_bf16 v[56:59], v[36:39], v[200:207], v84\n" \
  "v_smfmac_f32_16x16x64_bf16 v[60:63], v[12:15], a[48:55], v84\n" \
  "v_smfmac_f32_16x16x64_bf16 v[64:67], v[12:15], a[56:63], v84\n" \
  "v_smfmac_f32_16x16x64_bf16 v[60:63], v[16:19], a[64:71], v84\n" \
  "v_smfmac_f32_16x16x64_bf16 v[64:67], v[16:19], a[72:79], v84\n" \
  "v_smfmac_f32_16x16x64_bf16 v[60:63], v[20:23], a[80:87], v84\n" \
  "v_smfmac_f32_16x16x64_bf16 v[64:67], v[20:23], a[88:95], v84\n" \
  "v_smfmac_f32_16x16x64_bf16 v[52:55], v[12:15], v[152:159], v84\n" \
  "s_waitcnt lgkmcnt(8)\n" \
  "v_smfmac_f32_16x16x64_bf16 v[56:59], v[12:15], v[104:111], v84\n" \
  "s_waitcnt lgkmcnt(6)\n" \
  "v_smfmac_f32_16x16x64_bf16 v[52:55], v[16:19], v[112:119], v84\n" \
  "s_waitcnt lgkmcnt(4)\n" \
  "v_smfmac_f32_16x16x64_bf16 v[56:59], v[16:19], v[120:127], v84\n" \
  "s_waitcnt lgkmcnt(2)\n" \
  "v_smfmac_f32_16x16x64_bf16 v[52:55], v[20:23], v[128:135], v84\n" \
  "s_waitcnt lgkmcnt(0)\n" \
  "v_smfmac_f32_16x16x64_bf16 v[56:59], v[20:23], v[136:143], v84\n" \
  "v_smfmac_f32_16x16x64_bf16 v[60:63], v[24:27], a[96:103], v84\n" \
  "v_smfmac_f32_16x16x64_bf16 v[64:67], v[24:27], a[104:111], v84\n" \
  "v_smfmac_f32_16x16x64_bf16 v[60:63], v[28:31], a[112:119], v84\n" \
  "v_cvt_pk_bf16_f32 v44, v52, v53\n" \
  "v_cvt_pk_bf16_f32 v45, v54, v55\n" \
  "v_smfmac_f32_16x16x64_bf16 v[64:67], v[28:31], a[120:127], v84\n" \
  "v_cvt_pk_bf16_f32 v46, v56, v57\n" \
  "v_cvt_pk_bf16_f32 v47, v58, v59\n" \
  "v_smfmac_f32_16x16x64_bf16 v[60:63], v[32:35], a[128:135], v84\n" \
  "v_mfma_f32_16x16x32_bf16 v[48:51], v[44:47], v[96:99], 0\n" \
  "v_smfmac_f32_16x16x64_bf16 v[64:67], v[32:35], a[136:143], v84\n" \
  "v_smfmac_f32_16x16x64_bf16 v[60:63], v[36:39], a[144:151], v84\n" \
  "v_smfmac_f32_16x16x64_bf16 v[64:67], v[36:39], a[152:159], v84\n" \
  "v_cvt_pk_bf16_f32 v40, v48, v49\n" \
  "v_cvt_pk_bf16_f32 v41, v50, v51\n" \
  "s_nop 7\n" \
  "s_nop 3\n" \
  "s_branch HX_%=\n" \
  "HY_%=:\n" \
  "s_sub_u32 s44, s44, 1\n" \
  "s_cselect_b32 s43, -1, s43\n" \
  "s_max_i32 s44, s44, 0\n" \
  "buffer_load_dwordx4 v[0:3], v92, s[8:11], s88 offen offset:0 sc1\n" \
  "v_mov_b32 v84, v84\n" \
  "s_nop 7\n" \
  "buffer_load_dwordx4 v[4:7], v92, s[8:11], s89 offen offset:1024 sc1\n" \
  "v_mov_b32 v84, v84\n" \
  "s_nop 7\n" \
  "buffer_load_dwordx4 v[8:11], v92, s[8:11], s90 offen offset:2048 sc1\n" \
  "s_branch HZ_%=\n" \
  "HT_%=:\n" \
  "s_sub_u32 s44, s44, 1\n" \
  "s_cselect_b32 s43, -1, s43\n" \
  "s_max_i32 s44, s44, 0\n" \
  "s_waitcnt lgkmcnt(0)\n" \
  "buffer_load_dwordx4 v[0:3], v92, s[8:11], s88 offen offset:0 sc1\n" \
  "v_mov_b32 v84, v84\n" \
  "s_nop 7\n" \
  "buffer_load_dwordx4 v[4:7], v92, s[8:11], s89 offen offset:1024 sc1\n" \
  "v_mov_b32 v84, v84\n" \
  "s_nop 7\n" \
  "buffer_load_dwordx4 v[8:11], v92, s[8:11], s90 offen offset:2048 sc1\n" \
  "s_branch HQ0_%=\n" \
  "HR_%=:\n" \
  "s_sub_u32 s44, s44, 1\n" \
  "s_cbranch_scc0 HP_%=\n" \
  "s_mov_b32 s43, -1\n" \
  "HP_%=:\n" \
  "s_waitcnt lgkmcnt(0)\n" \
  "buffer_load_dwordx4 v[0:3], v92, s[8:11], s88 offen offset:0 sc1\n" \
  "v_mov_b32 v84, v84\n" \
  "s_nop 7\n" \
  "buffer_load_dwordx4 v[4:7], v92, s[8:11], s89 offen offset:1024 sc1\n" \
  "v_mov_b32 v84, v84\n" \
  "s_nop 7\n" \
  "buffer_load_dwordx4 v[8:11], v92, s[8:11], s90 offen offset:2048 sc1\n" \
  "ds_read_b128 v[36:39], v85 offset:10240\n" \
  "ds_read_b128 v[40:43], v85 offset:11264\n" \
  "s_nop 7\n" \
  "v_mov_b32 v44, 0\n" \
  "v_mov_b32 v45, 0\n" \
  "v_mov_b32 v46, 0\n" \
  "v_mov_b32 v47, 0\n" \
  "v_mov_b32 v48, 0\n" \
  "v_mov_b32 v49, 0\n" \
  "v_mov_b32 v50, 0\n" \
  "v_mov_b32 v51, 0\n" \
  "v_mov_b32 v52, 0\n" \
  "v_mov_b32 v53, 0\n" \
  "v_mov_b32 v54, 0\n" \
  "v_mov_b32 v55, 0\n" \
  "v_mov_b32 v56, 0\n" \
  "v_mov_b32 v57, 0\n" \
  "v_mov_b32 v58, 0\n" \
  "v_mov_b32 v59, 0\n" \
  "v_mov_b32 v60, 0\n" \
  "v_mov_b32 v61, 0\n" \
  "v_mov_b32 v62, 0\n" \
  "v_mov_b32 v63, 0\n" \
  "v_mov_b32 v64, 0\n" \
  "v_mov_b32 v65, 0\n" \
  "v_mov_b32 v66, 0\n" \
  "v_mov_b32 v67, 0\n" \
  "s_waitcnt lgkmcnt(0)\n" \
  "s_branch HL_%=\n" \
  "HF_%=:\n" \
  "s_nop 7\n" \
  "ds_read_b128 v[104:107], v85 offset:32768\n" \
  "ds_read_b128 v[108:111], v85 offset:33792\n" \
  "ds_read_b128 v[112:115], v85 offset:34816\n" \
  "ds_read_b128 v[116:119], v86 offset:32768\n" \
  "ds_read_b128 v[120:123], v86 offset:33792\n" \
  "ds_read_b128 v[124:127], v87 offset:32768\n" \
  "ds_read_b128 v[128:131], v87 offset:33792\n" \
  "ds_read_b128 v[132:135], v87 offset:34816\n" \
  "ds_read_b128 v[136:139], v88 offset:32768\n" \
  "ds_read_b128 v[140:143], v88 offset:33792\n" \
  "s_waitcnt lgkmcnt(0)\n" \
  "v_max_u32 v59, v104, v105\n" \
  "v_max3_u32 v59, v59, v106, v107\n" \
  "v_max3_u32 v59, v59, v108, v109\n" \
  "v_max3_u32 v59, v59, v110, v111\n" \
  "v_max3_u32 v59, v59, v112, v113\n" \
  "v_max3_u32 v59, v59, v114, v115\n" \
  "v_max3_u32 v59, v59, v116, v117\n" \
  "v_max3_u32 v59, v59, v118, v119\n" \
  "v_max3_u32 v59, v59, v120, v121\n" \
  "v_max3_u32 v59, v59, v122, v123\n" \
  "v_max3_u32 v59, v59, v124, v125\n" \
  "v_max3_u32 v59, v59, v126, v127\n" \
  "v_max3_u32 v59, v59, v128, v129\n" \
  "v_max3_u32 v59, v59, v130, v131\n" \
  "v_max3_u32 v59, v59, v132, v133\n" \
  "v_max3_u32 v59, v59, v134, v135\n" \
  "v_max3_u32 v59, v59, v136, v137\n" \
  "v_max3_u32 v59, v59, v138, v139\n" \
  "v_max3_u32 v59, v59, v140, v141\n" \
  "v_max3_u32 v59, v59, v142, v143\n" \
  "v_cmp_lt_u32 vcc, s43, v59\n" \
  "s_barrier\n" \
  "s_cbranch_vccnz HG_%=\n" \
  "v_max3_f32 v58, v44, v45, v46\n" \
  "v_max3_f32 v58, v148, v149, v58\n" \
  "v_max3_f32 v58, v150, v151, v58\n" \
  "v_max_f32 v58, v58, v47\n" \
  "s_nop 1\n" \
  "v_max_f32_dpp v58, v58, v58 row_ror:8 row_mask:0xf bank_mask:0xf\n" \
  "v_mov_b32 v59, v58\n" \
  "s_nop 1\n" \
  "v_permlane32_swap_b32 v58, v59\n" \
  "v_max_f32 v58, v58, v59\n" \
  "v_mov_b32 v59, v58\n" \
  "s_nop 1\n" \
  "v_permlane16_swap_b32 v58, v59\n" \
  "v_max_f32 v58, v58, v59\n" \
  "v_sub_f32 v48, v44, v58\n" \
  "v_exp_f32 v48, v48\n" \
  "v_sub_f32 v49, v45, v58\n" \
  "v_exp_f32 v49, v49\n" \
  "v_sub_f32 v50, v46, v58\n" \
  "v_exp_f32 v50, v50\n" \
  "v_sub_f32 v51, v47, v58\n" \
  "v_exp_f32 v51, v51\n" \
  "v_sub_f32 v52, v148, v58\n" \
  "v_exp_f32 v52, v52\n" \
  "v_sub_f32 v53, v149, v58\n" \
  "v_exp_f32 v53, v53\n" \
  "v_sub_f32 v54, v150, v58\n" \
  "v_exp_f32 v54, v54\n" \
  "v_sub_f32 v55, v151, v58\n" \
  "v_exp_f32 v55, v55\n" \
  "v_cvt_pk_bf16_f32 v72, v48, v49\n" \
  "v_cvt_pk_bf16_f32 v73, v50, v51\n" \
  "v_cvt_pk_bf16_f32 v56, v52, v53\n" \
  "v_cvt_pk_bf16_f32 v57, v54, v55\n" \
  "v_mov_b32_dpp v74, v72 row_ror:8 row_mask:0xf bank_mask:0xf\n" \
  "v_mov_b32_dpp v75, v73 row_ror:8 row_mask:0xf bank_mask:0xf\n" \
  "s_nop 1\n" \
  "v_mfma_f32_16x16x32_bf16 v[152:155], v[100:103], v[72:75], 0\n" \
  "v_mfma_f32_16x16x32_bf16 v[156:159], v[76:79], v[72:75], 0\n" \
  "v_mfma_f32_16x16x32_bf16 v[136:139], v[80:83], v[72:75], 0\n" \
  "s_mov_b32 s49, -1\n" \
  "s_nop 7\n" \
  "s_branch HM_%=\n" \
  "HG_%=:\n" \
  "s_sub_u32 s44, s44, 1\n" \
  "s_cbranch_scc0 HH_%=\n" \
  "s_mov_b32 s43, -1\n" \
  "HH_%=:\n" \
  "s_barrier\n" \
  "buffer_load_dwordx4 v[68:71], v85, s[8:11], s40 offen sc1\n" \
  "buffer_load_dwordx2 v[72:73], v85, s[8:11], s40 offen offset:1032 sc1\n" \
  "buffer_load_dwordx2 v[74:75], v85, s[8:11], s40 offen offset:1024 sc1\n" \
  "buffer_load_dwordx4 v[76:79], v85, s[8:11], s45 offen sc1\n" \
  "buffer_load_dwordx4 v[80:83], v85, s[8:11], s45 offen offset:1024 sc1\n" \
  "ds_read_b128 v[12:15], v85 offset:24576\n" \
  "ds_read_b128 v[16:19], v85 offset:25600\n" \
  "ds_read_b128 v[20:23], v85 offset:26624\n" \
  "ds_read_b128 v[24:27], v85 offset:27648\n" \
  "ds_read_b128 v[28:31], v85 offset:8192\n" \
  "ds_read_b128 v[32:35], v85 offset:9216\n" \
  "ds_read_b128 v[36:39], v85 offset:10240\n" \
  "ds_read_b128 v[40:43], v85 offset:11264\n" \
  "ds_read_b128 v[104:107], v85 offset:28672\n" \
  "ds_read_b128 v[108:111], v85 offset:29696\n" \
  "ds_read_b128 v[112:115], v85 offset:30720\n" \
  "ds_read_b128 v[116:119], v85 offset:31744\n" \
  "ds_read_b128 v[120:123], v85 offset:16384\n" \
  "ds_read_b128 v[124:127], v85 offset:17408\n" \
  "ds_read_b128 v[128:131], v85 offset:18432\n" \
  "ds_read_b128 v[132:135], v85 offset:19456\n" \
  "ds_read_b128 v[136:139], v85 offset:4096\n" \
  "ds_read_b128 v[140:143], v85 offset:5120\n" \
  "ds_read_b128 v[144:147], v85 offset:6144\n" \
  "ds_read_b128 v[148:151], v85 offset:7168\n" \
  "ds_read_b128 v[152:155], v85 offset:12288\n" \
  "ds_read_b128 v[156:159], v85 offset:13312\n" \
  "v_mov_b32 v44, 0\n" \
  "v_mov_b32 v45, 0\n" \
  "v_mov_b32 v46, 0\n" \
  "v_mov_b32 v47, 0\n" \
  "v_mov_b32 v48, 0\n" \
  "v_mov_b32 v49, 0\n" \
  "v_mov_b32 v50, 0\n" \
  "v_mov_b32 v51, 0\n" \
  "v_mov_b32 v52, 0\n" \
  "v_mov_b32 v53, 0\n" \
  "v_mov_b32 v54, 0\n" \
  "v_mov_b32 v55, 0\n" \
  "v_mov_b32 v56, 0\n" \
  "v_mov_b32 v57, 0\n" \
  "v_mov_b32 v58, 0\n" \
  "v_mov_b32 v59, 0\n" \
  "v_mov_b32 v60, 0\n" \
  "v_mov_b32 v61, 0\n" \
  "v_mov_b32 v62, 0\n" \
  "v_mov_b32 v63, 0\n" \
  "v_mov_b32 v64, 0\n" \
  "v_mov_b32 v65, 0\n" \
  "v_mov_b32 v66, 0\n" \
  "v_mov_b32 v67, 0\n" \
  "s_waitcnt vmcnt(0)\n" \
  "s_waitcnt lgkmcnt(0)\n" \
  "buffer_load_dwordx4 v[0:3], v92, s[8:11], s88 offen offset:0 sc1\n" \
  "v_mov_b32 v84, v84\n" \
  "s_nop 7\n" \
  "buffer_load_dwordx4 v[4:7], v92, s[8:11], s89 offen offset:1024 sc1\n" \
  "v_mov_b32 v84, v84\n" \
  "s_nop 7\n" \
  "buffer_load_dwordx4 v[8:11], v92, s[8:11], s90 offen offset:2048 sc1\n" \
  "s_branch HL_%=\n" \
  "HE_%=:\n" \
  "s_waitcnt vmcnt(0)\n" \
  "v_mov_b32 v41, 0x40000000\n" \
  "v_xor_b32 v93, 0x2000, v93\n" \
  "buffer_store_dword v41, v93, s[8:11], 0 offen sc1\n" \
  "s_waitcnt vmcnt(0)\n"

#define ASM_H_0 \
  "s_add_u32 m0, s34, 0\n" \
  "s_nop 0\n" \
  "buffer_load_dwordx4 v108, s[20:23], s24 offen lds\n" \
  "buffer_load_dwordx4 v109, s[20:23], s24 offen offset:1024 lds\n" \
  "buffer_load_dwordx4 v110, s[20:23], s24 offen offset:2048 lds\n" \
  "buffer_load_dwordx4 v111, s[20:23], s24 offen offset:3072 lds\n" \
  "s_add_u32 m0, s34, 4096\n" \
  "s_nop 0\n" \
  "buffer_load_dwordx4 v108, s[20:23], s25 offen lds\n" \
  "buffer_load_dwordx4 v109, s[20:23], s25 offen offset:1024 lds\n" \
  "buffer_load_dwordx4 v110, s[20:23], s25 offen offset:2048 lds\n" \
  "buffer_load_dwordx4 v111, s[20:23], s25 offen offset:3072 lds\n" \
  "s_add_u32 m0, s34, 8192\n" \
  "s_nop 0\n" \
  "buffer_load_dwordx4 v108, s[20:23], s26 offen lds\n" \
  "buffer_load_dwordx4 v109, s[20:23], s26 offen offset:1024 lds\n" \
  "buffer_load_dwordx4 v110, s[20:23], s26 offen offset:2048 lds\n" \
  "buffer_load_dwordx4 v111, s[20:23], s26 offen offset:3072 lds\n" \
  "s_add_u32 m0, s34, 12288\n" \
  "s_nop 0\n" \
  "buffer_load_dwordx4 v108, s[20:23], s27 offen lds\n" \
  "buffer_load_dwordx4 v109, s[20:23], s27 offen offset:1024 lds\n" \
  "buffer_load_dwordx4 v110, s[20:23], s27 offen offset:2048 lds\n" \
  "buffer_load_dwordx4 v111, s[20:23], s27 offen offset:3072 lds\n" \
  "s_add_u32 m0, s34, 16384\n" \
  "s_nop 0\n" \
  "buffer_load_dwordx4 v108, s[20:23], s28 offen lds\n" \
  "buffer_load_dwordx4 v109, s[20:23], s28 offen offset:1024 lds\n" \
  "buffer_load_dwordx4 v110, s[20:23], s28 offen offset:2048 lds\n" \
  "buffer_load_dwordx4 v111, s[20:23], s28 offen offset:3072 lds\n" \
  "s_add_u32 m0, s34, 20480\n" \
  "s_nop 0\n" \
  "buffer_load_dwordx4 v108, s[20:23], s29 offen lds\n" \
  "buffer_load_dwordx4 v109, s[20:23], s29 offen offset:1024 lds\n" \
  "buffer_load_dwordx4 v110, s[20:23], s29 offen offset:2048 lds\n" \
  "buffer_load_dwordx4 v111, s[20:23], s29 offen offset:3072 lds\n" \
  "s_add_u32 m0, s34, 24576\n" \
  "s_nop 0\n" \
  "buffer_load_dwordx4 v108, s[20:23], s30 offen lds\n" \
  "buffer_load_dwordx4 v109, s[20:23], s30 offen offset:1024 lds\n" \
  "buffer_load_dwordx4 v110, s[20:23], s30 offen offset:2048 lds\n" \
  "buffer_load_dwordx4 v111, s[20:23], s30 offen offset:3072 lds\n" \
  "s_add_u32 m0, s34, 28672\n" \
  "s_nop 0\n" \
  "buffer_load_dwordx4 v108, s[20:23], s31 offen lds\n" \
  "buffer_load_dwordx4 v109, s[20:23], s31 offen offset:1024 lds\n" \
  "buffer_load_dwordx4 v110, s[20:23], s31 offen offset:2048 lds\n" \
  "buffer_load_dwordx4 v111, s[20:23], s31 offen offset:3072 lds\n"

#define ASM_Y2 \
  "s_add_u32 m0, s34, 0\n" \
  "s_add_u32 s42, s32, 0\n" \
  "buffer_load_dwordx4 v56, s[12:15], s42 offen lds\n" \
  "buffer_load_dwordx4 v57, s[12:15], s42 offen offset:1024 lds\n" \
  "buffer_load_dwordx4 v58, s[12:15], s42 offen offset:2048 lds\n" \
  "buffer_load_dwordx4 v59, s[12:15], s42 offen offset:3072 lds\n" \
  "s_add_u32 m0, s34, 4096\n" \
  "s_add_u32 s42, s32, 10240\n" \
  "buffer_load_dwordx4 v56, s[12:15], s42 offen lds\n" \
  "buffer_load_dwordx4 v57, s[12:15], s42 offen offset:1024 lds\n" \
  "buffer_load_dwordx4 v58, s[12:15], s42 offen offset:2048 lds\n" \
  "buffer_load_dwordx4 v59, s[12:15], s42 offen offset:3072 lds\n" \
  "s_add_u32 m0, s34, 8192\n" \
  "s_add_u32 s42, s32, 163840\n" \
  "buffer_load_dwordx4 v56, s[12:15], s42 offen lds\n" \
  "buffer_load_dwordx4 v57, s[12:15], s42 offen offset:1024 lds\n" \
  "buffer_load_dwordx4 v58, s[12:15], s42 offen offset:2048 lds\n" \
  "buffer_load_dwordx4 v59, s[12:15], s42 offen offset:3072 lds\n" \
  "s_add_u32 m0, s34, 12288\n" \
  "s_add_u32 s42, s32, 174080\n" \
  "buffer_load_dwordx4 v56, s[12:15], s42 offen lds\n" \
  "buffer_load_dwordx4 v57, s[12:15], s42 offen offset:1024 lds\n" \
  "buffer_load_dwordx4 v58, s[12:15], s42 offen offset:2048 lds\n" \
  "buffer_load_dwordx4 v59, s[12:15], s42 offen offset:3072 lds\n" \
  "s_add_u32 m0, s34, 16384\n" \
  "s_add_u32 s42, s32, 327680\n" \
  "buffer_load_dwordx4 v56, s[12:15], s42 offen lds\n" \
  "buffer_load_dwordx4 v57, s[12:15], s42 offen offset:1024 lds\n" \
  "buffer_load_dwordx4 v58, s[12:15], s42 offen offset:2048 lds\n" \
  "buffer_load_dwordx4 v59, s[12:15], s42 offen offset:3072 lds\n" \
  "s_add_u32 m0, s34, 20480\n" \
  "s_add_u32 s42, s32, 337920\n" \
  "buffer_load_dwordx4 v56, s[12:15], s42 offen lds\n" \
  "buffer_load_dwordx4 v57, s[12:15], s42 offen offset:1024 lds\n" \
  "buffer_load_dwordx4 v58, s[12:15], s42 offen offset:2048 lds\n" \
  "buffer_load_dwordx4 v59, s[12:15], s42 offen offset:3072 lds\n" \
  "s_add_u32 m0, s34, 24576\n" \
  "s_add_u32 s42, s32, 491520\n" \
  "buffer_load_dwordx4 v56, s[12:15], s42 offen lds\n" \
  "buffer_load_dwordx4 v57, s[12:15], s42 offen offset:1024 lds\n" \
  "buffer_load_dwordx4 v58, s[12:15], s42 offen offset:2048 lds\n" \
  "buffer_load_dwordx4 v59, s[12:15], s42 offen offset:3072 lds\n" \
  "s_add_u32 m0, s34, 28672\n" \
  "s_add_u32 s42, s32, 501760\n" \
  "buffer_load_dwordx4 v56, s[12:15], s42 offen lds\n" \
  "buffer_load_dwordx4 v57, s[12:15], s42 offen offset:1024 lds\n" \
  "buffer_load_dwordx4 v58, s[12:15], s42 offen offset:2048 lds\n" \
  "buffer_load_dwordx4 v59, s[12:15], s42 offen offset:3072 lds\n" \
  "s_waitcnt vmcnt(28)\n" \
  "ds_read_b64_tr_b16 a[0:1], v60\n" \
  "ds_read_b64_tr_b16 a[2:3], v60 offset:1024\n" \
  "ds_read_b64_tr_b16 a[4:5], v60 offset:2048\n" \
  "ds_read_b64_tr_b16 a[6:7], v60 offset:3072\n" \
  "ds_read_b64_tr_b16 a[8:9], v61\n" \
  "ds_read_b64_tr_b16 a[10:11], v61 offset:1024\n" \
  "ds_read_b64_tr_b16 a[12:13], v61 offset:2048\n" \
  "ds_read_b64_tr_b16 a[14:15], v61 offset:3072\n" \
  "s_waitcnt lgkmcnt(0)\n" \
  "s_add_u32 m0, s34, 0\n" \
  "s_add_u32 s42, s33, 0\n" \
  "buffer_load_dwordx4 v56, s[12:15], s42 offen lds\n" \
  "buffer_load_dwordx4 v57, s[12:15], s42 offen offset:1024 lds\n" \
  "buffer_load_dwordx4 v58, s[12:15], s42 offen offset:2048 lds\n" \
  "buffer_load_dwordx4 v59, s[12:15], s42 offen offset:3072 lds\n" \
  "s_waitcnt vmcnt(28)\n" \
  "ds_read_b64_tr_b16 a[16:17], v60 offset:4096\n" \
  "ds_read_b64_tr_b16 a[18:19], v60 offset:5120\n" \
  "ds_read_b64_tr_b16 a[20:21], v60 offset:6144\n" \
  "ds_read_b64_tr_b16 a[22:23], v60 offset:7168\n" \
  "ds_read_b64_tr_b16 a[24:25], v61 offset:4096\n" \
  "ds_read_b64_tr_b16 a[26:27], v61 offset:5120\n" \
  "ds_read_b64_tr_b16 a[28:29], v61 offset:6144\n" \
  "ds_read_b64_tr_b16 a[30:31], v61 offset:7168\n" \
  "s_waitcnt lgkmcnt(0)\n" \
  "s_add_u32 m0, s34, 4096\n" \
  "s_add_u32 s42, s33, 10240\n" \
  "buffer_load_dwordx4 v56, s[12:15], s42 offen lds\n" \
  "buffer_load_dwordx4 v57, s[12:15], s42 offen offset:1024 lds\n" \
  "buffer_load_dwordx4 v58, s[12:15], s42 offen offset:2048 lds\n" \
  "buffer_load_dwordx4 v59, s[12:15], s42 offen offset:3072 lds\n" \
  "s_waitcnt vmcnt(28)\n" \
  "ds_read_b64_tr_b16 a[32:33], v60 offset:8192\n" \
  "ds_read_b64_tr_b16 a[34:35], v60 offset:9216\n" \
  "ds_read_b64_tr_b16 a[36:37], v60 offset:10240\n" \
  "ds_read_b64_tr_b16 a[38:39], v60 offset:11264\n" \
  "ds_read_b64_tr_b16 a[40:41], v61 offset:8192\n" \
  "ds_read_b64_tr_b16 a[42:43], v61 offset:9216\n" \
  "ds_read_b64_tr_b16 a[44:45], v61 offset:10240\n" \
  "ds_read_b64_tr_b16 a[46:47], v61 offset:11264\n" \
  "s_waitcnt lgkmcnt(0)\n" \
  "s_add_u32 m0, s34, 8192\n" \
  "s_add_u32 s42, s33, 163840\n" \
  "buffer_load_dwordx4 v56, s[12:15], s42 offen lds\n" \
  "buffer_load_dwordx4 v57, s[12:15], s42 offen offset:1024 lds\n" \
  "buffer_load_dwordx4 v58, s[12:15], s42 offen offset:2048 lds\n" \
  "buffer_load_dwordx4 v59, s[12:15], s42 offen offset:3072 lds\n" \
  "s_waitcnt vmcnt(28)\n" \
  "ds_read_b64_tr_b16 a[48:49], v60 offset:12288\n" \
  "ds_read_b64_tr_b16 a[50:51], v60 offset:13312\n" \
  "ds_read_b64_tr_b16 a[52:53], v60 offset:14336\n" \
  "ds_read_b64_tr_b16 a[54:55], v60 offset:15360\n" \
  "ds_read_b64_tr_b16 a[56:57], v61 offset:12288\n" \
  "ds_read_b64_tr_b16 a[58:59], v61 offset:13312\n" \
  "ds_read_b64_tr_b16 a[60:61], v61 offset:14336\n" \
  "ds_read_b64_tr_b16 a[62:63], v61 offset:15360\n" \
  "s_waitcnt lgkmcnt(0)\n" \
  "s_add_u32 m0, s34, 12288\n" \
  "s_add_u32 s42, s33, 174080\n" \
  "buffer_load_dwordx4 v56, s[12:15], s42 offen lds\n" \
  "buffer_load_dwordx4 v57, s[12:15], s42 offen offset:1024 lds\n" \
  "buffer_load_dwordx4 v58, s[12:15], s42 offen offset:2048 lds\n" \
  "buffer_load_dwordx4 v59, s[12:15], s42 offen offset:3072 lds\n" \
  "s_waitcnt vmcnt(28)\n" \
  "ds_read_b64_tr_b16 a[64:65], v60 offset:16384\n" \
  "ds_read_b64_tr_b16 a[66:67], v60 offset:17408\n" \
  "ds_read_b64_tr_b16 a[68:69], v60 offset:18432\n" \
  "ds_read_b64_tr_b16 a[70:71], v60 offset:19456\n" \
  "ds_read_b64_tr_b16 a[72:73], v61 offset:16384\n" \
  "ds_read_b64_tr_b16 a[74:75], v61 offset:17408\n" \
  "ds_read_b64_tr_b16 a[76:77], v61 offset:18432\n" \
  "ds_read_b64_tr_b16 a[78:79], v61 offset:19456\n" \
  "s_waitcnt lgkmcnt(0)\n" \
  "s_add_u32 m0, s34, 16384\n" \
  "s_add_u32 s42, s33, 327680\n" \
  "buffer_load_dwordx4 v56, s[12:15], s42 offen lds\n" \
  "buffer_load_dwordx4 v57, s[12:15], s42 offen offset:1024 lds\n" \
  "buffer_load_dwordx4 v58, s[12:15], s42 offen offset:2048 lds\n" \
  "buffer_load_dwordx4 v59, s[12:15], s42 offen offset:3072 lds\n" \
  "s_waitcnt vmcnt(28)\n" \
  "ds_read_b64_tr_b16 a[80:81], v60 offset:20480\n" \
  "ds_read_b64_tr_b16 a[82:83], v60 offset:21504\n" \
  "ds_read_b64_tr_b16 a[84:85], v60 offset:22528\n" \
  "ds_read_b64_tr_b16 a[86:87], v60 offset:23552\n" \
  "ds_read_b64_tr_b16 a[88:89], v61 offset:20480\n" \
  "ds_read_b64_tr_b16 a[90:91], v61 offset:21504\n" \
  "ds_read_b64_tr_b16 a[92:93], v61 offset:22528\n" \
  "ds_read_b64_tr_b16 a[94:95], v61 offset:23552\n" \
  "s_waitcnt lgkmcnt(0)\n" \
  "s_add_u32 m0, s34, 20480\n" \
  "s_add_u32 s42, s33, 337920\n" \
  "buffer_load_dwordx4 v56, s[12:15], s42 offen lds\n" \
  "buffer_load_dwordx4 v57, s[12:15], s42 offen offset:1024 lds\n" \
  "buffer_load_dwordx4 v58, s[12:15], s42 offen offset:2048 lds\n" \
  "buffer_load_dwordx4 v59, s[12:15], s42 offen offset:3072 lds\n" \
  "s_waitcnt vmcnt(28)\n" \
  "ds_read_b64_tr_b16 a[96:97], v60 offset:24576\n" \
  "ds_read_b64_tr_b16 a[98:99], v60 offset:25600\n" \
  "ds_read_b64_tr_b16 a[100:101], v60 offset:26624\n" \
  "ds_read_b64_tr_b16 a[102:103], v60 offset:27648\n" \
  "ds_read_b64_tr_b16 a[104:105], v61 offset:24576\n" \
  "ds_read_b64_tr_b16 a[106:107], v61 offset:25600\n" \
  "ds_read_b64_tr_b16 a[108:109], v61 offset:26624\n" \
  "ds_read_b64_tr_b16 a[110:111], v61 offset:27648\n" \
  "s_waitcnt lgkmcnt(0)\n" \
  "s_add_u32 m0, s34, 24576\n" \
  "s_add_u32 s42, s33, 491520\n" \
  "buffer_load_dwordx4 v56, s[12:15], s42 offen lds\n" \
  "buffer_load_dwordx4 v57, s[12:15], s42 offen offset:1024 lds\n" \
  "buffer_load_dwordx4 v58, s[12:15], s42 offen offset:2048 lds\n" \
  "buffer_load_dwordx4 v59, s[12:15], s42 offen offset:3072 lds\n" \
  "s_waitcnt vmcnt(28)\n" \
  "ds_read_b64_tr_b16 a[112:113], v60 offset:28672\n" \
  "ds_read_b64_tr_b16 a[114:115], v60 offset:29696\n" \
  "ds_read_b64_tr_b16 a[116:117], v60 offset:30720\n" \
  "ds_read_b64_tr_b16 a[118:119], v60 offset:31744\n" \
  "ds_read_b64_tr_b16 a[120:121], v61 offset:28672\n" \
  "ds_read_b64_tr_b16 a[122:123], v61 offset:29696\n" \
  "ds_read_b64_tr_b16 a[124:125], v61 offset:30720\n" \
  "ds_read_b64_tr_b16 a[126:127], v61 offset:31744\n" \
  "s_waitcnt lgkmcnt(0)\n" \
  "s_add_u32 m0, s34, 28672\n" \
  "s_add_u32 s42, s33, 501760\n" \
  "buffer_load_dwordx4 v56, s[12:15], s42 offen lds\n" \
  "buffer_load_dwordx4 v57, s[12:15], s42 offen offset:1024 lds\n" \
  "buffer_load_dwordx4 v58, s[12:15], s42 offen offset:2048 lds\n" \
  "buffer_load_dwordx4 v59, s[12:15], s42 offen offset:3072 lds\n" \
  "buffer_load_dwordx2 v[0:1], off, s[8:11], s48 offset:4 sc1\n" \
  "s_waitcnt vmcnt(29)\n" \
  "ds_read_b64_tr_b16 a[128:129], v60\n" \
  "ds_read_b64_tr_b16 a[130:131], v60 offset:1024\n" \
  "ds_read_b64_tr_b16 a[132:133], v60 offset:2048\n" \
  "ds_read_b64_tr_b16 a[134:135], v60 offset:3072\n" \
  "ds_read_b64_tr_b16 a[136:137], v61\n" \
  "ds_read_b64_tr_b16 a[138:139], v61 offset:1024\n" \
  "ds_read_b64_tr_b16 a[140:141], v61 offset:2048\n" \
  "ds_read_b64_tr_b16 a[142:143], v61 offset:3072\n" \
  "s_waitcnt lgkmcnt(0)\n" \
  "s_waitcnt vmcnt(25)\n" \
  "ds_read_b64_tr_b16 a[144:145], v60 offset:4096\n" \
  "ds_read_b64_tr_b16 a[146:147], v60 offset:5120\n" \
  "ds_read_b64_tr_b16 a[148:149], v60 offset:6144\n" \
  "ds_read_b64_tr_b16 a[150:151], v60 offset:7168\n" \
  "ds_read_b64_tr_b16 a[152:153], v61 offset:4096\n" \
  "ds_read_b64_tr_b16 a[154:155], v61 offset:5120\n" \
  "ds_read_b64_tr_b16 a[156:157], v61 offset:6144\n" \
  "ds_read_b64_tr_b16 a[158:159], v61 offset:7168\n" \
  "s_waitcnt lgkmcnt(0)\n" \
  "s_waitcnt vmcnt(21)\n" \
  "ds_read_b64_tr_b16 a[160:161], v60 offset:8192\n" \
  "ds_read_b64_tr_b16 a[162:163], v60 offset:9216\n" \
  "ds_read_b64_tr_b16 a[164:165], v60 offset:10240\n" \
  "ds_read_b64_tr_b16 a[166:167], v60 offset:11264\n" \
  "ds_read_b64_tr_b16 a[168:169], v61 offset:8192\n" \
  "ds_read_b64_tr_b16 a[170:171], v61 offset:9216\n" \
  "ds_read_b64_tr_b16 a[172:173], v61 offset:10240\n" \
  "ds_read_b64_tr_b16 a[174:175], v61 offset:11264\n" \
  "s_waitcnt lgkmcnt(0)\n" \
  "s_waitcnt vmcnt(17)\n" \
  "ds_read_b64_tr_b16 a[176:177], v60 offset:12288\n" \
  "ds_read_b64_tr_b16 a[178:179], v60 offset:13312\n" \
  "ds_read_b64_tr_b16 a[180:181], v60 offset:14336\n" \
  "ds_read_b64_tr_b16 a[182:183], v60 offset:15360\n" \
  "ds_read_b64_tr_b16 a[184:185], v61 offset:12288\n" \
  "ds_read_b64_tr_b16 a[186:187], v61 offset:13312\n" \
  "ds_read_b64_tr_b16 a[188:189], v61 offset:14336\n" \
  "ds_read_b64_tr_b16 a[190:191], v61 offset:15360\n" \
  "s_waitcnt lgkmcnt(0)\n" \
  "s_waitcnt vmcnt(13)\n" \
  "ds_read_b64_tr_b16 a[192:193], v60 offset:16384\n" \
  "ds_read_b64_tr_b16 a[194:195], v60 offset:17408\n" \
  "ds_read_b64_tr_b16 a[196:197], v60 offset:18432\n" \
  "ds_read_b64_tr_b16 a[198:199], v60 offset:19456\n" \
  "ds_read_b64_tr_b16 a[200:201], v61 offset:16384\n" \
  "ds_read_b64_tr_b16 a[202:203], v61 offset:17408\n" \
  "ds_read_b64_tr_b16 a[204:205], v61 offset:18432\n" \
  "ds_read_b64_tr_b16 a[206:207], v61 offset:19456\n" \
  "s_waitcnt lgkmcnt(0)\n" \
  "s_waitcnt vmcnt(9)\n" \
  "ds_read_b64_tr_b16 a[208:209], v60 offset:20480\n" \
  "ds_read_b64_tr_b16 a[210:211], v60 offset:21504\n" \
  "ds_read_b64_tr_b16 a[212:213], v60 offset:22528\n" \
  "ds_read_b64_tr_b16 a[214:215], v60 offset:23552\n" \
  "ds_read_b64_tr_b16 a[216:217], v61 offset:20480\n" \
  "ds_read_b64_tr_b16 a[218:219], v61 offset:21504\n" \
  "ds_read_b64_tr_b16 a[220:221], v61 offset:22528\n" \
  "ds_read_b64_tr_b16 a[222:223], v61 offset:23552\n" \
  "s_waitcnt lgkmcnt(0)\n" \
  "s_waitcnt vmcnt(5)\n" \
  "ds_read_b64_tr_b16 a[224:225], v60 offset:24576\n" \
  "ds_read_b64_tr_b16 a[226:227], v60 offset:25600\n" \
  "ds_read_b64_tr_b16 a[228:229], v60 offset:26624\n" \
  "ds_read_b64_tr_b16 a[230:231], v60 offset:27648\n" \
  "ds_read_b64_tr_b16 a[232:233], v61 offset:24576\n" \
  "ds_read_b64_tr_b16 a[234:235], v61 offset:25600\n" \
  "ds_read_b64_tr_b16 a[236:237], v61 offset:26624\n" \
  "ds_read_b64_tr_b16 a[238:239], v61 offset:27648\n" \
  "s_waitcnt lgkmcnt(0)\n" \
  "s_waitcnt vmcnt(1)\n" \
  "ds_read_b64_tr_b16 a[240:241], v60 offset:28672\n" \
  "ds_read_b64_tr_b16 a[242:243], v60 offset:29696\n" \
  "ds_read_b64_tr_b16 a[244:245], v60 offset:30720\n" \
  "ds_read_b64_tr_b16 a[246:247], v60 offset:31744\n" \
  "ds_read_b64_tr_b16 a[248:249], v61 offset:28672\n" \
  "ds_read_b64_tr_b16 a[250:251], v61 offset:29696\n" \
  "ds_read_b64_tr_b16 a[252:253], v61 offset:30720\n" \
  "ds_read_b64_tr_b16 a[254:255], v61 offset:31744\n" \
  "s_waitcnt lgkmcnt(0)\n" \
  "s_branch YAS_%=\n" \
  "YAR_%=:\n" \
  "buffer_load_dwordx2 v[0:1], off, s[8:11], s48 offset:4 sc1\n" \
  "YAS_%=:\n" \
  "s_waitcnt vmcnt(0)\n" \
  "v_readfirstlane_b32 s46, v0\n" \
  "v_readfirstlane_b32 s47, v1\n" \
  "s_nop 3\n" \
  "s_or_b32 s46, s46, 0x3fff\n" \
  "s_add_u32 s46, s46, 1\n" \
  "s_xor_b32 s46, s46, s47\n" \
  "s_andn2_b32 s47, s46, 0x2000\n" \
  "s_cbranch_scc0 YAT_%=\n" \
  "s_sleep 2\n" \
  "s_sub_u32 s44, s44, 1\n" \
  "s_cbranch_scc0 YAR_%=\n" \
  "s_mov_b32 s44, 0\n" \
  "YAT_%=:\n" \
  "s_and_b32 s46, s46, 0x2000\n" \
  "s_add_u32 s37, s37, s46\n" \
  "s_add_u32 s38, s38, s46\n" \
  "s_add_u32 s39, s39, s46\n" \
  "s_add_u32 s40, s40, s46\n" \
  "s_add_u32 s57, s57, s46\n" \
  "s_getpc_b64 s[74:75]\n" \
  "YPCP_%=:\n" \
  "s_add_u32 s74, s74, YPCE_%=-YPCP_%=\n" \
  "s_addc_u32 s75, s75, 0\n" \
  "s_memtime  s[70:71]\n" \
  "s_mov_b32 s68, 0\n" \
  "s_waitcnt lgkmcnt(0)\n" \
  "s_mov_b32 s59, s70\n" \
  "YL_%=:\n" \
  "s_sub_u32 s46, s59, s68\n" \
  "YQ_%=:\n" \
  "s_memtime  s[70:71]\n" \
  "s_waitcnt lgkmcnt(0)\n" \
  "s_sub_u32 s70, s70, s46\n" \
  "s_cmp_lt_i32 s70, 0\n" \
  "s_cbranch_scc1 YQ_%=\n" \
  "s_memtime  s[72:73]\n" \
  "s_memrealtime s[70:71]\n" \
  "s_nop 2\n" \
  "s_memrealtime s[66:67]\n" \
  "s_nop 2\n" \
  "s_memrealtime s[60:61]\n" \
  "s_nop 2\n" \
  "s_memrealtime s[62:63]\n" \
  "s_nop 2\n" \
  "s_memrealtime s[64:65]\n" \
  "s_nop 2\n" \
  "s_memrealtime s[50:51]\n" \
  "s_waitcnt lgkmcnt(0)\n" \
  "s_add_u32 s66, s66, s60\n" \
  "s_add_u32 s62, s62, s64\n" \
  "s_add_u32 s66, s66, s50\n" \
  "s_mul_i32 s60, s70, 5\n" \
  "s_add_u32 s66, s66, s62\n" \
  "s_sub_u32 s66, s66, s60\n" \
  "s_mul_i32 s66, s66, 245\n" \
  "s_lshr_b32 s66, s66, 6\n" \
  "s_mul_i32 s68, s70, 23\n" \
  "s_add_u32 s68, s68, s66\n" \
  "s_sub_u32 s68, s68, s72\n" \
  "s_mov_b64 exec, 0xff\n" \
  "s_mov_b32 s46, 0x2000\n" \
  "buffer_load_dword v0, v62, s[8:11], s57 offen sc1\n" \
  "s_sleep 3\n" \
  "buffer_load_dword v4, v62, s[8:11], s57 offen sc1\n" \
  "s_sleep 3\n" \
  "YPA_%=:\n" \
  "s_waitcnt vmcnt(1)\n" \
  "v_mov_b32 v12, v0\n" \
  "v_cmp_gt_i32 vcc, s56, v0\n" \
  "s_cbranch_vccz YPB_%=\n" \
  "buffer_load_dword v0, v62, s[8:11], s57 offen sc1\n" \
  "s_waitcnt vmcnt(1)\n" \
  "v_mov_b32 v12, v4\n" \
  "v_cmp_gt_i32 vcc, s56, v4\n" \
  "s_cbranch_vccz YPB_%=\n" \
  "buffer_load_dword v4, v62, s[8:11], s57 offen sc1\n" \
  "s_sub_u32 s46, s46, 1\n" \
  "s_cbranch_scc0 YPA_%=\n" \
  "YPB_%=:\n" \
  "s_add_u32 s69, s72, s68\n" \
  "v_subrev_u32 v12, s69, v12\n" \
  "v_lshlrev_b32 v12, 8, v12\n" \
  "s_nop 1\n" \
  "v_max_i32_dpp v12, v12, v12 row_shr:1 row_mask:0xf bank_mask:0xf\n" \
  "s_nop 1\n" \
  "v_max_i32_dpp v12, v12, v12 row_shr:2 row_mask:0xf bank_mask:0xf\n" \
  "s_nop 1\n" \
  "v_max_i32_dpp v12, v12, v12 row_shr:4 row_mask:0xf bank_mask:0xf\n" \
  "s_nop 1\n" \
  "v_readlane_b32 s46, v12, 7\n" \
  "s_nop 3\n" \
  "s_ashr_i32 s46, s46, 8\n" \
  "s_min_i32 s46, s46, 0x4000\n" \
  "s_add_u32 s46, s46, s35\n" \
  "s_add_u32 s46, s46, s72\n" \
  "s_add_u32 s59, s46, s68\n" \
  "s_add_u32 s59, s59, s55\n" \
  "s_mov_b64 exec, -1\n" \
  "YPC_%=:\n" \
  "s_memtime  s[70:71]\n" \
  "v_mov_b32 v48, v48\n" \
  "v_mov_b32 v48, v48\n" \
  "s_waitcnt lgkmcnt(0)\n" \
  "s_sub_u32 s70, s46, s70\n" \
  "s_cmp_gt_i32 s70, 224\n" \
  "s_cbranch_scc1 YPC_%=\n" \
  "s_sub_u32 s70, s70, 96\n" \
  "s_max_i32 s70, s70, 0\n" \
  "s_and_b32 s70, s70, -4\n" \
  "s_sub_u32 s70, s74, s70\n" \
  "s_subb_u32 s71, s75, 0\n" \
  "s_setpc_b64 s[70:71]\n" \
  "v_mov_b32 v48, v48\n" \
  "v_mov_b32 v48, v48\n" \
  "v_mov_b32 v48, v48\n" \
  "v_mov_b32 v48, v48\n" \
  "v_mov_b32 v48, v48\n" \
  "v_mov_b32 v48, v48\n" \
  "v_mov_b32 v48, v48\n" \
  "v_mov_b32 v48, v48\n" \
  "v_mov_b32 v48, v48\n" \
  "v_mov_b32 v48, v48\n" \
  "v_mov_b32 v48, v48\n" \
  "v_mov_b32 v48, v48\n" \
  "v_mov_b32 v48, v48\n" \
  "v_mov_b32 v48, v48\n" \
  "v_mov_b32 v48, v48\n" \
  "v_mov_b32 v48, v48\n" \
  "v_mov_b32 v48, v48\n" \
  "v_mov_b32 v48, v48\n" \
  "v_mov_b32 v48, v48\n" \
  "v_mov_b32 v48, v48\n" \
  "v_mov_b32 v48, v48\n" \
  "v_mov_b32 v48, v48\n" \
  "v_mov_b32 v48, v48\n" \
  "v_mov_b32 v48, v48\n" \
  "v_mov_b32 v48, v48\n" \
  "v_mov_b32 v48, v48\n" \
  "v_mov_b32 v48, v48\n" \
  "v_mov_b32 v48, v48\n" \
  "v_mov_b32 v48, v48\n" \
  "v_mov_b32 v48, v48\n" \
  "v_mov_b32 v48, v48\n" \
  "v_mov_b32 v48, v48\n" \
  "YPCE_%=:\n" \
  "buffer_load_dwordx4 v[0:3], v49, s[8:11], s37 offen sc1\n" \
  "v_mov_b32 v32, 0\n" \
  "v_mov_b32 v33, 0\n" \
  "v_mov_b32 v34, 0\n" \
  "s_nop 7\n" \
  "s_nop 1\n" \
  "buffer_load_dwordx4 v[4:7], v49, s[8:11], s37 offen offset:1024 sc1\n" \
  "v_mov_b32 v35, 0\n" \
  "v_mov_b32 v36, 0\n" \
  "v_mov_b32 v37, 0\n" \
  "s_nop 7\n" \
  "s_nop 1\n" \
  "buffer_load_dwordx4 v[8:11], v49, s[8:11], s37 offen offset:2048 sc1\n" \
  "v_mov_b32 v38, 0\n" \
  "v_mov_b32 v39, 0\n" \
  "v_mov_b32 v40, 0\n" \
  "s_nop 7\n" \
  "s_nop 1\n" \
  "buffer_load_dwordx4 v[12:15], v49, s[8:11], s37 offen offset:3072 sc1\n" \
  "v_mov_b32 v41, 0\n" \
  "v_mov_b32 v42, 0\n" \
  "v_mov_b32 v43, 0\n" \
  "s_nop 7\n" \
  "s_nop 1\n" \
  "buffer_load_dwordx4 v[16:19], v49, s[8:11], s40 offen sc1\n" \
  "v_mov_b32 v44, 0\n" \
  "v_mov_b32 v45, 0\n" \
  "v_mov_b32 v46, 0\n" \
  "s_nop 7\n" \
  "s_nop 1\n" \
  "buffer_load_dwordx4 v[20:23], v49, s[8:11], s40 offen offset:1024 sc1\n" \
  "v_mov_b32 v47, 0\n" \
  "s_nop 7\n" \
  "s_nop 3\n" \
  "buffer_load_dwordx4 v[24:27], v49, s[8:11], s40 offen offset:2048 sc1\n" \
  "s_nop 7\n" \
  "s_nop 4\n" \
  "buffer_load_dwordx4 v[28:31], v49, s[8:11], s40 offen offset:3072 sc1\n" \
  "YC_%=:\n" \
  "s_waitcnt vmcnt(7)\n" \
  "v_max_u32 v54, v0, v1\n" \
  "v_max3_u32 v54, v54, v2, v3\n" \
  "v_cmp_lt_u32 vcc, s43, v54\n" \
  "s_cbranch_vccnz YF0_%=\n" \
  "v_smfmac_f32_16x16x64_bf16 v[32:35], v[0:3], a[0:7], v48\n" \
  "v_smfmac_f32_16x16x64_bf16 v[36:39], v[0:3], a[16:23], v48\n" \
  "s_waitcnt vmcnt(6)\n" \
  "v_max3_u32 v54, v54, v4, v5\n" \
  "v_smfmac_f32_16x16x64_bf16 v[40:43], v[0:3], a[8:15], v48\n" \
  "v_max3_u32 v54, v54, v6, v7\n" \
  "v_smfmac_f32_16x16x64_bf16 v[44:47], v[0:3], a[24:31], v48\n" \
  "v_cmp_lt_u32 vcc, s43, v54\n" \
  "s_cbranch_vccnz YF1_%=\n" \
  "v_smfmac_f32_16x16x64_bf16 v[32:35], v[4:7], a[32:39], v48\n" \
  "v_smfmac_f32_16x16x64_bf16 v[36:39], v[4:7], a[48:55], v48\n" \
  "v_smfmac_f32_16x16x64_bf16 v[40:43], v[4:7], a[40:47], v48\n" \
  "v_smfmac_f32_16x16x64_bf16 v[44:47], v[4:7], a[56:63], v48\n" \
  "s_waitcnt vmcnt(5)\n" \
  "v_smfmac_f32_16x16x64_bf16 v[32:35], v[8:11], a[64:71], v48\n" \
  "v_max3_u32 v54, v54, v8, v9\n" \
  "v_smfmac_f32_16x16x64_bf16 v[36:39], v[8:11], a[80:87], v48\n" \
  "v_max3_u32 v54, v54, v10, v11\n" \
  "v_smfmac_f32_16x16x64_bf16 v[40:43], v[8:11], a[72:79], v48\n" \
  "v_smfmac_f32_16x16x64_bf16 v[44:47], v[8:11], a[88:95], v48\n" \
  "s_waitcnt vmcnt(4)\n" \
  "v_smfmac_f32_16x16x64_bf16 v[32:35], v[12:15], a[96:103], v48\n" \
  "v_max3_u32 v54, v54, v12, v13\n" \
  "v_smfmac_f32_16x16x64_bf16 v[36:39], v[12:15], a[112:119], v48\n" \
  "v_max3_u32 v54, v54, v14, v15\n" \
  "v_smfmac_f32_16x16x64_bf16 v[40:43], v[12:15], a[104:111], v48\n" \
  "v_smfmac_f32_16x16x64_bf16 v[44:47], v[12:15], a[120:127], v48\n" \
  "s_waitcnt vmcnt(3)\n" \
  "v_smfmac_f32_16x16x64_bf16 v[32:35], v[16:19], a[128:135], v48\n" \
  "v_max3_u32 v54, v54, v16, v17\n" \
  "v_smfmac_f32_16x16x64_bf16 v[36:39], v[16:19], a[144:151], v48\n" \
  "v_max3_u32 v54, v54, v18, v19\n" \
  "v_smfmac_f32_16x16x64_bf16 v[40:43], v[16:19], a[136:143], v48\n" \
  "v_smfmac_f32_16x16x64_bf16 v[44:47], v[16:19], a[152:159], v48\n" \
  "s_waitcnt vmcnt(2)\n" \
  "v_smfmac_f32_16x16x64_bf16 v[32:35], v[20:23], a[160:167], v48\n" \
  "v_max3_u32 v54, v54, v20, v21\n" \
  "v_smfmac_f32_16x16x64_bf16 v[36:39], v[20:23], a[176:183], v48\n" \
  "v_max3_u32 v54, v54, v22, v23\n" \
  "v_smfmac_f32_16x16x64_bf16 v[40:43], v[20:23], a[168:175], v48\n" \
  "v_smfmac_f32_16x16x64_bf16 v[44:47], v[20:23], a[184:191], v48\n" \
  "s_waitcnt vmcnt(1)\n" \
  "v_smfmac_f32_16x16x64_bf16 v[32:35], v[24:27], a[192:199], v48\n" \
  "v_max3_u32 v54, v54, v24, v25\n" \
  "v_smfmac_f32_16x16x64_bf16 v[36:39], v[24:27], a[208:215], v48\n" \
  "v_max3_u32 v54, v54, v26, v27\n" \
  "s_waitcnt vmcnt(0)\n" \
  "v_smfmac_f32_16x16x64_bf16 v[32:35], v[28:31], a[224:231], v48\n" \
  "v_max3_u32 v54, v54, v28, v29\n" \
  "v_smfmac_f32_16x16x64_bf16 v[36:39], v[28:31], a[240:247], v48\n" \
  "v_max3_u32 v54, v54, v30, v31\n" \
  "v_smfmac_f32_16x16x64_bf16 v[40:43], v[24:27], a[200:207], v48\n" \
  "v_cmp_lt_u32 vcc, s43, v54\n" \
  "v_smfmac_f32_16x16x64_bf16 v[44:47], v[24:27], a[216:223], v48\n" \
  "v_add_f32 v32, v32, v33\n" \
  "v_smfmac_f32_16x16x64_bf16 v[40:43], v[28:31], a[232:239], v48\n" \
  "v_add_f32 v38, v38, v39\n" \
  "v_smfmac_f32_16x16x64_bf16 v[44:47], v[28:31], a[248:255], v48\n" \
  "v_add_f32 v32, v32, v38\n" \
  "ds_write_b32 v51, v32\n" \
  "s_nop 1\n" \
  "v_add_f32 v40, v40, v41\n" \
  "s_nop 2\n" \
  "v_add_f32 v46, v46, v47\n" \
  "v_add_f32 v40, v40, v46\n" \
  "ds_write_b32 v51, v40 offset:256\n" \
  "s_cmp_lg_u32 s41, 0\n" \
  "s_cbranch_vccnz YM_%=\n" \
  "s_cbranch_scc1 YO_%=\n" \
  "v_mov_b32 v55, -1\n" \
  "s_waitcnt lgkmcnt(0)\n" \
  "s_barrier\n" \
  "ds_read2_b64 v[32:35], v52 offset0:0 offset1:64\n" \
  "ds_read2_b64 v[36:39], v52 offset0:128 offset1:192\n" \
  "s_waitcnt lgkmcnt(1)\n" \
  "v_pk_add_f32 v[32:33], v[32:33], v[34:35]\n" \
  "s_waitcnt lgkmcnt(0)\n" \
  "v_pk_add_f32 v[36:37], v[36:37], v[38:39]\n" \
  "v_pk_add_f32 v[32:33], v[32:33], v[36:37]\n" \
  "v_cvt_pk_bf16_f32 v32, v32, v33\n" \
  "v_min_u32 v32, -2, v32\n" \
  "buffer_store_dword v32, v50, s[8:11], s38 offen sc1\n" \
  "buffer_store_dword v55, v50, s[8:11], s37 offen sc1\n" \
  "YN_%=:\n" \
  "s_mov_b32 s49, s39\n" \
  "s_mov_b32 s39, s37\n" \
  "s_mov_b32 s37, s38\n" \
  "s_mov_b32 s38, s49\n" \
  "s_add_u32 s40, s37, s54\n" \
  "s_add_u32 s36, s36, 1\n" \
  "s_add_u32 s56, s56, 0x1000000\n" \
  "s_cmp_eq_u32 s36, 63\n" \
  "s_cbranch_scc1 YE_%=\n" \
  "s_branch YL_%=\n" \
  "YO_%=:\n" \
  "s_waitcnt lgkmcnt(0)\n" \
  "s_barrier\n" \
  "s_branch YN_%=\n" \
  "YF1_%=:\n" \
  "buffer_load_dwordx4 v[4:7], v49, s[8:11], s37 offen offset:1024 sc1\n" \
  "s_branch YG2_%=\n" \
  "YF0_%=:\n" \
  "buffer_load_dwordx4 v[0:3], v49, s[8:11], s37 offen sc1\n" \
  "s_waitcnt vmcnt(7)\n" \
  "v_max3_u32 v54, v4, v5, v6\n" \
  "v_max_u32 v54, v54, v7\n" \
  "v_cmp_lt_u32 vcc, s43, v54\n" \
  "s_cbranch_vccz YH1_%=\n" \
  "buffer_load_dwordx4 v[4:7], v49, s[8:11], s37 offen offset:1024 sc1\n" \
  "YH1_%=:\n" \
  "YG2_%=:\n" \
  "s_waitcnt vmcnt(6)\n" \
  "v_max3_u32 v54, v8, v9, v10\n" \
  "v_max_u32 v54, v54, v11\n" \
  "v_cmp_lt_u32 vcc, s43, v54\n" \
  "s_cbranch_vccz YH2_%=\n" \
  "buffer_load_dwordx4 v[8:11], v49, s[8:11], s37 offen offset:2048 sc1\n" \
  "YH2_%=:\n" \
  "s_waitcnt vmcnt(5)\n" \
  "v_max3_u32 v54, v12, v13, v14\n" \
  "v_max_u32 v54, v54, v15\n" \
  "v_cmp_lt_u32 vcc, s43, v54\n" \
  "s_cbranch_vccz YH3_%=\n" \
  "buffer_load_dwordx4 v[12:15], v49, s[8:11], s37 offen offset:3072 sc1\n" \
  "YH3_%=:\n" \
  "s_waitcnt vmcnt(4)\n" \
  "v_max3_u32 v54, v16, v17, v18\n" \
  "v_max_u32 v54, v54, v19\n" \
  "v_cmp_lt_u32 vcc, s43, v54\n" \
  "s_cbranch_vccz YH4_%=\n" \
  "buffer_load_dwordx4 v[16:19], v49, s[8:11], s40 offen sc1\n" \
  "YH4_%=:\n" \
  "s_waitcnt vmcnt(3)\n" \
  "v_max3_u32 v54, v20, v21, v22\n" \
  "v_max_u32 v54, v54, v23\n" \
  "v_cmp_lt_u32 vcc, s43, v54\n" \
  "s_cbranch_vccz YH5_%=\n" \
  "buffer_load_dwordx4 v[20:23], v49, s[8:11], s40 offen offset:1024 sc1\n" \
  "YH5_%=:\n" \
  "s_waitcnt vmcnt(2)\n" \
  "v_max3_u32 v54, v24, v25, v26\n" \
  "v_max_u32 v54, v54, v27\n" \
  "v_cmp_lt_u32 vcc, s43, v54\n" \
  "s_cbranch_vccz YH6_%=\n" \
  "buffer_load_dwordx4 v[24:27], v49, s[8:11], s40 offen offset:2048 sc1\n" \
  "YH6_%=:\n" \
  "s_waitcnt vmcnt(1)\n" \
  "v_max3_u32 v54, v28, v29, v30\n" \
  "v_max_u32 v54, v54, v31\n" \
  "v_cmp_lt_u32 vcc, s43, v54\n" \
  "s_cbranch_vccz YH7_%=\n" \
  "buffer_load_dwordx4 v[28:31], v49, s[8:11], s40 offen offset:3072 sc1\n" \
  "YH7_%=:\n" \
  "s_sub_u32 s44, s44, 1\n" \
  "s_cselect_b32 s43, -1, s43\n" \
  "s_max_i32 s44, s44, 0\n" \
  "s_cmp_lg_u32 s36, 0\n" \
  "s_cselect_b32 s49, 0, 0\n" \
  "s_add_u32 s35, s35, s49\n" \
  "s_min_i32 s35, s35, s45\n" \
  "s_branch YZ_%=\n" \
  "YM_%=:\n" \
  "s_sub_u32 s44, s44, 1\n" \
  "s_cselect_b32 s43, -1, s43\n" \
  "s_max_i32 s44, s44, 0\n" \
  "s_cmp_lg_u32 s36, 0\n" \
  "s_cselect_b32 s49, 0, 0\n" \
  "s_add_u32 s35, s35, s49\n" \
  "s_min_i32 s35, s35, s45\n" \
  "v_max3_u32 v54, v0, v1, v2\n" \
  "v_max_u32 v54, v54, v3\n" \
  "v_cmp_lt_u32 vcc, s43, v54\n" \
  "s_cbranch_vccz YW0_%=\n" \
  "buffer_load_dwordx4 v[0:3], v49, s[8:11], s37 offen sc1\n" \
  "YW0_%=:\n" \
  "v_max3_u32 v54, v4, v5, v6\n" \
  "v_max_u32 v54, v54, v7\n" \
  "v_cmp_lt_u32 vcc, s43, v54\n" \
  "s_cbranch_vccz YW1_%=\n" \
  "buffer_load_dwordx4 v[4:7], v49, s[8:11], s37 offen offset:1024 sc1\n" \
  "YW1_%=:\n" \
  "v_max3_u32 v54, v8, v9, v10\n" \
  "v_max_u32 v54, v54, v11\n" \
  "v_cmp_lt_u32 vcc, s43, v54\n" \
  "s_cbranch_vccz YW2_%=\n" \
  "buffer_load_dwordx4 v[8:11], v49, s[8:11], s37 offen offset:2048 sc1\n" \
  "YW2_%=:\n" \
  "v_max3_u32 v54, v12, v13, v14\n" \
  "v_max_u32 v54, v54, v15\n" \
  "v_cmp_lt_u32 vcc, s43, v54\n" \
  "s_cbranch_vccz YW3_%=\n" \
  "buffer_load_dwordx4 v[12:15], v49, s[8:11], s37 offen offset:3072 sc1\n" \
  "YW3_%=:\n" \
  "v_max3_u32 v54, v16, v17, v18\n" \
  "v_max_u32 v54, v54, v19\n" \
  "v_cmp_lt_u32 vcc, s43, v54\n" \
  "s_cbranch_vccz YW4_%=\n" \
  "buffer_load_dwordx4 v[16:19], v49, s[8:11], s40 offen sc1\n" \
  "YW4_%=:\n" \
  "v_max3_u32 v54, v20, v21, v22\n" \
  "v_max_u32 v54, v54, v23\n" \
  "v_cmp_lt_u32 vcc, s43, v54\n" \
  "s_cbranch_vccz YW5_%=\n" \
  "buffer_load_dwordx4 v[20:23], v49, s[8:11], s40 offen offset:1024 sc1\n" \
  "YW5_%=:\n" \
  "v_max3_u32 v54, v24, v25, v26\n" \
  "v_max_u32 v54, v54, v27\n" \
  "v_cmp_lt_u32 vcc, s43, v54\n" \
  "s_cbranch_vccz YW6_%=\n" \
  "buffer_load_dwordx4 v[24:27], v49, s[8:11], s40 offen offset:2048 sc1\n" \
  "YW6_%=:\n" \
  "v_max3_u32 v54, v28, v29, v30\n" \
  "v_max_u32 v54, v54, v31\n" \
  "v_cmp_lt_u32 vcc, s43, v54\n" \
  "s_cbranch_vccz YW7_%=\n" \
  "buffer_load_dwordx4 v[28:31], v49, s[8:11], s40 offen offset:3072 sc1\n" \
  "YW7_%=:\n" \
  "YZ_%=:\n" \
  "v_mov_b32 v32, 0\n" \
  "v_mov_b32 v33, 0\n" \
  "v_mov_b32 v34, 0\n" \
  "v_mov_b32 v35, 0\n" \
  "v_mov_b32 v36, 0\n" \
  "v_mov_b32 v37, 0\n" \
  "v_mov_b32 v38, 0\n" \
  "v_mov_b32 v39, 0\n" \
  "v_mov_b32 v40, 0\n" \
  "v_mov_b32 v41, 0\n" \
  "v_mov_b32 v42, 0\n" \
  "v_mov_b32 v43, 0\n" \
  "v_mov_b32 v44, 0\n" \
  "v_mov_b32 v45, 0\n" \
  "v_mov_b32 v46, 0\n" \
  "v_mov_b32 v47, 0\n" \
  "s_waitcnt vmcnt(0)\n" \
  "s_branch YC_%=\n" \
  "YE_%=:\n" \
  "s_waitcnt vmcnt(0)\n"

#define ASM_GK \
  "v_mbcnt_lo_u32_b32 v132, -1, 0\n" \
  "v_lshlrev_b32 v132, 2, v132\n" \
  "s_add_u32 s37, s24, s30\n" \
  "s_add_u32 m0, s37, 0\n" \
  "s_nop 0\n" \
  "buffer_load_dwordx4 v24, s[12:15], s32 offen lds\n" \
  "buffer_load_dwordx4 v25, s[12:15], s32 offen offset:1024 lds\n" \
  "buffer_load_dwordx4 v26, s[12:15], s32 offen offset:2048 lds\n" \
  "buffer_load_dwordx4 v27, s[12:15], s32 offen offset:3072 lds\n" \
  "s_add_u32 s37, s24, s30\n" \
  "s_add_u32 m0, s37, 4096\n" \
  "s_nop 0\n" \
  "buffer_load_dwordx4 v28, s[12:15], s32 offen lds\n" \
  "buffer_load_dwordx4 v29, s[12:15], s32 offen offset:1024 lds\n" \
  "buffer_load_dwordx4 v30, s[12:15], s32 offen offset:2048 lds\n" \
  "buffer_load_dwordx4 v31, s[12:15], s32 offen offset:3072 lds\n" \
  "s_add_u32 s37, s24, s30\n" \
  "s_add_u32 m0, s37, 8192\n" \
  "s_nop 0\n" \
  "buffer_load_dwordx4 v32, s[12:15], s32 offen lds\n" \
  "buffer_load_dwordx4 v33, s[12:15], s32 offen offset:1024 lds\n" \
  "s_mov_b32 s31, 40960\n" \
  "s_waitcnt vmcnt(38)\n" \
  "ds_read_b64_tr_b16 a[0:1], v47\n" \
  "ds_read_b64_tr_b16 a[2:3], v47 offset:512\n" \
  "ds_read_b64_tr_b16 a[4:5], v47 offset:64\n" \
  "ds_read_b64_tr_b16 a[6:7], v47 offset:576\n" \
  "s_waitcnt vmcnt(36)\n" \
  "ds_read_b64_tr_b16 a[8:9], v47 offset:2048\n" \
  "ds_read_b64_tr_b16 a[10:11], v47 offset:2560\n" \
  "ds_read_b64_tr_b16 a[12:13], v47 offset:2112\n" \
  "ds_read_b64_tr_b16 a[14:15], v47 offset:2624\n" \
  "s_waitcnt lgkmcnt(4)\n" \
  "s_add_u32 m0, s25, 0\n" \
  "s_nop 0\n" \
  "buffer_load_dwordx4 v45, s[16:19], s26 offen lds\n" \
  "buffer_load_dwordx4 v46, s[20:23], s26 offen offset:1024 lds\n" \
  "s_add_u32 s26, s26, s27\n" \
  "s_waitcnt vmcnt(36)\n" \
  "ds_read_b64_tr_b16 a[16:17], v47 offset:4096\n" \
  "ds_read_b64_tr_b16 a[18:19], v47 offset:4608\n" \
  "ds_read_b64_tr_b16 a[20:21], v47 offset:4160\n" \
  "ds_read_b64_tr_b16 a[22:23], v47 offset:4672\n" \
  "s_waitcnt lgkmcnt(4)\n" \
  "s_add_u32 m0, s25, 2048\n" \
  "s_nop 0\n" \
  "buffer_load_dwordx4 v45, s[16:19], s26 offen lds\n" \
  "buffer_load_dwordx4 v46, s[20:23], s26 offen offset:1024 lds\n" \
  "s_add_u32 s26, s26, s27\n" \
  "s_waitcnt vmcnt(36)\n" \
  "ds_read_b64_tr_b16 a[24:25], v47 offset:6144\n" \
  "ds_read_b64_tr_b16 a[26:27], v47 offset:6656\n" \
  "ds_read_b64_tr_b16 a[28:29], v47 offset:6208\n" \
  "ds_read_b64_tr_b16 a[30:31], v47 offset:6720\n" \
  "s_waitcnt lgkmcnt(4)\n" \
  "s_add_u32 m0, s25, 4096\n" \
  "s_nop 0\n" \
  "buffer_load_dwordx4 v45, s[16:19], s26 offen lds\n" \
  "buffer_load_dwordx4 v46, s[20:23], s26 offen offset:1024 lds\n" \
  "s_add_u32 s26, s26, s27\n" \
  "s_waitcnt vmcnt(36)\n" \
  "ds_read_b64_tr_b16 a[32:33], v47 offset:8192\n" \
  "ds_read_b64_tr_b16 a[34:35], v47 offset:8704\n" \
  "ds_read_b64_tr_b16 a[36:37], v47 offset:8256\n" \
  "ds_read_b64_tr_b16 a[38:39], v47 offset:8768\n" \
  "s_waitcnt lgkmcnt(4)\n" \
  "s_add_u32 m0, s25, 6144\n" \
  "s_nop 0\n" \
  "buffer_load_dwordx4 v45, s[16:19], s26 offen lds\n" \
  "buffer_load_dwordx4 v46, s[20:23], s26 offen offset:1024 lds\n" \
  "s_add_u32 s26, s26, s27\n" \
  "s_waitcnt vmcnt(36)\n" \
  "ds_read_b64_tr_b16 a[40:41], v47 offset:10240\n" \
  "ds_read_b64_tr_b16 a[42:43], v47 offset:10752\n" \
  "ds_read_b64_tr_b16 a[44:45], v47 offset:10304\n" \
  "ds_read_b64_tr_b16 a[46:47], v47 offset:10816\n" \
  "s_waitcnt lgkmcnt(4)\n" \
  "s_add_u32 m0, s25, 8192\n" \
  "s_nop 0\n" \
  "buffer_load_dwordx4 v45, s[16:19], s26 offen lds\n" \
  "buffer_load_dwordx4 v46, s[20:23], s26 offen offset:1024 lds\n" \
  "s_add_u32 s26, s26, s27\n" \
  "s_waitcnt vmcnt(36)\n" \
  "ds_read_b64_tr_b16 a[48:49], v47 offset:12288\n" \
  "ds_read_b64_tr_b16 a[50:51], v47 offset:12800\n" \
  "ds_read_b64_tr_b16 a[52:53], v47 offset:12352\n" \
  "ds_read_b64_tr_b16 a[54:55], v47 offset:12864\n" \
  "s_waitcnt lgkmcnt(4)\n" \
  "s_add_u32 m0, s25, 10240\n" \
  "s_nop 0\n" \
  "buffer_load_dwordx4 v45, s[16:19], s26 offen lds\n" \
  "buffer_load_dwordx4 v46, s[20:23], s26 offen offset:1024 lds\n" \
  "s_add_u32 s26, s26, s27\n" \
  "s_waitcnt vmcnt(36)\n" \
  "ds_read_b64_tr_b16 a[56:57], v47 offset:14336\n" \
  "ds_read_b64_tr_b16 a[58:59], v47 offset:14848\n" \
  "ds_read_b64_tr_b16 a[60:61], v47 offset:14400\n" \
  "ds_read_b64_tr_b16 a[62:63], v47 offset:14912\n" \
  "s_waitcnt lgkmcnt(4)\n" \
  "s_add_u32 m0, s25, 12288\n" \
  "s_nop 0\n" \
  "buffer_load_dwordx4 v45, s[16:19], s26 offen lds\n" \
  "buffer_load_dwordx4 v46, s[20:23], s26 offen offset:1024 lds\n" \
  "s_add_u32 s26, s26, s27\n" \
  "s_waitcnt vmcnt(36)\n" \
  "ds_read_b64_tr_b16 a[64:65], v47 offset:16384\n" \
  "ds_read_b64_tr_b16 a[66:67], v47 offset:16896\n" \
  "ds_read_b64_tr_b16 a[68:69], v47 offset:16448\n" \
  "ds_read_b64_tr_b16 a[70:71], v47 offset:16960\n" \
  "s_waitcnt lgkmcnt(4)\n" \
  "s_add_u32 m0, s25, 14336\n" \
  "s_nop 0\n" \
  "buffer_load_dwordx4 v45, s[16:19], s26 offen lds\n" \
  "buffer_load_dwordx4 v46, s[20:23], s26 offen offset:1024 lds\n" \
  "s_add_u32 s26, s26, s27\n" \
  "s_waitcnt vmcnt(36)\n" \
  "ds_read_b64_tr_b16 a[72:73], v47 offset:18432\n" \
  "ds_read_b64_tr_b16 a[74:75], v47 offset:18944\n" \
  "ds_read_b64_tr_b16 a[76:77], v47 offset:18496\n" \
  "ds_read_b64_tr_b16 a[78:79], v47 offset:19008\n" \
  "s_waitcnt lgkmcnt(4)\n" \
  "s_add_u32 m0, s25, 16384\n" \
  "s_nop 0\n" \
  "buffer_load_dwordx4 v45, s[16:19], s26 offen lds\n" \
  "buffer_load_dwordx4 v46, s[20:23], s26 offen offset:1024 lds\n" \
  "s_add_u32 s26, s26, s27\n" \
  "s_waitcnt vmcnt(36)\n" \
  "ds_read_b64_tr_b16 a[80:81], v47 offset:20480\n" \
  "ds_read_b64_tr_b16 a[82:83], v47 offset:20992\n" \
  "ds_read_b64_tr_b16 a[84:85], v47 offset:20544\n" \
  "ds_read_b64_tr_b16 a[86:87], v47 offset:21056\n" \
  "s_waitcnt lgkmcnt(4)\n" \
  "s_add_u32 m0, s25, 18432\n" \
  "s_nop 0\n" \
  "buffer_load_dwordx4 v45, s[16:19], s26 offen lds\n" \
  "buffer_load_dwordx4 v46, s[20:23], s26 offen offset:1024 lds\n" \
  "s_add_u32 s26, s26, s27\n" \
  "s_waitcnt vmcnt(36)\n" \
  "ds_read_b64_tr_b16 a[88:89], v47 offset:22528\n" \
  "ds_read_b64_tr_b16 a[90:91], v47 offset:23040\n" \
  "ds_read_b64_tr_b16 a[92:93], v47 offset:22592\n" \
  "ds_read_b64_tr_b16 a[94:95], v47 offset:23104\n" \
  "s_waitcnt lgkmcnt(4)\n" \
  "s_add_u32 m0, s25, 20480\n" \
  "s_nop 0\n" \
  "buffer_load_dwordx4 v45, s[16:19], s26 offen lds\n" \
  "buffer_load_dwordx4 v46, s[20:23], s26 offen offset:1024 lds\n" \
  "s_add_u32 s26, s26, s27\n" \
  "s_waitcnt vmcnt(36)\n" \
  "ds_read_b64_tr_b16 a[96:97], v47 offset:24576\n" \
  "ds_read_b64_tr_b16 a[98:99], v47 offset:25088\n" \
  "ds_read_b64_tr_b16 a[100:101], v47 offset:24640\n" \
  "ds_read_b64_tr_b16 a[102:103], v47 offset:25152\n" \
  "s_waitcnt lgkmcnt(4)\n" \
  "s_add_u32 m0, s25, 22528\n" \
  "s_nop 0\n" \
  "buffer_load_dwordx4 v45, s[16:19], s26 offen lds\n" \
  "buffer_load_dwordx4 v46, s[20:23], s26 offen offset:1024 lds\n" \
  "s_add_u32 s26, s26, s27\n" \
  "s_waitcnt vmcnt(36)\n" \
  "ds_read_b64_tr_b16 a[104:105], v47 offset:26624\n" \
  "ds_read_b64_tr_b16 a[106:107], v47 offset:27136\n" \
  "ds_read_b64_tr_b16 a[108:109], v47 offset:26688\n" \
  "ds_read_b64_tr_b16 a[110:111], v47 offset:27200\n" \
  "s_waitcnt lgkmcnt(4)\n" \
  "s_add_u32 m0, s25, 24576\n" \
  "s_nop 0\n" \
  "buffer_load_dwordx4 v45, s[16:19], s26 offen lds\n" \
  "buffer_load_dwordx4 v46, s[20:23], s26 offen offset:1024 lds\n" \
  "s_add_u32 s26, s26, s27\n" \
  "s_waitcnt vmcnt(36)\n" \
  "ds_read_b64_tr_b16 a[112:113], v47 offset:28672\n" \
  "ds_read_b64_tr_b16 a[114:115], v47 offset:29184\n" \
  "ds_read_b64_tr_b16 a[116:117], v47 offset:28736\n" \
  "ds_read_b64_tr_b16 a[118:119], v47 offset:29248\n" \
  "s_waitcnt lgkmcnt(4)\n" \
  "s_add_u32 m0, s25, 26624\n" \
  "s_nop 0\n" \
  "buffer_load_dwordx4 v45, s[16:19], s26 offen lds\n" \
  "buffer_load_dwordx4 v46, s[20:23], s26 offen offset:1024 lds\n" \
  "s_add_u32 s26, s26, s27\n" \
  "s_waitcnt vmcnt(26)\n" \
  "ds_read_b64_tr_b16 a[120:121], v47\n" \
  "ds_read_b64_tr_b16 a[122:123], v47 offset:512\n" \
  "ds_read_b64_tr_b16 a[124:125], v47 offset:64\n" \
  "ds_read_b64_tr_b16 a[126:127], v47 offset:576\n" \
  "s_waitcnt lgkmcnt(4)\n" \
  "s_add_u32 m0, s25, 28672\n" \
  "s_nop 0\n" \
  "buffer_load_dwordx4 v45, s[16:19], s26 offen lds\n" \
  "buffer_load_dwordx4 v46, s[20:23], s26 offen offset:1024 lds\n" \
  "s_add_u32 s26, s26, s27\n" \
  "s_barrier\n" \
  "ds_read_b128 v[48:51], v34\n" \
  "ds_read_b128 v[52:55], v35\n" \
  "ds_read_b128 v[56:59], v36\n" \
  "ds_read_b128 v[60:63], v37\n" \
  "ds_read_b128 v[64:67], v38\n" \
  "s_waitcnt lgkmcnt(4)\n" \
  "v_mfma_f32_32x32x16_bf16 v[68:83], a[0:3], v[48:51], 0\n" \
  "v_mfma_f32_32x32x16_bf16 v[84:99], a[4:7], v[48:51], 0\n" \
  "ds_read_b128 v[48:51], v39\n" \
  "s_waitcnt lgkmcnt(4)\n" \
  "v_mfma_f32_32x32x16_bf16 v[68:83], a[8:11], v[52:55], v[68:83]\n" \
  "v_mfma_f32_32x32x16_bf16 v[84:99], a[12:15], v[52:55], v[84:99]\n" \
  "s_waitcnt vmcnt(26)\n" \
  "ds_read_b64_tr_b16 a[128:129], v47 offset:2048\n" \
  "ds_read_b64_tr_b16 a[130:131], v47 offset:2560\n" \
  "ds_read_b64_tr_b16 a[132:133], v47 offset:2112\n" \
  "ds_read_b64_tr_b16 a[134:135], v47 offset:2624\n" \
  "s_add_u32 m0, s25, 0\n" \
  "s_nop 0\n" \
  "buffer_load_dwordx4 v45, s[16:19], s26 offen lds\n" \
  "buffer_load_dwordx4 v46, s[20:23], s26 offen offset:1024 lds\n" \
  "s_add_u32 s26, s26, s27\n" \
  "ds_read_b128 v[52:55], v40\n" \
  "s_waitcnt lgkmcnt(8)\n" \
  "v_mfma_f32_32x32x16_bf16 v[68:83], a[16:19], v[56:59], v[68:83]\n" \
  "v_mfma_f32_32x32x16_bf16 v[84:99], a[20:23], v[56:59], v[84:99]\n" \
  "ds_read_b128 v[56:59], v41\n" \
  "s_waitcnt lgkmcnt(8)\n" \
  "v_mfma_f32_32x32x16_bf16 v[68:83], a[24:27], v[60:63], v[68:83]\n" \
  "v_mfma_f32_32x32x16_bf16 v[84:99], a[28:31], v[60:63], v[84:99]\n" \
  "s_waitcnt vmcnt(26)\n" \
  "ds_read_b64_tr_b16 a[136:137], v47 offset:4096\n" \
  "ds_read_b64_tr_b16 a[138:139], v47 offset:4608\n" \
  "ds_read_b64_tr_b16 a[140:141], v47 offset:4160\n" \
  "ds_read_b64_tr_b16 a[142:143], v47 offset:4672\n" \
  "s_waitcnt lgkmcnt(6)\n" \
  "s_add_u32 m0, s25, 2048\n" \
  "s_nop 0\n" \
  "buffer_load_dwordx4 v45, s[16:19], s26 offen lds\n" \
  "buffer_load_dwordx4 v46, s[20:23], s26 offen offset:1024 lds\n" \
  "s_add_u32 s26, s26, s27\n" \
  "ds_read_b128 v[60:63], v34 offset:256\n" \
  "v_mfma_f32_32x32x16_bf16 v[68:83], a[32:35], v[64:67], v[68:83]\n" \
  "v_mfma_f32_32x32x16_bf16 v[84:99], a[36:39], v[64:67], v[84:99]\n" \
  "ds_read_b128 v[64:67], v35 offset:256\n" \
  "v_mfma_f32_32x32x16_bf16 v[68:83], a[40:43], v[48:51], v[68:83]\n" \
  "v_mfma_f32_32x32x16_bf16 v[84:99], a[44:47], v[48:51], v[84:99]\n" \
  "s_waitcnt vmcnt(26)\n" \
  "ds_read_b64_tr_b16 a[144:145], v47 offset:6144\n" \
  "ds_read_b64_tr_b16 a[146:147], v47 offset:6656\n" \
  "ds_read_b64_tr_b16 a[148:149], v47 offset:6208\n" \
  "ds_read_b64_tr_b16 a[150:151], v47 offset:6720\n" \
  "s_waitcnt lgkmcnt(6)\n" \
  "s_add_u32 m0, s25, 4096\n" \
  "s_nop 0\n" \
  "buffer_load_dwordx4 v45, s[16:19], s26 offen lds\n" \
  "buffer_load_dwordx4 v46, s[20:23], s26 offen offset:1024 lds\n" \
  "s_add_u32 s26, s26, s27\n" \
  "ds_read_b128 v[48:51], v36 offset:256\n" \
  "v_mfma_f32_32x32x16_bf16 v[68:83], a[48:51], v[52:55], v[68:83]\n" \
  "v_mfma_f32_32x32x16_bf16 v[84:99], a[52:55], v[52:55], v[84:99]\n" \
  "ds_read_b128 v[52:55], v37 offset:256\n" \
  "v_mfma_f32_32x32x16_bf16 v[68:83], a[56:59], v[56:59], v[68:83]\n" \
  "v_mfma_f32_32x32x16_bf16 v[84:99], a[60:63], v[56:59], v[84:99]\n" \
  "s_waitcnt vmcnt(26)\n" \
  "ds_read_b64_tr_b16 a[152:153], v47 offset:8192\n" \
  "ds_read_b64_tr_b16 a[154:155], v47 offset:8704\n" \
  "ds_read_b64_tr_b16 a[156:157], v47 offset:8256\n" \
  "ds_read_b64_tr_b16 a[158:159], v47 offset:8768\n" \
  "s_waitcnt lgkmcnt(6)\n" \
  "s_add_u32 m0, s25, 6144\n" \
  "s_nop 0\n" \
  "buffer_load_dwordx4 v45, s[16:19], s26 offen lds\n" \
  "buffer_load_dwordx4 v46, s[20:23], s26 offen offset:1024 lds\n" \
  "s_add_u32 s26, s26, s27\n" \
  "ds_read_b128 v[56:59], v38 offset:256\n" \
  "v_mfma_f32_32x32x16_bf16 v[68:83], a[64:67], v[60:63], v[68:83]\n" \
  "v_mfma_f32_32x32x16_bf16 v[84:99], a[68:71], v[60:63], v[84:99]\n" \
  "ds_read_b128 v[60:63], v39 offset:256\n" \
  "v_mfma_f32_32x32x16_bf16 v[68:83], a[72:75], v[64:67], v[68:83]\n" \
  "v_mfma_f32_32x32x16_bf16 v[84:99], a[76:79], v[64:67], v[84:99]\n" \
  "s_waitcnt vmcnt(26)\n" \
  "ds_read_b64_tr_b16 a[160:161], v47 offset:10240\n" \
  "ds_read_b64_tr_b16 a[162:163], v47 offset:10752\n" \
  "ds_read_b64_tr_b16 a[164:165], v47 offset:10304\n" \
  "ds_read_b64_tr_b16 a[166:167], v47 offset:10816\n" \
  "s_waitcnt lgkmcnt(6)\n" \
  "s_add_u32 m0, s25, 8192\n" \
  "s_nop 0\n" \
  "buffer_load_dwordx4 v45, s[16:19], s26 offen lds\n" \
  "buffer_load_dwordx4 v46, s[20:23], s26 offen offset:1024 lds\n" \
  "s_add_u32 s26, s26, s27\n" \
  "ds_read_b128 v[64:67], v40 offset:256\n" \
  "v_mfma_f32_32x32x16_bf16 v[68:83], a[80:83], v[48:51], v[68:83]\n" \
  "v_mfma_f32_32x32x16_bf16 v[84:99], a[84:87], v[48:51], v[84:99]\n" \
  "ds_read_b128 v[48:51], v41 offset:256\n" \
  "v_mfma_f32_32x32x16_bf16 v[68:83], a[88:91], v[52:55], v[68:83]\n" \
  "v_mfma_f32_32x32x16_bf16 v[84:99], a[92:95], v[52:55], v[84:99]\n" \
  "s_waitcnt vmcnt(26)\n" \
  "ds_read_b64_tr_b16 a[168:169], v47 offset:12288\n" \
  "ds_read_b64_tr_b16 a[170:171], v47 offset:12800\n" \
  "ds_read_b64_tr_b16 a[172:173], v47 offset:12352\n" \
  "ds_read_b64_tr_b16 a[174:175], v47 offset:12864\n" \
  "s_waitcnt lgkmcnt(6)\n" \
  "s_add_u32 m0, s25, 10240\n" \
  "s_nop 0\n" \
  "buffer_load_dwordx4 v45, s[16:19], s26 offen lds\n" \
  "buffer_load_dwordx4 v46, s[20:23], s26 offen offset:1024 lds\n" \
  "s_add_u32 s26, s26, s27\n" \
  "ds_read_b128 v[52:55], v34 offset:512\n" \
  "v_mfma_f32_32x32x16_bf16 v[68:83], a[96:99], v[56:59], v[68:83]\n" \
  "v_mfma_f32_32x32x16_bf16 v[84:99], a[100:103], v[56:59], v[84:99]\n" \
  "ds_read_b128 v[56:59], v35 offset:512\n" \
  "v_mfma_f32_32x32x16_bf16 v[68:83], a[104:107], v[60:63], v[68:83]\n" \
  "v_mfma_f32_32x32x16_bf16 v[84:99], a[108:111], v[60:63], v[84:99]\n" \
  "s_waitcnt vmcnt(26)\n" \
  "ds_read_b64_tr_b16 a[176:177], v47 offset:14336\n" \
  "ds_read_b64_tr_b16 a[178:179], v47 offset:14848\n" \
  "ds_read_b64_tr_b16 a[180:181], v47 offset:14400\n" \
  "ds_read_b64_tr_b16 a[182:183], v47 offset:14912\n" \
  "s_waitcnt lgkmcnt(6)\n" \
  "s_add_u32 m0, s25, 12288\n" \
  "s_nop 0\n" \
  "buffer_load_dwordx4 v45, s[16:19], s26 offen lds\n" \
  "buffer_load_dwordx4 v46, s[20:23], s26 offen offset:1024 lds\n" \
  "s_add_u32 s26, s26, s27\n" \
  "ds_read_b128 v[60:63], v36 offset:512\n" \
  "v_mfma_f32_32x32x16_bf16 v[68:83], a[112:115], v[64:67], v[68:83]\n" \
  "v_mfma_f32_32x32x16_bf16 v[84:99], a[116:119], v[64:67], v[84:99]\n" \
  "ds_read_b128 v[64:67], v37 offset:512\n" \
  "v_mfma_f32_32x32x16_bf16 v[68:83], a[120:123], v[48:51], v[68:83]\n" \
  "v_mfma_f32_32x32x16_bf16 v[84:99], a[124:127], v[48:51], v[84:99]\n" \
  "s_waitcnt vmcnt(26)\n" \
  "ds_read_b64_tr_b16 a[184:185], v47 offset:16384\n" \
  "ds_read_b64_tr_b16 a[186:187], v47 offset:16896\n" \
  "ds_read_b64_tr_b16 a[188:189], v47 offset:16448\n" \
  "ds_read_b64_tr_b16 a[190:191], v47 offset:16960\n" \
  "s_waitcnt lgkmcnt(6)\n" \
  "s_add_u32 m0, s25, 14336\n" \
  "s_nop 0\n" \
  "buffer_load_dwordx4 v45, s[16:19], s26 offen lds\n" \
  "buffer_load_dwordx4 v46, s[20:23], s26 offen offset:1024 lds\n" \
  "s_add_u32 s26, s26, s27\n" \
  "ds_read_b128 v[48:51], v38 offset:512\n" \
  "v_mfma_f32_32x32x16_bf16 v[68:83], a[128:131], v[52:55], v[68:83]\n" \
  "v_mfma_f32_32x32x16_bf16 v[84:99], a[132:135], v[52:55], v[84:99]\n" \
  "ds_read_b128 v[52:55], v39 offset:512\n" \
  "v_mfma_f32_32x32x16_bf16 v[68:83], a[136:139], v[56:59], v[68:83]\n" \
  "v_mfma_f32_32x32x16_bf16 v[84:99], a[140:143], v[56:59], v[84:99]\n" \
  "s_waitcnt vmcnt(26)\n" \
  "ds_read_b64_tr_b16 a[192:193], v47 offset:18432\n" \
  "ds_read_b64_tr_b16 a[194:195], v47 offset:18944\n" \
  "ds_read_b64_tr_b16 a[196:197], v47 offset:18496\n" \
  "ds_read_b64_tr_b16 a[198:199], v47 offset:19008\n" \
  "s_waitcnt lgkmcnt(6)\n" \
  "s_add_u32 m0, s25, 16384\n" \
  "s_nop 0\n" \
  "buffer_load_dwordx4 v45, s[16:19], s26 offen lds\n" \
  "buffer_load_dwordx4 v46, s[20:23], s26 offen offset:1024 lds\n" \
  "s_add_u32 s26, s26, s27\n" \
  "ds_read_b128 v[56:59], v40 offset:512\n" \
  "v_mfma_f32_32x32x16_bf16 v[68:83], a[144:147], v[60:63], v[68:83]\n" \
  "v_mfma_f32_32x32x16_bf16 v[84:99], a[148:151], v[60:63], v[84:99]\n" \
  "ds_read_b128 v[60:63], v41 offset:512\n" \
  "v_mfma_f32_32x32x16_bf16 v[68:83], a[152:155], v[64:67], v[68:83]\n" \
  "v_mfma_f32_32x32x16_bf16 v[84:99], a[156:159], v[64:67], v[84:99]\n" \
  "s_waitcnt vmcnt(26)\n" \
  "ds_read_b64_tr_b16 a[200:201], v47 offset:20480\n" \
  "ds_read_b64_tr_b16 a[202:203], v47 offset:20992\n" \
  "ds_read_b64_tr_b16 a[204:205], v47 offset:20544\n" \
  "ds_read_b64_tr_b16 a[206:207], v47 offset:21056\n" \
  "s_waitcnt lgkmcnt(6)\n" \
  "s_add_u32 m0, s25, 18432\n" \
  "s_nop 0\n" \
  "buffer_load_dwordx4 v45, s[16:19], s26 offen lds\n" \
  "buffer_load_dwordx4 v46, s[20:23], s26 offen offset:1024 lds\n" \
  "s_add_u32 s26, s26, s27\n" \
  "ds_read_b128 v[64:67], v34 offset:768\n" \
  "v_mfma_f32_32x32x16_bf16 v[68:83], a[160:163], v[48:51], v[68:83]\n" \
  "v_mfma_f32_32x32x16_bf16 v[84:99], a[164:167], v[48:51], v[84:99]\n" \
  "ds_read_b128 v[48:51], v35 offset:768\n" \
  "v_mfma_f32_32x32x16_bf16 v[68:83], a[168:171], v[52:55], v[68:83]\n" \
  "v_mfma_f32_32x32x16_bf16 v[84:99], a[172:175], v[52:55], v[84:99]\n" \
  "s_waitcnt vmcnt(26)\n" \
  "ds_read_b64_tr_b16 a[208:209], v47 offset:22528\n" \
  "ds_read_b64_tr_b16 a[210:211], v47 offset:23040\n" \
  "ds_read_b64_tr_b16 a[212:213], v47 offset:22592\n" \
  "ds_read_b64_tr_b16 a[214:215], v47 offset:23104\n" \
  "ds_read_b128 v[52:55], v36 offset:768\n" \
  "s_waitcnt lgkmcnt(12)\n" \
  "v_mfma_f32_32x32x16_bf16 v[68:83], a[176:179], v[56:59], v[68:83]\n" \
  "v_mfma_f32_32x32x16_bf16 v[84:99], a[180:183], v[56:59], v[84:99]\n" \
  "ds_read_b128 v[56:59], v37 offset:768\n" \
  "s_waitcnt lgkmcnt(12)\n" \
  "v_mfma_f32_32x32x16_bf16 v[68:83], a[184:187], v[60:63], v[68:83]\n" \
  "v_mfma_f32_32x32x16_bf16 v[84:99], a[188:191], v[60:63], v[84:99]\n" \
  "s_waitcnt vmcnt(24)\n" \
  "ds_read_b64_tr_b16 a[216:217], v47 offset:24576\n" \
  "ds_read_b64_tr_b16 a[218:219], v47 offset:25088\n" \
  "ds_read_b64_tr_b16 a[220:221], v47 offset:24640\n" \
  "ds_read_b64_tr_b16 a[222:223], v47 offset:25152\n" \
  "ds_read_b128 v[60:63], v38 offset:768\n" \
  "s_waitcnt lgkmcnt(12)\n" \
  "v_mfma_f32_32x32x16_bf16 v[68:83], a[192:195], v[64:67], v[68:83]\n" \
  "v_mfma_f32_32x32x16_bf16 v[84:99], a[196:199], v[64:67], v[84:99]\n" \
  "ds_read_b128 v[64:67], v39 offset:768\n" \
  "s_waitcnt lgkmcnt(12)\n" \
  "v_mfma_f32_32x32x16_bf16 v[68:83], a[200:203], v[48:51], v[68:83]\n" \
  "v_mfma_f32_32x32x16_bf16 v[84:99], a[204:207], v[48:51], v[84:99]\n" \
  "s_waitcnt vmcnt(22)\n" \
  "ds_read_b64_tr_b16 a[224:225], v47 offset:26624\n" \
  "ds_read_b64_tr_b16 a[226:227], v47 offset:27136\n" \
  "ds_read_b64_tr_b16 a[228:229], v47 offset:26688\n" \
  "ds_read_b64_tr_b16 a[230:231], v47 offset:27200\n" \
  "ds_read_b128 v[48:51], v40 offset:768\n" \
  "s_waitcnt lgkmcnt(12)\n" \
  "v_mfma_f32_32x32x16_bf16 v[68:83], a[208:211], v[52:55], v[68:83]\n" \
  "v_mfma_f32_32x32x16_bf16 v[84:99], a[212:215], v[52:55], v[84:99]\n" \
  "ds_read_b128 v[52:55], v41 offset:768\n" \
  "s_waitcnt lgkmcnt(10)\n" \
  "v_mfma_f32_32x32x16_bf16 v[68:83], a[216:219], v[56:59], v[68:83]\n" \
  "s_waitcnt lgkmcnt(8)\n" \
  "v_mfma_f32_32x32x16_bf16 v[84:99], a[220:223], v[56:59], v[84:99]\n" \
  "s_waitcnt vmcnt(20)\n" \
  "ds_read_b64_tr_b16 a[232:233], v47 offset:28672\n" \
  "ds_read_b64_tr_b16 a[234:235], v47 offset:29184\n" \
  "ds_read_b64_tr_b16 a[236:237], v47 offset:28736\n" \
  "ds_read_b64_tr_b16 a[238:239], v47 offset:29248\n" \
  "ds_read_b128 v[56:59], v34 offset:1024\n" \
  "v_add_u32 v34, s31, v34\n" \
  "s_waitcnt lgkmcnt(9)\n" \
  "v_mfma_f32_32x32x16_bf16 v[68:83], a[224:227], v[60:63], v[68:83]\n" \
  "s_waitcnt lgkmcnt(7)\n" \
  "v_mfma_f32_32x32x16_bf16 v[84:99], a[228:231], v[60:63], v[84:99]\n" \
  "s_waitcnt vmcnt(18)\n" \
  "ds_read_b64_tr_b16 a[240:241], v47\n" \
  "ds_read_b64_tr_b16 a[242:243], v47 offset:512\n" \
  "ds_read_b64_tr_b16 a[244:245], v47 offset:64\n" \
  "ds_read_b64_tr_b16 a[246:247], v47 offset:576\n" \
  "ds_read_b128 v[60:63], v35 offset:1024\n" \
  "v_add_u32 v35, s31, v35\n" \
  "s_waitcnt lgkmcnt(8)\n" \
  "v_mfma_f32_32x32x16_bf16 v[68:83], a[232:235], v[64:67], v[68:83]\n" \
  "s_waitcnt lgkmcnt(6)\n" \
  "v_mfma_f32_32x32x16_bf16 v[84:99], a[236:239], v[64:67], v[84:99]\n" \
  "s_waitcnt vmcnt(16)\n" \
  "ds_read_b64_tr_b16 a[248:249], v47 offset:2048\n" \
  "ds_read_b64_tr_b16 a[250:251], v47 offset:2560\n" \
  "ds_read_b64_tr_b16 a[252:253], v47 offset:2112\n" \
  "ds_read_b64_tr_b16 a[254:255], v47 offset:2624\n" \
  "ds_read_b128 v[64:67], v36 offset:1024\n" \
  "v_add_u32 v36, s31, v36\n" \
  "s_waitcnt lgkmcnt(8)\n" \
  "v_mfma_f32_32x32x16_bf16 v[68:83], a[240:243], v[48:51], v[68:83]\n" \
  "s_waitcnt lgkmcnt(6)\n" \
  "v_mfma_f32_32x32x16_bf16 v[84:99], a[244:247], v[48:51], v[84:99]\n" \
  "s_waitcnt vmcnt(14)\n" \
  "ds_read_b64_tr_b16 v[192:193], v47 offset:4096\n" \
  "ds_read_b64_tr_b16 v[194:195], v47 offset:4608\n" \
  "ds_read_b64_tr_b16 v[196:197], v47 offset:4160\n" \
  "ds_read_b64_tr_b16 v[198:199], v47 offset:4672\n" \
  "ds_read_b128 v[48:51], v37 offset:1024\n" \
  "v_add_u32 v37, s31, v37\n" \
  "s_waitcnt lgkmcnt(8)\n" \
  "v_mfma_f32_32x32x16_bf16 v[68:83], a[248:251], v[52:55], v[68:83]\n" \
  "s_waitcnt lgkmcnt(6)\n" \
  "v_mfma_f32_32x32x16_bf16 v[84:99], a[252:255], v[52:55], v[84:99]\n" \
  "s_waitcnt vmcnt(12)\n" \
  "ds_read_b64_tr_b16 v[200:201], v47 offset:6144\n" \
  "ds_read_b64_tr_b16 v[202:203], v47 offset:6656\n" \
  "ds_read_b64_tr_b16 v[204:205], v47 offset:6208\n" \
  "ds_read_b64_tr_b16 v[206:207], v47 offset:6720\n" \
  "ds_read_b128 v[52:55], v38 offset:1024\n" \
  "v_add_u32 v38, s31, v38\n" \
  "s_waitcnt lgkmcnt(8)\n" \
  "v_mfma_f32_32x32x16_bf16 v[68:83], v[192:195], v[56:59], v[68:83]\n" \
  "s_waitcnt lgkmcnt(6)\n" \
  "v_mfma_f32_32x32x16_bf16 v[84:99], v[196:199], v[56:59], v[84:99]\n" \
  "s_waitcnt vmcnt(10)\n" \
  "ds_read_b64_tr_b16 v[208:209], v47 offset:8192\n" \
  "ds_read_b64_tr_b16 v[210:211], v47 offset:8704\n" \
  "ds_read_b64_tr_b16 v[212:213], v47 offset:8256\n" \
  "ds_read_b64_tr_b16 v[214:215], v47 offset:8768\n" \
  "ds_read_b128 v[56:59], v39 offset:1024\n" \
  "v_add_u32 v39, s31, v39\n" \
  "s_waitcnt lgkmcnt(8)\n" \
  "v_mfma_f32_32x32x16_bf16 v[68:83], v[200:203], v[60:63], v[68:83]\n" \
  "s_waitcnt lgkmcnt(6)\n" \
  "v_mfma_f32_32x32x16_bf16 v[84:99], v[204:207], v[60:63], v[84:99]\n" \
  "s_waitcnt vmcnt(8)\n" \
  "ds_read_b64_tr_b16 v[216:217], v47 offset:10240\n" \
  "ds_read_b64_tr_b16 v[218:219], v47 offset:10752\n" \
  "ds_read_b64_tr_b16 v[220:221], v47 offset:10304\n" \
  "ds_read_b64_tr_b16 v[222:223], v47 offset:10816\n" \
  "ds_read_b128 v[60:63], v40 offset:1024\n" \
  "v_add_u32 v40, s31, v40\n" \
  "s_waitcnt lgkmcnt(8)\n" \
  "v_mfma_f32_32x32x16_bf16 v[68:83], v[208:211], v[64:67], v[68:83]\n" \
  "s_waitcnt lgkmcnt(6)\n" \
  "v_mfma_f32_32x32x16_bf16 v[84:99], v[212:215], v[64:67], v[84:99]\n" \
  "s_waitcnt vmcnt(6)\n" \
  "ds_read_b64_tr_b16 v[224:225], v47 offset:12288\n" \
  "ds_read_b64_tr_b16 v[226:227], v47 offset:12800\n" \
  "ds_read_b64_tr_b16 v[228:229], v47 offset:12352\n" \
  "ds_read_b64_tr_b16 v[230:231], v47 offset:12864\n" \
  "ds_read_b128 v[64:67], v41 offset:1024\n" \
  "v_add_u32 v41, s31, v41\n" \
  "s_waitcnt lgkmcnt(8)\n" \
  "v_mfma_f32_32x32x16_bf16 v[68:83], v[216:219], v[48:51], v[68:83]\n" \
  "s_waitcnt lgkmcnt(6)\n" \
  "v_mfma_f32_32x32x16_bf16 v[84:99], v[220:223], v[48:51], v[84:99]\n" \
  "s_waitcnt vmcnt(4)\n" \
  "ds_read_b64_tr_b16 v[232:233], v47 offset:14336\n" \
  "ds_read_b64_tr_b16 v[234:235], v47 offset:14848\n" \
  "ds_read_b64_tr_b16 v[236:237], v47 offset:14400\n" \
  "ds_read_b64_tr_b16 v[238:239], v47 offset:14912\n" \
  "s_waitcnt lgkmcnt(7)\n" \
  "v_mfma_f32_32x32x16_bf16 v[68:83], v[224:227], v[52:55], v[68:83]\n" \
  "s_waitcnt lgkmcnt(5)\n" \
  "v_mfma_f32_32x32x16_bf16 v[84:99], v[228:231], v[52:55], v[84:99]\n" \
  "s_waitcnt vmcnt(2)\n" \
  "ds_read_b64_tr_b16 v[240:241], v47 offset:16384\n" \
  "ds_read_b64_tr_b16 v[242:243], v47 offset:16896\n" \
  "ds_read_b64_tr_b16 v[244:245], v47 offset:16448\n" \
  "ds_read_b64_tr_b16 v[246:247], v47 offset:16960\n" \
  "s_waitcnt lgkmcnt(6)\n" \
  "v_mfma_f32_32x32x16_bf16 v[68:83], v[232:235], v[56:59], v[68:83]\n" \
  "s_waitcnt lgkmcnt(4)\n" \
  "v_mfma_f32_32x32x16_bf16 v[84:99], v[236:239], v[56:59], v[84:99]\n" \
  "s_waitcnt vmcnt(0)\n" \
  "ds_read_b64_tr_b16 v[248:249], v47 offset:18432\n" \
  "ds_read_b64_tr_b16 v[250:251], v47 offset:18944\n" \
  "ds_read_b64_tr_b16 v[252:253], v47 offset:18496\n" \
  "ds_read_b64_tr_b16 v[254:255], v47 offset:19008\n" \
  "s_waitcnt lgkmcnt(6)\n" \
  "v_mfma_f32_32x32x16_bf16 v[68:83], v[240:243], v[60:63], v[68:83]\n" \
  "s_waitcnt lgkmcnt(4)\n" \
  "v_mfma_f32_32x32x16_bf16 v[84:99], v[244:247], v[60:63], v[84:99]\n" \
  "s_waitcnt lgkmcnt(2)\n" \
  "v_mfma_f32_32x32x16_bf16 v[68:83], v[248:251], v[64:67], v[68:83]\n" \
  "s_waitcnt lgkmcnt(0)\n" \
  "v_mfma_f32_32x32x16_bf16 v[84:99], v[252:255], v[64:67], v[84:99]\n" \
  "s_mov_b32 s37, 647168\n" \
  "buffer_load_dwordx2 v[20:21], off, s[8:11], s37 sc1\n" \
  "s_nop 1\n" \
  "v_mul_f32 v68, s36, v68\n" \
  "v_mul_f32 v69, s36, v69\n" \
  "v_mul_f32 v70, s36, v70\n" \
  "v_mul_f32 v71, s36, v71\n" \
  "v_mul_f32 v72, s36, v72\n" \
  "v_mul_f32 v73, s36, v73\n" \
  "v_mul_f32 v74, s36, v74\n" \
  "v_mul_f32 v75, s36, v75\n" \
  "v_cvt_pk_bf16_f32 v0, v68, v69\n" \
  "v_cvt_pk_bf16_f32 v1, v70, v71\n" \
  "v_cvt_pk_bf16_f32 v2, v72, v73\n" \
  "v_cvt_pk_bf16_f32 v3, v74, v75\n" \
  "buffer_store_dwordx4 v[0:3], v42, s[8:11], s33 offen sc1\n" \
  "v_mul_f32 v76, s36, v76\n" \
  "v_mul_f32 v77, s36, v77\n" \
  "v_mul_f32 v78, s36, v78\n" \
  "v_mul_f32 v79, s36, v79\n" \
  "v_mul_f32 v80, s36, v80\n" \
  "v_mul_f32 v81, s36, v81\n" \
  "v_mul_f32 v82, s36, v82\n" \
  "v_mul_f32 v83, s36, v83\n" \
  "v_cvt_pk_bf16_f32 v4, v76, v77\n" \
  "v_cvt_pk_bf16_f32 v5, v78, v79\n" \
  "v_cvt_pk_bf16_f32 v6, v80, v81\n" \
  "v_cvt_pk_bf16_f32 v7, v82, v83\n" \
  "buffer_store_dwordx4 v[4:7], v42, s[8:11], s33 offen offset:512 sc1\n" \
  "v_mul_f32 v84, s36, v84\n" \
  "v_mul_f32 v85, s36, v85\n" \
  "v_mul_f32 v86, s36, v86\n" \
  "v_mul_f32 v87, s36, v87\n" \
  "v_mul_f32 v88, s36, v88\n" \
  "v_mul_f32 v89, s36, v89\n" \
  "v_mul_f32 v90, s36, v90\n" \
  "v_mul_f32 v91, s36, v91\n" \
  "v_cvt_pk_bf16_f32 v8, v84, v85\n" \
  "v_cvt_pk_bf16_f32 v9, v86, v87\n" \
  "v_cvt_pk_bf16_f32 v10, v88, v89\n" \
  "v_cvt_pk_bf16_f32 v11, v90, v91\n" \
  "buffer_store_dwordx4 v[8:11], v42, s[8:11], s33 offen offset:2048 sc1\n" \
  "v_mul_f32 v92, s36, v92\n" \
  "v_mul_f32 v93, s36, v93\n" \
  "v_mul_f32 v94, s36, v94\n" \
  "v_mul_f32 v95, s36, v95\n" \
  "v_mul_f32 v96, s36, v96\n" \
  "v_mul_f32 v97, s36, v97\n" \
  "v_mul_f32 v98, s36, v98\n" \
  "v_mul_f32 v99, s36, v99\n" \
  "v_cvt_pk_bf16_f32 v12, v92, v93\n" \
  "v_cvt_pk_bf16_f32 v13, v94, v95\n" \
  "v_cvt_pk_bf16_f32 v14, v96, v97\n" \
  "v_cvt_pk_bf16_f32 v15, v98, v99\n" \
  "buffer_store_dwordx4 v[12:15], v42, s[8:11], s33 offen offset:2560 sc1\n" \
  "s_barrier\n" \
  "s_mov_b32 s30, 40960\n" \
  "s_mov_b32 s32, 40960\n" \
  "s_add_u32 s37, s24, s30\n" \
  "s_add_u32 m0, s37, 0\n" \
  "s_nop 0\n" \
  "buffer_load_dwordx4 v24, s[12:15], s32 offen lds\n" \
  "buffer_load_dwordx4 v25, s[12:15], s32 offen offset:1024 lds\n" \
  "buffer_load_dwordx4 v26, s[12:15], s32 offen offset:2048 lds\n" \
  "buffer_load_dwordx4 v27, s[12:15], s32 offen offset:3072 lds\n" \
  "s_add_u32 s37, s24, s30\n" \
  "s_add_u32 m0, s37, 4096\n" \
  "s_nop 0\n" \
  "buffer_load_dwordx4 v28, s[12:15], s32 offen lds\n" \
  "buffer_load_dwordx4 v29, s[12:15], s32 offen offset:1024 lds\n" \
  "buffer_load_dwordx4 v30, s[12:15], s32 offen offset:2048 lds\n" \
  "buffer_load_dwordx4 v31, s[12:15], s32 offen offset:3072 lds\n" \
  "s_add_u32 s37, s24, s30\n" \
  "s_add_u32 m0, s37, 8192\n" \
  "s_nop 0\n" \
  "buffer_load_dwordx4 v32, s[12:15], s32 offen lds\n" \
  "buffer_load_dwordx4 v33, s[12:15], s32 offen offset:1024 lds\n" \
  "s_mov_b32 s30, 81920\n" \
  "s_mov_b32 s32, 81920\n" \
  "s_add_u32 s37, s24, s30\n" \
  "s_add_u32 m0, s37, 0\n" \
  "s_nop 0\n" \
  "buffer_load_dwordx4 v24, s[12:15], s32 offen lds\n" \
  "buffer_load_dwordx4 v25, s[12:15], s32 offen offset:1024 lds\n" \
  "buffer_load_dwordx4 v26, s[12:15], s32 offen offset:2048 lds\n" \
  "buffer_load_dwordx4 v27, s[12:15], s32 offen offset:3072 lds\n" \
  "s_add_u32 s37, s24, s30\n" \
  "s_add_u32 m0, s37, 4096\n" \
  "s_nop 0\n" \
  "buffer_load_dwordx4 v28, s[12:15], s32 offen lds\n" \
  "buffer_load_dwordx4 v29, s[12:15], s32 offen offset:1024 lds\n" \
  "buffer_load_dwordx4 v30, s[12:15], s32 offen offset:2048 lds\n" \
  "buffer_load_dwordx4 v31, s[12:15], s32 offen offset:3072 lds\n" \
  "s_add_u32 s37, s24, s30\n" \
  "s_add_u32 m0, s37, 8192\n" \
  "s_nop 0\n" \
  "buffer_load_dwordx4 v32, s[12:15], s32 offen lds\n" \
  "buffer_load_dwordx4 v33, s[12:15], s32 offen offset:1024 lds\n" \
  "s_mov_b32 s30, 0\n" \
  "s_mov_b32 s32, 122880\n" \
  "s_mov_b32 s29, 40960\n" \
  "s_mov_b32 s37, 647168\n" \
  "s_waitcnt vmcnt(20)\n" \
  "v_mov_b32 v42, 0x80000000\n" \
  "GA_%=:\n" \
  "v_or_b32 v21, 0x3fff, v21\n" \
  "v_add_u32 v21, 1, v21\n" \
  "v_cmp_ne_u32 vcc, v20, v21\n" \
  "s_cbranch_vccz GAG_%=\n" \
  "s_sleep 2\n" \
  "buffer_load_dwordx2 v[20:21], off, s[8:11], s37 sc1\n" \
  "s_waitcnt vmcnt(0)\n" \
  "s_sub_u32 s38, s38, 1\n" \
  "s_cbranch_scc0 GA_%=\n" \
  "GAG_%=:\n" \
  "v_cmp_ne_u32 vcc, 0, v132\n" \
  "v_cndmask_b32 v44, v44, v20, vcc\n" \
  "s_mov_b32 exec_hi, 0\n" \
  "buffer_store_dword v44, v132, s[8:11], s35 offen sc1\n" \
  "s_mov_b32 exec_hi, -1\n" \
  "s_waitcnt vmcnt(1)\n" \
  "s_barrier\n" \
  "ds_read_b128 v[48:51], v34\n" \
  "ds_read_b128 v[52:55], v35\n" \
  "ds_read_b128 v[56:59], v36\n" \
  "ds_read_b128 v[60:63], v37\n" \
  "GL_%=:\n" \
  "s_add_u32 s37, s29, 40960\n" \
  "s_cmp_eq_u32 s37, 122880\n" \
  "s_cselect_b32 s37, 0, s37\n" \
  "s_sub_u32 s31, s37, s29\n" \
  "ds_read_b128 v[64:67], v38\n" \
  "s_waitcnt lgkmcnt(4)\n" \
  "v_mfma_f32_32x32x16_bf16 v[100:115], a[0:3], v[48:51], 0\n" \
  "v_mfma_f32_32x32x16_bf16 v[116:131], a[4:7], v[48:51], 0\n" \
  "ds_read_b128 v[48:51], v39\n" \
  "s_waitcnt lgkmcnt(4)\n" \
  "v_mfma_f32_32x32x16_bf16 v[100:115], a[8:11], v[52:55], v[100:115]\n" \
  "v_mul_f32 v68, s36, v68\n" \
  "v_mul_f32 v69, s36, v69\n" \
  "v_mul_f32 v70, s36, v70\n" \
  "v_mfma_f32_32x32x16_bf16 v[116:131], a[12:15], v[52:55], v[116:131]\n" \
  "v_mul_f32 v71, s36, v71\n" \
  "v_mul_f32 v72, s36, v72\n" \
  "v_mul_f32 v73, s36, v73\n" \
  "ds_read_b128 v[52:55], v40\n" \
  "s_waitcnt lgkmcnt(4)\n" \
  "v_mfma_f32_32x32x16_bf16 v[100:115], a[16:19], v[56:59], v[100:115]\n" \
  "v_mul_f32 v74, s36, v74\n" \
  "v_mul_f32 v75, s36, v75\n" \
  "v_cvt_pk_bf16_f32 v0, v68, v69\n" \
  "v_mfma_f32_32x32x16_bf16 v[116:131], a[20:23], v[56:59], v[116:131]\n" \
  "v_cvt_pk_bf16_f32 v1, v70, v71\n" \
  "v_cvt_pk_bf16_f32 v2, v72, v73\n" \
  "v_cvt_pk_bf16_f32 v3, v74, v75\n" \
  "ds_read_b128 v[56:59], v41\n" \
  "s_waitcnt lgkmcnt(4)\n" \
  "v_mfma_f32_32x32x16_bf16 v[100:115], a[24:27], v[60:63], v[100:115]\n" \
  "buffer_store_dwordx4 v[0:3], v42, s[8:11], s33 offen sc1\n" \
  "v_mul_f32 v76, s36, v76\n" \
  "v_mul_f32 v77, s36, v77\n" \
  "v_mfma_f32_32x32x16_bf16 v[116:131], a[28:31], v[60:63], v[116:131]\n" \
  "v_mul_f32 v78, s36, v78\n" \
  "v_mul_f32 v79, s36, v79\n" \
  "v_mul_f32 v80, s36, v80\n" \
  "ds_read_b128 v[60:63], v34 offset:256\n" \
  "s_waitcnt lgkmcnt(4)\n" \
  "v_mfma_f32_32x32x16_bf16 v[100:115], a[32:35], v[64:67], v[100:115]\n" \
  "v_mul_f32 v81, s36, v81\n" \
  "v_mul_f32 v82, s36, v82\n" \
  "v_mul_f32 v83, s36, v83\n" \
  "v_mfma_f32_32x32x16_bf16 v[116:131], a[36:39], v[64:67], v[116:131]\n" \
  "v_cvt_pk_bf16_f32 v4, v76, v77\n" \
  "v_cvt_pk_bf16_f32 v5, v78, v79\n" \
  "v_cvt_pk_bf16_f32 v6, v80, v81\n" \
  "ds_read_b128 v[64:67], v35 offset:256\n" \
  "s_waitcnt lgkmcnt(4)\n" \
  "v_mfma_f32_32x32x16_bf16 v[100:115], a[40:43], v[48:51], v[100:115]\n" \
  "v_cvt_pk_bf16_f32 v7, v82, v83\n" \
  "buffer_store_dwordx4 v[4:7], v42, s[8:11], s33 offen offset:512 sc1\n" \
  "v_mul_f32 v84, s36, v84\n" \
  "v_mfma_f32_32x32x16_bf16 v[116:131], a[44:47], v[48:51], v[116:131]\n" \
  "v_mul_f32 v85, s36, v85\n" \
  "v_mul_f32 v86, s36, v86\n" \
  "v_mul_f32 v87, s36, v87\n" \
  "ds_read_b128 v[48:51], v36 offset:256\n" \
  "s_waitcnt lgkmcnt(4)\n" \
  "v_mfma_f32_32x32x16_bf16 v[100:115], a[48:51], v[52:55], v[100:115]\n" \
  "v_mul_f32 v88, s36, v88\n" \
  "v_mul_f32 v89, s36, v89\n" \
  "v_mul_f32 v90, s36, v90\n" \
  "v_mfma_f32_32x32x16_bf16 v[116:131], a[52:55], v[52:55], v[116:131]\n" \
  "v_mul_f32 v91, s36, v91\n" \
  "v_cvt_pk_bf16_f32 v8, v84, v85\n" \
  "v_cvt_pk_bf16_f32 v9, v86, v87\n" \
  "ds_read_b128 v[52:55], v37 offset:256\n" \
  "s_waitcnt lgkmcnt(4)\n" \
  "v_mfma_f32_32x32x16_bf16 v[100:115], a[56:59], v[56:59], v[100:115]\n" \
  "v_cvt_pk_bf16_f32 v10, v88, v89\n" \
  "v_cvt_pk_bf16_f32 v11, v90, v91\n" \
  "buffer_store_dwordx4 v[8:11], v42, s[8:11], s33 offen offset:2048 sc1\n" \
  "v_mfma_f32_32x32x16_bf16 v[116:131], a[60:63], v[56:59], v[116:131]\n" \
  "v_mul_f32 v92, s36, v92\n" \
  "v_mul_f32 v93, s36, v93\n" \
  "v_mul_f32 v94, s36, v94\n" \
  "ds_read_b128 v[56:59], v38 offset:256\n" \
  "s_waitcnt lgkmcnt(4)\n" \
  "v_mfma_f32_32x32x16_bf16 v[100:115], a[64:67], v[60:63], v[100:115]\n" \
  "v_mul_f32 v95, s36, v95\n" \
  "v_mul_f32 v96, s36, v96\n" \
  "v_mul_f32 v97, s36, v97\n" \
  "v_mfma_f32_32x32x16_bf16 v[116:131], a[68:71], v[60:63], v[116:131]\n" \
  "v_mul_f32 v98, s36, v98\n" \
  "v_mul_f32 v99, s36, v99\n" \
  "v_cvt_pk_bf16_f32 v12, v92, v93\n" \
  "ds_read_b128 v[60:63], v39 offset:256\n" \
  "s_waitcnt lgkmcnt(4)\n" \
  "v_mfma_f32_32x32x16_bf16 v[100:115], a[72:75], v[64:67], v[100:115]\n" \
  "v_cvt_pk_bf16_f32 v13, v94, v95\n" \
  "v_cvt_pk_bf16_f32 v14, v96, v97\n" \
  "v_cvt_pk_bf16_f32 v15, v98, v99\n" \
  "v_mfma_f32_32x32x16_bf16 v[116:131], a[76:79], v[64:67], v[116:131]\n" \
  "buffer_store_dwordx4 v[12:15], v42, s[8:11], s33 offen offset:2560 sc1\n" \
  "ds_read_b128 v[64:67], v40 offset:256\n" \
  "s_waitcnt lgkmcnt(4)\n" \
  "v_mfma_f32_32x32x16_bf16 v[100:115], a[80:83], v[48:51], v[100:115]\n" \
  "v_mfma_f32_32x32x16_bf16 v[116:131], a[84:87], v[48:51], v[116:131]\n" \
  "ds_read_b128 v[48:51], v41 offset:256\n" \
  "s_waitcnt lgkmcnt(4)\n" \
  "v_mfma_f32_32x32x16_bf16 v[100:115], a[88:91], v[52:55], v[100:115]\n" \
  "v_mfma_f32_32x32x16_bf16 v[116:131], a[92:95], v[52:55], v[116:131]\n" \
  "ds_read_b128 v[52:55], v34 offset:512\n" \
  "s_waitcnt lgkmcnt(4)\n" \
  "v_mfma_f32_32x32x16_bf16 v[100:115], a[96:99], v[56:59], v[100:115]\n" \
  "s_add_u32 s37, s24, s30\n" \
  "s_add_u32 m0, s37, 0\n" \
  "s_nop 0\n" \
  "buffer_load_dwordx4 v24, s[12:15], s32 offen lds\n" \
  "v_mfma_f32_32x32x16_bf16 v[116:131], a[100:103], v[56:59], v[116:131]\n" \
  "ds_read_b128 v[56:59], v35 offset:512\n" \
  "s_waitcnt lgkmcnt(4)\n" \
  "v_mfma_f32_32x32x16_bf16 v[100:115], a[104:107], v[60:63], v[100:115]\n" \
  "v_mfma_f32_32x32x16_bf16 v[116:131], a[108:111], v[60:63], v[116:131]\n" \
  "ds_read_b128 v[60:63], v36 offset:512\n" \
  "s_waitcnt lgkmcnt(4)\n" \
  "v_mfma_f32_32x32x16_bf16 v[100:115], a[112:115], v[64:67], v[100:115]\n" \
  "buffer_load_dwordx4 v25, s[12:15], s32 offen offset:1024 lds\n" \
  "v_mfma_f32_32x32x16_bf16 v[116:131], a[116:119], v[64:67], v[116:131]\n" \
  "ds_read_b128 v[64:67], v37 offset:512\n" \
  "s_waitcnt lgkmcnt(4)\n" \
  "v_mfma_f32_32x32x16_bf16 v[100:115], a[120:123], v[48:51], v[100:115]\n" \
  "v_mfma_f32_32x32x16_bf16 v[116:131], a[124:127], v[48:51], v[116:131]\n" \
  "ds_read_b128 v[48:51], v38 offset:512\n" \
  "s_waitcnt lgkmcnt(4)\n" \
  "v_mfma_f32_32x32x16_bf16 v[100:115], a[128:131], v[52:55], v[100:115]\n" \
  "buffer_load_dwordx4 v26, s[12:15], s32 offen offset:2048 lds\n" \
  "v_mfma_f32_32x32x16_bf16 v[116:131], a[132:135], v[52:55], v[116:131]\n" \
  "ds_read_b128 v[52:55], v39 offset:512\n" \
  "s_waitcnt lgkmcnt(4)\n" \
  "v_mfma_f32_32x32x16_bf16 v[100:115], a[136:139], v[56:59], v[100:115]\n" \
  "v_mfma_f32_32x32x16_bf16 v[116:131], a[140:143], v[56:59], v[116:131]\n" \
  "ds_read_b128 v[56:59], v40 offset:512\n" \
  "s_waitcnt lgkmcnt(4)\n" \
  "v_mfma_f32_32x32x16_bf16 v[100:115], a[144:147], v[60:63], v[100:115]\n" \
  "buffer_load_dwordx4 v27, s[12:15], s32 offen offset:3072 lds\n" \
  "v_mfma_f32_32x32x16_bf16 v[116:131], a[148:151], v[60:63], v[116:131]\n" \
  "ds_read_b128 v[60:63], v41 offset:512\n" \
  "s_waitcnt lgkmcnt(4)\n" \
  "v_mfma_f32_32x32x16_bf16 v[100:115], a[152:155], v[64:67], v[100:115]\n" \
  "v_mfma_f32_32x32x16_bf16 v[116:131], a[156:159], v[64:67], v[116:131]\n" \
  "ds_read_b128 v[64:67], v34 offset:768\n" \
  "s_waitcnt lgkmcnt(4)\n" \
  "v_mfma_f32_32x32x16_bf16 v[100:115], a[160:163], v[48:51], v[100:115]\n" \
  "s_add_u32 s37, s24, s30\n" \
  "s_add_u32 m0, s37, 4096\n" \
  "s_nop 0\n" \
  "buffer_load_dwordx4 v28, s[12:15], s32 offen lds\n" \
  "v_mfma_f32_32x32x16_bf16 v[116:131], a[164:167], v[48:51], v[116:131]\n" \
  "ds_read_b128 v[48:51], v35 offset:768\n" \
  "s_waitcnt lgkmcnt(4)\n" \
  "v_mfma_f32_32x32x16_bf16 v[100:115], a[168:171], v[52:55], v[100:115]\n" \
  "v_mfma_f32_32x32x16_bf16 v[116:131], a[172:175], v[52:55], v[116:131]\n" \
  "ds_read_b128 v[52:55], v36 offset:768\n" \
  "s_waitcnt lgkmcnt(4)\n" \
  "v_mfma_f32_32x32x16_bf16 v[100:115], a[176:179], v[56:59], v[100:115]\n" \
  "buffer_load_dwordx4 v29, s[12:15], s32 offen offset:1024 lds\n" \
  "v_mfma_f32_32x32x16_bf16 v[116:131], a[180:183], v[56:59], v[116:131]\n" \
  "ds_read_b128 v[56:59], v37 offset:768\n" \
  "s_waitcnt lgkmcnt(4)\n" \
  "v_mfma_f32_32x32x16_bf16 v[100:115], a[184:187], v[60:63], v[100:115]\n" \
  "v_mfma_f32_32x32x16_bf16 v[116:131], a[188:191], v[60:63], v[116:131]\n" \
  "ds_read_b128 v[60:63], v38 offset:768\n" \
  "s_waitcnt lgkmcnt(4)\n" \
  "v_mfma_f32_32x32x16_bf16 v[100:115], a[192:195], v[64:67], v[100:115]\n" \
  "buffer_load_dwordx4 v30, s[12:15], s32 offen offset:2048 lds\n" \
  "v_mfma_f32_32x32x16_bf16 v[116:131], a[196:199], v[64:67], v[116:131]\n" \
  "ds_read_b128 v[64:67], v39 offset:768\n" \
  "s_waitcnt lgkmcnt(4)\n" \
  "v_mfma_f32_32x32x16_bf16 v[100:115], a[200:203], v[48:51], v[100:115]\n" \
  "v_mfma_f32_32x32x16_bf16 v[116:131], a[204:207], v[48:51], v[116:131]\n" \
  "ds_read_b128 v[48:51], v40 offset:768\n" \
  "s_waitcnt lgkmcnt(4)\n" \
  "v_mfma_f32_32x32x16_bf16 v[100:115], a[208:211], v[52:55], v[100:115]\n" \
  "buffer_load_dwordx4 v31, s[12:15], s32 offen offset:3072 lds\n" \
  "v_mfma_f32_32x32x16_bf16 v[116:131], a[212:215], v[52:55], v[116:131]\n" \
  "ds_read_b128 v[52:55], v41 offset:768\n" \
  "s_waitcnt lgkmcnt(4)\n" \
  "v_mfma_f32_32x32x16_bf16 v[100:115], a[216:219], v[56:59], v[100:115]\n" \
  "v_mfma_f32_32x32x16_bf16 v[116:131], a[220:223], v[56:59], v[116:131]\n" \
  "ds_read_b128 v[56:59], v34 offset:1024\n" \
  "v_add_u32 v34, s31, v34\n" \
  "s_waitcnt lgkmcnt(4)\n" \
  "v_mfma_f32_32x32x16_bf16 v[100:115], a[224:227], v[60:63], v[100:115]\n" \
  "s_add_u32 s37, s24, s30\n" \
  "s_add_u32 m0, s37, 8192\n" \
  "s_nop 0\n" \
  "buffer_load_dwordx4 v32, s[12:15], s32 offen lds\n" \
  "v_mfma_f32_32x32x16_bf16 v[116:131], a[228:231], v[60:63], v[116:131]\n" \
  "ds_read_b128 v[60:63], v35 offset:1024\n" \
  "v_add_u32 v35, s31, v35\n" \
  "s_waitcnt lgkmcnt(4)\n" \
  "v_mfma_f32_32x32x16_bf16 v[100:115], a[232:235], v[64:67], v[100:115]\n" \
  "v_mfma_f32_32x32x16_bf16 v[116:131], a[236:239], v[64:67], v[116:131]\n" \
  "ds_read_b128 v[64:67], v36 offset:1024\n" \
  "v_add_u32 v36, s31, v36\n" \
  "s_waitcnt lgkmcnt(4)\n" \
  "v_mfma_f32_32x32x16_bf16 v[100:115], a[240:243], v[48:51], v[100:115]\n" \
  "buffer_load_dwordx4 v33, s[12:15], s32 offen offset:1024 lds\n" \
  "v_mfma_f32_32x32x16_bf16 v[116:131], a[244:247], v[48:51], v[116:131]\n" \
  "ds_read_b128 v[48:51], v37 offset:1024\n" \
  "v_add_u32 v37, s31, v37\n" \
  "s_waitcnt lgkmcnt(4)\n" \
  "v_mfma_f32_32x32x16_bf16 v[100:115], a[248:251], v[52:55], v[100:115]\n" \
  "v_mfma_f32_32x32x16_bf16 v[116:131], a[252:255], v[52:55], v[116:131]\n" \
  "ds_read_b128 v[52:55], v38 offset:1024\n" \
  "v_add_u32 v38, s31, v38\n" \
  "s_waitcnt lgkmcnt(4)\n" \
  "v_mfma_f32_32x32x16_bf16 v[100:115], v[192:195], v[56:59], v[100:115]\n" \
  "v_mfma_f32_32x32x16_bf16 v[116:131], v[196:199], v[56:59], v[116:131]\n" \
  "ds_read_b128 v[56:59], v39 offset:1024\n" \
  "v_add_u32 v39, s31, v39\n" \
  "s_waitcnt lgkmcnt(4)\n" \
  "v_mfma_f32_32x32x16_bf16 v[100:115], v[200:203], v[60:63], v[100:115]\n" \
  "v_mfma_f32_32x32x16_bf16 v[116:131], v[204:207], v[60:63], v[116:131]\n" \
  "ds_read_b128 v[60:63], v40 offset:1024\n" \
  "v_add_u32 v40, s31, v40\n" \
  "s_waitcnt lgkmcnt(4)\n" \
  "v_mfma_f32_32x32x16_bf16 v[100:115], v[208:211], v[64:67], v[100:115]\n" \
  "v_mfma_f32_32x32x16_bf16 v[116:131], v[212:215], v[64:67], v[116:131]\n" \
  "s_add_u32 s33, s33, s34\n" \
  "v_mov_b32 v42, v43\n" \
  "ds_read_b128 v[64:67], v41 offset:1024\n" \
  "v_add_u32 v41, s31, v41\n" \
  "s_waitcnt lgkmcnt(4)\n" \
  "v_mfma_f32_32x32x16_bf16 v[100:115], v[216:219], v[48:51], v[100:115]\n" \
  "v_mfma_f32_32x32x16_bf16 v[116:131], v[220:223], v[48:51], v[116:131]\n" \
  "s_waitcnt lgkmcnt(0)\n" \
  "s_waitcnt vmcnt(10)\n" \
  "s_barrier\n" \
  "s_mov_b32 exec_hi, 0\n" \
  "buffer_store_dword v44, v132, s[8:11], s35 offen sc1\n" \
  "s_mov_b32 exec_hi, -1\n" \
  "v_add_u32 v44, 1, v44\n" \
  "ds_read_b128 v[48:51], v34\n" \
  "v_mfma_f32_32x32x16_bf16 v[100:115], v[224:227], v[52:55], v[100:115]\n" \
  "v_mfma_f32_32x32x16_bf16 v[116:131], v[228:231], v[52:55], v[116:131]\n" \
  "ds_read_b128 v[52:55], v35\n" \
  "v_mfma_f32_32x32x16_bf16 v[100:115], v[232:235], v[56:59], v[100:115]\n" \
  "v_mfma_f32_32x32x16_bf16 v[116:131], v[236:239], v[56:59], v[116:131]\n" \
  "ds_read_b128 v[56:59], v36\n" \
  "v_mfma_f32_32x32x16_bf16 v[100:115], v[240:243], v[60:63], v[100:115]\n" \
  "v_mfma_f32_32x32x16_bf16 v[116:131], v[244:247], v[60:63], v[116:131]\n" \
  "ds_read_b128 v[60:63], v37\n" \
  "v_mfma_f32_32x32x16_bf16 v[100:115], v[248:251], v[64:67], v[100:115]\n" \
  "v_mfma_f32_32x32x16_bf16 v[116:131], v[252:255], v[64:67], v[116:131]\n" \
  "s_mov_b32 s30, s29\n" \
  "s_add_u32 s29, s29, s31\n" \
  "s_add_u32 s32, s32, 40960\n" \
  "s_min_u32 s32, s32, 2580480\n" \
  "s_cmp_eq_u32 s28, 62\n" \
  "s_cbranch_scc1 GE_%=\n" \
  "s_add_u32 s37, s29, 40960\n" \
  "s_cmp_eq_u32 s37, 122880\n" \
  "s_cselect_b32 s37, 0, s37\n" \
  "s_sub_u32 s31, s37, s29\n" \
  "ds_read_b128 v[64:67], v38\n" \
  "s_waitcnt lgkmcnt(4)\n" \
  "v_mfma_f32_32x32x16_bf16 v[68:83], a[0:3], v[48:51], 0\n" \
  "v_mfma_f32_32x32x16_bf16 v[84:99], a[4:7], v[48:51], 0\n" \
  "ds_read_b128 v[48:51], v39\n" \
  "s_waitcnt lgkmcnt(4)\n" \
  "v_mfma_f32_32x32x16_bf16 v[68:83], a[8:11], v[52:55], v[68:83]\n" \
  "v_mul_f32 v100, s36, v100\n" \
  "v_mul_f32 v101, s36, v101\n" \
  "v_mul_f32 v102, s36, v102\n" \
  "v_mfma_f32_32x32x16_bf16 v[84:99], a[12:15], v[52:55], v[84:99]\n" \
  "v_mul_f32 v103, s36, v103\n" \
  "v_mul_f32 v104, s36, v104\n" \
  "v_mul_f32 v105, s36, v105\n" \
  "ds_read_b128 v[52:55], v40\n" \
  "s_waitcnt lgkmcnt(4)\n" \
  "v_mfma_f32_32x32x16_bf16 v[68:83], a[16:19], v[56:59], v[68:83]\n" \
  "v_mul_f32 v106, s36, v106\n" \
  "v_mul_f32 v107, s36, v107\n" \
  "v_cvt_pk_bf16_f32 v0, v100, v101\n" \
  "v_mfma_f32_32x32x16_bf16 v[84:99], a[20:23], v[56:59], v[84:99]\n" \
  "v_cvt_pk_bf16_f32 v1, v102, v103\n" \
  "v_cvt_pk_bf16_f32 v2, v104, v105\n" \
  "v_cvt_pk_bf16_f32 v3, v106, v107\n" \
  "ds_read_b128 v[56:59], v41\n" \
  "s_waitcnt lgkmcnt(4)\n" \
  "v_mfma_f32_32x32x16_bf16 v[68:83], a[24:27], v[60:63], v[68:83]\n" \
  "buffer_store_dwordx4 v[0:3], v42, s[8:11], s33 offen sc1\n" \
  "v_mul_f32 v108, s36, v108\n" \
  "v_mul_f32 v109, s36, v109\n" \
  "v_mfma_f32_32x32x16_bf16 v[84:99], a[28:31], v[60:63], v[84:99]\n" \
  "v_mul_f32 v110, s36, v110\n" \
  "v_mul_f32 v111, s36, v111\n" \
  "v_mul_f32 v112, s36, v112\n" \
  "ds_read_b128 v[60:63], v34 offset:256\n" \
  "s_waitcnt lgkmcnt(4)\n" \
  "v_mfma_f32_32x32x16_bf16 v[68:83], a[32:35], v[64:67], v[68:83]\n" \
  "v_mul_f32 v113, s36, v113\n" \
  "v_mul_f32 v114, s36, v114\n" \
  "v_mul_f32 v115, s36, v115\n" \
  "v_mfma_f32_32x32x16_bf16 v[84:99], a[36:39], v[64:67], v[84:99]\n" \
  "v_cvt_pk_bf16_f32 v4, v108, v109\n" \
  "v_cvt_pk_bf16_f32 v5, v110, v111\n" \
  "v_cvt_pk_bf16_f32 v6, v112, v113\n" \
  "ds_read_b128 v[64:67], v35 offset:256\n" \
  "s_waitcnt lgkmcnt(4)\n" \
  "v_mfma_f32_32x32x16_bf16 v[68:83], a[40:43], v[48:51], v[68:83]\n" \
  "v_cvt_pk_bf16_f32 v7, v114, v115\n" \
  "buffer_store_dwordx4 v[4:7], v42, s[8:11], s33 offen offset:512 sc1\n" \
  "v_mul_f32 v116, s36, v116\n" \
  "v_mfma_f32_32x32x16_bf16 v[84:99], a[44:47], v[48:51], v[84:99]\n" \
  "v_mul_f32 v117, s36, v117\n" \
  "v_mul_f32 v118, s36, v118\n" \
  "v_mul_f32 v119, s36, v119\n" \
  "ds_read_b128 v[48:51], v36 offset:256\n" \
  "s_waitcnt lgkmcnt(4)\n" \
  "v_mfma_f32_32x32x16_bf16 v[68:83], a[48:51], v[52:55], v[68:83]\n" \
  "v_mul_f32 v120, s36, v120\n" \
  "v_mul_f32 v121, s36, v121\n" \
  "v_mul_f32 v122, s36, v122\n" \
  "v_mfma_f32_32x32x16_bf16 v[84:99], a[52:55], v[52:55], v[84:99]\n" \
  "v_mul_f32 v123, s36, v123\n" \
  "v_cvt_pk_bf16_f32 v8, v116, v117\n" \
  "v_cvt_pk_bf16_f32 v9, v118, v119\n" \
  "ds_read_b128 v[52:55], v37 offset:256\n" \
  "s_waitcnt lgkmcnt(4)\n" \
  "v_mfma_f32_32x32x16_bf16 v[68:83], a[56:59], v[56:59], v[68:83]\n" \
  "v_cvt_pk_bf16_f32 v10, v120, v121\n" \
  "v_cvt_pk_bf16_f32 v11, v122, v123\n" \
  "buffer_store_dwordx4 v[8:11], v42, s[8:11], s33 offen offset:2048 sc1\n" \
  "v_mfma_f32_32x32x16_bf16 v[84:99], a[60:63], v[56:59], v[84:99]\n" \
  "v_mul_f32 v124, s36, v124\n" \
  "v_mul_f32 v125, s36, v125\n" \
  "v_mul_f32 v126, s36, v126\n" \
  "ds_read_b128 v[56:59], v38 offset:256\n" \
  "s_waitcnt lgkmcnt(4)\n" \
  "v_mfma_f32_32x32x16_bf16 v[68:83], a[64:67], v[60:63], v[68:83]\n" \
  "v_mul_f32 v127, s36, v127\n" \
  "v_mul_f32 v128, s36, v128\n" \
  "v_mul_f32 v129, s36, v129\n" \
  "v_mfma_f32_32x32x16_bf16 v[84:99], a[68:71], v[60:63], v[84:99]\n" \
  "v_mul_f32 v130, s36, v130\n" \
  "v_mul_f32 v131, s36, v131\n" \
  "v_cvt_pk_bf16_f32 v12, v124, v125\n" \
  "ds_read_b128 v[60:63], v39 offset:256\n" \
  "s_waitcnt lgkmcnt(4)\n" \
  "v_mfma_f32_32x32x16_bf16 v[68:83], a[72:75], v[64:67], v[68:83]\n" \
  "v_cvt_pk_bf16_f32 v13, v126, v127\n" \
  "v_cvt_pk_bf16_f32 v14, v128, v129\n" \
  "v_cvt_pk_bf16_f32 v15, v130, v131\n" \
  "v_mfma_f32_32x32x16_bf16 v[84:99], a[76:79], v[64:67], v[84:99]\n" \
  "buffer_store_dwordx4 v[12:15], v42, s[8:11], s33 offen offset:2560 sc1\n" \
  "ds_read_b128 v[64:67], v40 offset:256\n" \
  "s_waitcnt lgkmcnt(4)\n" \
  "v_mfma_f32_32x32x16_bf16 v[68:83], a[80:83], v[48:51], v[68:83]\n" \
  "v_mfma_f32_32x32x16_bf16 v[84:99], a[84:87], v[48:51], v[84:99]\n" \
  "ds_read_b128 v[48:51], v41 offset:256\n" \
  "s_waitcnt lgkmcnt(4)\n" \
  "v_mfma_f32_32x32x16_bf16 v[68:83], a[88:91], v[52:55], v[68:83]\n" \
  "v_mfma_f32_32x32x16_bf16 v[84:99], a[92:95], v[52:55], v[84:99]\n" \
  "ds_read_b128 v[52:55], v34 offset:512\n" \
  "s_waitcnt lgkmcnt(4)\n" \
  "v_mfma_f32_32x32x16_bf16 v[68:83], a[96:99], v[56:59], v[68:83]\n" \
  "s_add_u32 s37, s24, s30\n" \
  "s_add_u32 m0, s37, 0\n" \
  "s_nop 0\n" \
  "buffer_load_dwordx4 v24, s[12:15], s32 offen lds\n" \
  "v_mfma_f32_32x32x16_bf16 v[84:99], a[100:103], v[56:59], v[84:99]\n" \
  "ds_read_b128 v[56:59], v35 offset:512\n" \
  "s_waitcnt lgkmcnt(4)\n" \
  "v_mfma_f32_32x32x16_bf16 v[68:83], a[104:107], v[60:63], v[68:83]\n" \
  "v_mfma_f32_32x32x16_bf16 v[84:99], a[108:111], v[60:63], v[84:99]\n" \
  "ds_read_b128 v[60:63], v36 offset:512\n" \
  "s_waitcnt lgkmcnt(4)\n" \
  "v_mfma_f32_32x32x16_bf16 v[68:83], a[112:115], v[64:67], v[68:83]\n" \
  "buffer_load_dwordx4 v25, s[12:15], s32 offen offset:1024 lds\n" \
  "v_mfma_f32_32x32x16_bf16 v[84:99], a[116:119], v[64:67], v[84:99]\n" \
  "ds_read_b128 v[64:67], v37 offset:512\n" \
  "s_waitcnt lgkmcnt(4)\n" \
  "v_mfma_f32_32x32x16_bf16 v[68:83], a[120:123], v[48:51], v[68:83]\n" \
  "v_mfma_f32_32x32x16_bf16 v[84:99], a[124:127], v[48:51], v[84:99]\n" \
  "ds_read_b128 v[48:51], v38 offset:512\n" \
  "s_waitcnt lgkmcnt(4)\n" \
  "v_mfma_f32_32x32x16_bf16 v[68:83], a[128:131], v[52:55], v[68:83]\n" \
  "buffer_load_dwordx4 v26, s[12:15], s32 offen offset:2048 lds\n" \
  "v_mfma_f32_32x32x16_bf16 v[84:99], a[132:135], v[52:55], v[84:99]\n" \
  "ds_read_b128 v[52:55], v39 offset:512\n" \
  "s_waitcnt lgkmcnt(4)\n" \
  "v_mfma_f32_32x32x16_bf16 v[68:83], a[136:139], v[56:59], v[68:83]\n" \
  "v_mfma_f32_32x32x16_bf16 v[84:99], a[140:143], v[56:59], v[84:99]\n" \
  "ds_read_b128 v[56:59], v40 offset:512\n" \
  "s_waitcnt lgkmcnt(4)\n" \
  "v_mfma_f32_32x32x16_bf16 v[68:83], a[144:147], v[60:63], v[68:83]\n" \
  "buffer_load_dwordx4 v27, s[12:15], s32 offen offset:3072 lds\n" \
  "v_mfma_f32_32x32x16_bf16 v[84:99], a[148:151], v[60:63], v[84:99]\n" \
  "ds_read_b128 v[60:63], v41 offset:512\n" \
  "s_waitcnt lgkmcnt(4)\n" \
  "v_mfma_f32_32x32x16_bf16 v[68:83], a[152:155], v[64:67], v[68:83]\n" \
  "v_mfma_f32_32x32x16_bf16 v[84:99], a[156:159], v[64:67], v[84:99]\n" \
  "ds_read_b128 v[64:67], v34 offset:768\n" \
  "s_waitcnt lgkmcnt(4)\n" \
  "v_mfma_f32_32x32x16_bf16 v[68:83], a[160:163], v[48:51], v[68:83]\n" \
  "s_add_u32 s37, s24, s30\n" \
  "s_add_u32 m0, s37, 4096\n" \
  "s_nop 0\n" \
  "buffer_load_dwordx4 v28, s[12:15], s32 offen lds\n" \
  "v_mfma_f32_32x32x16_bf16 v[84:99], a[164:167], v[48:51], v[84:99]\n" \
  "ds_read_b128 v[48:51], v35 offset:768\n" \
  "s_waitcnt lgkmcnt(4)\n" \
  "v_mfma_f32_32x32x16_bf16 v[68:83], a[168:171], v[52:55], v[68:83]\n" \
  "v_mfma_f32_32x32x16_bf16 v[84:99], a[172:175], v[52:55], v[84:99]\n" \
  "ds_read_b128 v[52:55], v36 offset:768\n" \
  "s_waitcnt lgkmcnt(4)\n" \
  "v_mfma_f32_32x32x16_bf16 v[68:83], a[176:179], v[56:59], v[68:83]\n" \
  "buffer_load_dwordx4 v29, s[12:15], s32 offen offset:1024 lds\n" \
  "v_mfma_f32_32x32x16_bf16 v[84:99], a[180:183], v[56:59], v[84:99]\n" \
  "ds_read_b128 v[56:59], v37 offset:768\n" \
  "s_waitcnt lgkmcnt(4)\n" \
  "v_mfma_f32_32x32x16_bf16 v[68:83], a[184:187], v[60:63], v[68:83]\n" \
  "v_mfma_f32_32x32x16_bf16 v[84:99], a[188:191], v[60:63], v[84:99]\n" \
  "ds_read_b128 v[60:63], v38 offset:768\n" \
  "s_waitcnt lgkmcnt(4)\n" \
  "v_mfma_f32_32x32x16_bf16 v[68:83], a[192:195], v[64:67], v[68:83]\n" \
  "buffer_load_dwordx4 v30, s[12:15], s32 offen offset:2048 lds\n" \
  "v_mfma_f32_32x32x16_bf16 v[84:99], a[196:199], v[64:67], v[84:99]\n" \
  "ds_read_b128 v[64:67], v39 offset:768\n" \
  "s_waitcnt lgkmcnt(4)\n" \
  "v_mfma_f32_32x32x16_bf16 v[68:83], a[200:203], v[48:51], v[68:83]\n" \
  "v_mfma_f32_32x32x16_bf16 v[84:99], a[204:207], v[48:51], v[84:99]\n" \
  "ds_read_b128 v[48:51], v40 offset:768\n" \
  "s_waitcnt lgkmcnt(4)\n" \
  "v_mfma_f32_32x32x16_bf16 v[68:83], a[208:211], v[52:55], v[68:83]\n" \
  "buffer_load_dwordx4 v31, s[12:15], s32 offen offset:3072 lds\n" \
  "v_mfma_f32_32x32x16_bf16 v[84:99], a[212:215], v[52:55], v[84:99]\n" \
  "ds_read_b128 v[52:55], v41 offset:768\n" \
  "s_waitcnt lgkmcnt(4)\n" \
  "v_mfma_f32_32x32x16_bf16 v[68:83], a[216:219], v[56:59], v[68:83]\n" \
  "v_mfma_f32_32x32x16_bf16 v[84:99], a[220:223], v[56:59], v[84:99]\n" \
  "ds_read_b128 v[56:59], v34 offset:1024\n" \
  "v_add_u32 v34, s31, v34\n" \
  "s_waitcnt lgkmcnt(4)\n" \
  "v_mfma_f32_32x32x16_bf16 v[68:83], a[224:227], v[60:63], v[68:83]\n" \
  "s_add_u32 s37, s24, s30\n" \
  "s_add_u32 m0, s37, 8192\n" \
  "s_nop 0\n" \
  "buffer_load_dwordx4 v32, s[12:15], s32 offen lds\n" \
  "v_mfma_f32_32x32x16_bf16 v[84:99], a[228:231], v[60:63], v[84:99]\n" \
  "ds_read_b128 v[60:63], v35 offset:1024\n" \
  "v_add_u32 v35, s31, v35\n" \
  "s_waitcnt lgkmcnt(4)\n" \
  "v_mfma_f32_32x32x16_bf16 v[68:83], a[232:235], v[64:67], v[68:83]\n" \
  "v_mfma_f32_32x32x16_bf16 v[84:99], a[236:239], v[64:67], v[84:99]\n" \
  "ds_read_b128 v[64:67], v36 offset:1024\n" \
  "v_add_u32 v36, s31, v36\n" \
  "s_waitcnt lgkmcnt(4)\n" \
  "v_mfma_f32_32x32x16_bf16 v[68:83], a[240:243], v[48:51], v[68:83]\n" \
  "buffer_load_dwordx4 v33, s[12:15], s32 offen offset:1024 lds\n" \
  "v_mfma_f32_32x32x16_bf16 v[84:99], a[244:247], v[48:51], v[84:99]\n" \
  "ds_read_b128 v[48:51], v37 offset:1024\n" \
  "v_add_u32 v37, s31, v37\n" \
  "s_waitcnt lgkmcnt(4)\n" \
  "v_mfma_f32_32x32x16_bf16 v[68:83], a[248:251], v[52:55], v[68:83]\n" \
  "v_mfma_f32_32x32x16_bf16 v[84:99], a[252:255], v[52:55], v[84:99]\n" \
  "ds_read_b128 v[52:55], v38 offset:1024\n" \
  "v_add_u32 v38, s31, v38\n" \
  "s_waitcnt lgkmcnt(4)\n" \
  "v_mfma_f32_32x32x16_bf16 v[68:83], v[192:195], v[56:59], v[68:83]\n" \
  "v_mfma_f32_32x32x16_bf16 v[84:99], v[196:199], v[56:59], v[84:99]\n" \
  "ds_read_b128 v[56:59], v39 offset:1024\n" \
  "v_add_u32 v39, s31, v39\n" \
  "s_waitcnt lgkmcnt(4)\n" \
  "v_mfma_f32_32x32x16_bf16 v[68:83], v[200:203], v[60:63], v[68:83]\n" \
  "v_mfma_f32_32x32x16_bf16 v[84:99], v[204:207], v[60:63], v[84:99]\n" \
  "ds_read_b128 v[60:63], v40 offset:1024\n" \
  "v_add_u32 v40, s31, v40\n" \
  "s_waitcnt lgkmcnt(4)\n" \
  "v_mfma_f32_32x32x16_bf16 v[68:83], v[208:211], v[64:67], v[68:83]\n" \
  "v_mfma_f32_32x32x16_bf16 v[84:99], v[212:215], v[64:67], v[84:99]\n" \
  "s_add_u32 s33, s33, s34\n" \
  "v_mov_b32 v42, v43\n" \
  "ds_read_b128 v[64:67], v41 offset:1024\n" \
  "v_add_u32 v41, s31, v41\n" \
  "s_waitcnt lgkmcnt(4)\n" \
  "v_mfma_f32_32x32x16_bf16 v[68:83], v[216:219], v[48:51], v[68:83]\n" \
  "v_mfma_f32_32x32x16_bf16 v[84:99], v[220:223], v[48:51], v[84:99]\n" \
  "s_waitcnt lgkmcnt(0)\n" \
  "s_waitcnt vmcnt(10)\n" \
  "s_barrier\n" \
  "s_mov_b32 exec_hi, 0\n" \
  "buffer_store_dword v44, v132, s[8:11], s35 offen sc1\n" \
  "s_mov_b32 exec_hi, -1\n" \
  "v_add_u32 v44, 1, v44\n" \
  "ds_read_b128 v[48:51], v34\n" \
  "v_mfma_f32_32x32x16_bf16 v[68:83], v[224:227], v[52:55], v[68:83]\n" \
  "v_mfma_f32_32x32x16_bf16 v[84:99], v[228:231], v[52:55], v[84:99]\n" \
  "ds_read_b128 v[52:55], v35\n" \
  "v_mfma_f32_32x32x16_bf16 v[68:83], v[232:235], v[56:59], v[68:83]\n" \
  "v_mfma_f32_32x32x16_bf16 v[84:99], v[236:239], v[56:59], v[84:99]\n" \
  "ds_read_b128 v[56:59], v36\n" \
  "v_mfma_f32_32x32x16_bf16 v[68:83], v[240:243], v[60:63], v[68:83]\n" \
  "v_mfma_f32_32x32x16_bf16 v[84:99], v[244:247], v[60:63], v[84:99]\n" \
  "ds_read_b128 v[60:63], v37\n" \
  "v_mfma_f32_32x32x16_bf16 v[68:83], v[248:251], v[64:67], v[68:83]\n" \
  "v_mfma_f32_32x32x16_bf16 v[84:99], v[252:255], v[64:67], v[84:99]\n" \
  "s_mov_b32 s30, s29\n" \
  "s_add_u32 s29, s29, s31\n" \
  "s_add_u32 s32, s32, 40960\n" \
  "s_min_u32 s32, s32, 2580480\n" \
  "s_add_u32 s28, s28, 2\n" \
  "s_branch GL_%=\n" \
  "GE_%=:\n" \
  "s_nop 7\n" \
  "s_nop 3\n" \
  "v_mul_f32 v100, s36, v100\n" \
  "v_mul_f32 v101, s36, v101\n" \
  "v_mul_f32 v102, s36, v102\n" \
  "v_mul_f32 v103, s36, v103\n" \
  "v_mul_f32 v104, s36, v104\n" \
  "v_mul_f32 v105, s36, v105\n" \
  "v_mul_f32 v106, s36, v106\n" \
  "v_mul_f32 v107, s36, v107\n" \
  "v_cvt_pk_bf16_f32 v0, v100, v101\n" \
  "v_cvt_pk_bf16_f32 v1, v102, v103\n" \
  "v_cvt_pk_bf16_f32 v2, v104, v105\n" \
  "v_cvt_pk_bf16_f32 v3, v106, v107\n" \
  "buffer_store_dwordx4 v[0:3], v42, s[8:11], s33 offen sc1\n" \
  "v_mul_f32 v108, s36, v108\n" \
  "v_mul_f32 v109, s36, v109\n" \
  "v_mul_f32 v110, s36, v110\n" \
  "v_mul_f32 v111, s36, v111\n" \
  "v_mul_f32 v112, s36, v112\n" \
  "v_mul_f32 v113, s36, v113\n" \
  "v_mul_f32 v114, s36, v114\n" \
  "v_mul_f32 v115, s36, v115\n" \
  "v_cvt_pk_bf16_f32 v4, v108, v109\n" \
  "v_cvt_pk_bf16_f32 v5, v110, v111\n" \
  "v_cvt_pk_bf16_f32 v6, v112, v113\n" \
  "v_cvt_pk_bf16_f32 v7, v114, v115\n" \
  "buffer_store_dwordx4 v[4:7], v42, s[8:11], s33 offen offset:512 sc1\n" \
  "v_mul_f32 v116, s36, v116\n" \
  "v_mul_f32 v117, s36, v117\n" \
  "v_mul_f32 v118, s36, v118\n" \
  "v_mul_f32 v119, s36, v119\n" \
  "v_mul_f32 v120, s36, v120\n" \
  "v_mul_f32 v121, s36, v121\n" \
  "v_mul_f32 v122, s36, v122\n" \
  "v_mul_f32 v123, s36, v123\n" \
  "v_cvt_pk_bf16_f32 v8, v116, v117\n" \
  "v_cvt_pk_bf16_f32 v9, v118, v119\n" \
  "v_cvt_pk_bf16_f32 v10, v120, v121\n" \
  "v_cvt_pk_bf16_f32 v11, v122, v123\n" \
  "buffer_store_dwordx4 v[8:11], v42, s[8:11], s33 offen offset:2048 sc1\n" \
  "v_mul_f32 v124, s36, v124\n" \
  "v_mul_f32 v125, s36, v125\n" \
  "v_mul_f32 v126, s36, v126\n" \
  "v_mul_f32 v127, s36, v127\n" \
  "v_mul_f32 v128, s36, v128\n" \
  "v_mul_f32 v129, s36, v129\n" \
  "v_mul_f32 v130, s36, v130\n" \
  "v_mul_f32 v131, s36, v131\n" \
  "v_cvt_pk_bf16_f32 v12, v124, v125\n" \
  "v_cvt_pk_bf16_f32 v13, v126, v127\n" \
  "v_cvt_pk_bf16_f32 v14, v128, v129\n" \
  "v_cvt_pk_bf16_f32 v15, v130, v131\n" \
  "buffer_store_dwordx4 v[12:15], v42, s[8:11], s33 offen offset:2560 sc1\n" \
  "s_waitcnt vmcnt(0)\n" \
  "s_waitcnt lgkmcnt(0)\n" \
  "s_mov_b32 exec_hi, 0\n" \
  "buffer_store_dword v44, v132, s[8:11], s35 offen sc1\n" \
  "s_mov_b32 exec_hi, -1\n" \
  "s_waitcnt vmcnt(0)\n"

#define ASM_GK_0 \
  "s_add_u32 m0, s25, 0\n" \
  "s_nop 0\n" \
  "buffer_load_dwordx4 v45, s[16:19], s26 offen lds\n" \
  "buffer_load_dwordx4 v46, s[20:23], s26 offen offset:1024 lds\n" \
  "s_add_u32 s26, s26, s27\n" \
  "s_add_u32 m0, s25, 2048\n" \
  "s_nop 0\n" \
  "buffer_load_dwordx4 v45, s[16:19], s26 offen lds\n" \
  "buffer_load_dwordx4 v46, s[20:23], s26 offen offset:1024 lds\n" \
  "s_add_u32 s26, s26, s27\n" \
  "s_add_u32 m0, s25, 4096\n" \
  "s_nop 0\n" \
  "buffer_load_dwordx4 v45, s[16:19], s26 offen lds\n" \
  "buffer_load_dwordx4 v46, s[20:23], s26 offen offset:1024 lds\n" \
  "s_add_u32 s26, s26, s27\n" \
  "s_add_u32 m0, s25, 6144\n" \
  "s_nop 0\n" \
  "buffer_load_dwordx4 v45, s[16:19], s26 offen lds\n" \
  "buffer_load_dwordx4 v46, s[20:23], s26 offen offset:1024 lds\n" \
  "s_add_u32 s26, s26, s27\n" \
  "s_add_u32 m0, s25, 8192\n" \
  "s_nop 0\n" \
  "buffer_load_dwordx4 v45, s[16:19], s26 offen lds\n" \
  "buffer_load_dwordx4 v46, s[20:23], s26 offen offset:1024 lds\n" \
  "s_add_u32 s26, s26, s27\n" \
  "s_add_u32 m0, s25, 10240\n" \
  "s_nop 0\n" \
  "buffer_load_dwordx4 v45, s[16:19], s26 offen lds\n" \
  "buffer_load_dwordx4 v46, s[20:23], s26 offen offset:1024 lds\n" \
  "s_add_u32 s26, s26, s27\n" \
  "s_add_u32 m0, s25, 12288\n" \
  "s_nop 0\n" \
  "buffer_load_dwordx4 v45, s[16:19], s26 offen lds\n" \
  "buffer_load_dwordx4 v46, s[20:23], s26 offen offset:1024 lds\n" \
  "s_add_u32 s26, s26, s27\n" \
  "s_add_u32 m0, s25, 14336\n" \
  "s_nop 0\n" \
  "buffer_load_dwordx4 v45, s[16:19], s26 offen lds\n" \
  "buffer_load_dwordx4 v46, s[20:23], s26 offen offset:1024 lds\n" \
  "s_add_u32 s26, s26, s27\n" \
  "s_add_u32 m0, s25, 16384\n" \
  "s_nop 0\n" \
  "buffer_load_dwordx4 v45, s[16:19], s26 offen lds\n" \
  "buffer_load_dwordx4 v46, s[20:23], s26 offen offset:1024 lds\n" \
  "s_add_u32 s26, s26, s27\n" \
  "s_add_u32 m0, s25, 18432\n" \
  "s_nop 0\n" \
  "buffer_load_dwordx4 v45, s[16:19], s26 offen lds\n" \
  "buffer_load_dwordx4 v46, s[20:23], s26 offen offset:1024 lds\n" \
  "s_add_u32 s26, s26, s27\n" \
  "s_add_u32 m0, s25, 20480\n" \
  "s_nop 0\n" \
  "buffer_load_dwordx4 v45, s[16:19], s26 offen lds\n" \
  "buffer_load_dwordx4 v46, s[20:23], s26 offen offset:1024 lds\n" \
  "s_add_u32 s26, s26, s27\n" \
  "s_add_u32 m0, s25, 22528\n" \
  "s_nop 0\n" \
  "buffer_load_dwordx4 v45, s[16:19], s26 offen lds\n" \
  "buffer_load_dwordx4 v46, s[20:23], s26 offen offset:1024 lds\n" \
  "s_add_u32 s26, s26, s27\n" \
  "s_add_u32 m0, s25, 24576\n" \
  "s_nop 0\n" \
  "buffer_load_dwordx4 v45, s[16:19], s26 offen lds\n" \
  "buffer_load_dwordx4 v46, s[20:23], s26 offen offset:1024 lds\n" \
  "s_add_u32 s26, s26, s27\n" \
  "s_add_u32 m0, s25, 26624\n" \
  "s_nop 0\n" \
  "buffer_load_dwordx4 v45, s[16:19], s26 offen lds\n" \
  "buffer_load_dwordx4 v46, s[20:23], s26 offen offset:1024 lds\n" \
  "s_add_u32 s26, s26, s27\n" \
  "s_add_u32 m0, s25, 28672\n" \
  "s_nop 0\n" \
  "buffer_load_dwordx4 v45, s[16:19], s26 offen lds\n" \
  "buffer_load_dwordx4 v46, s[20:23], s26 offen offset:1024 lds\n" \
  "s_add_u32 s26, s26, s27\n"

#define ASM_GV \
  "v_mbcnt_lo_u32_b32 v132, -1, 0\n" \
  "v_lshlrev_b32 v132, 2, v132\n" \
  "s_add_u32 s37, s24, s30\n" \
  "s_add_u32 m0, s37, 0\n" \
  "s_nop 0\n" \
  "buffer_load_dwordx4 v24, s[12:15], s32 offen lds\n" \
  "buffer_load_dwordx4 v25, s[12:15], s32 offen offset:1024 lds\n" \
  "buffer_load_dwordx4 v26, s[12:15], s32 offen offset:2048 lds\n" \
  "buffer_load_dwordx4 v27, s[12:15], s32 offen offset:3072 lds\n" \
  "s_add_u32 s37, s24, s30\n" \
  "s_add_u32 m0, s37, 4096\n" \
  "s_nop 0\n" \
  "buffer_load_dwordx4 v28, s[12:15], s32 offen lds\n" \
  "buffer_load_dwordx4 v29, s[12:15], s32 offen offset:1024 lds\n" \
  "buffer_load_dwordx4 v30, s[12:15], s32 offen offset:2048 lds\n" \
  "buffer_load_dwordx4 v31, s[12:15], s32 offen offset:3072 lds\n" \
  "s_add_u32 s37, s24, s30\n" \
  "s_add_u32 m0, s37, 8192\n" \
  "s_nop 0\n" \
  "buffer_load_dwordx4 v32, s[12:15], s32 offen lds\n" \
  "buffer_load_dwordx4 v33, s[12:15], s32 offen offset:1024 lds\n" \
  "s_mov_b32 s31, 40960\n" \
  "s_waitcnt vmcnt(38)\n" \
  "ds_read_b64_tr_b16 a[0:1], v47\n" \
  "ds_read_b64_tr_b16 a[2:3], v47 offset:512\n" \
  "ds_read_b64_tr_b16 a[4:5], v47 offset:64\n" \
  "ds_read_b64_tr_b16 a[6:7], v47 offset:576\n" \
  "s_waitcnt vmcnt(36)\n" \
  "ds_read_b64_tr_b16 a[8:9], v47 offset:2048\n" \
  "ds_read_b64_tr_b16 a[10:11], v47 offset:2560\n" \
  "ds_read_b64_tr_b16 a[12:13], v47 offset:2112\n" \
  "ds_read_b64_tr_b16 a[14:15], v47 offset:2624\n" \
  "s_waitcnt lgkmcnt(4)\n" \
  "s_add_u32 m0, s25, 0\n" \
  "s_nop 0\n" \
  "buffer_load_dwordx4 v45, s[16:19], s26 offen lds\n" \
  "buffer_load_dwordx4 v46, s[20:23], s26 offen offset:1024 lds\n" \
  "s_add_u32 s26, s26, s27\n" \
  "s_waitcnt vmcnt(36)\n" \
  "ds_read_b64_tr_b16 a[16:17], v47 offset:4096\n" \
  "ds_read_b64_tr_b16 a[18:19], v47 offset:4608\n" \
  "ds_read_b64_tr_b16 a[20:21], v47 offset:4160\n" \
  "ds_read_b64_tr_b16 a[22:23], v47 offset:4672\n" \
  "s_waitcnt lgkmcnt(4)\n" \
  "s_add_u32 m0, s25, 2048\n" \
  "s_nop 0\n" \
  "buffer_load_dwordx4 v45, s[16:19], s26 offen lds\n" \
  "buffer_load_dwordx4 v46, s[20:23], s26 offen offset:1024 lds\n" \
  "s_add_u32 s26, s26, s27\n" \
  "s_waitcnt vmcnt(36)\n" \
  "ds_read_b64_tr_b16 a[24:25], v47 offset:6144\n" \
  "ds_read_b64_tr_b16 a[26:27], v47 offset:6656\n" \
  "ds_read_b64_tr_b16 a[28:29], v47 offset:6208\n" \
  "ds_read_b64_tr_b16 a[30:31], v47 offset:6720\n" \
  "s_waitcnt lgkmcnt(4)\n" \
  "s_add_u32 m0, s25, 4096\n" \
  "s_nop 0\n" \
  "buffer_load_dwordx4 v45, s[16:19], s26 offen lds\n" \
  "buffer_load_dwordx4 v46, s[20:23], s26 offen offset:1024 lds\n" \
  "s_add_u32 s26, s26, s27\n" \
  "s_waitcnt vmcnt(36)\n" \
  "ds_read_b64_tr_b16 a[32:33], v47 offset:8192\n" \
  "ds_read_b64_tr_b16 a[34:35], v47 offset:8704\n" \
  "ds_read_b64_tr_b16 a[36:37], v47 offset:8256\n" \
  "ds_read_b64_tr_b16 a[38:39], v47 offset:8768\n" \
  "s_waitcnt lgkmcnt(4)\n" \
  "s_add_u32 m0, s25, 6144\n" \
  "s_nop 0\n" \
  "buffer_load_dwordx4 v45, s[16:19], s26 offen lds\n" \
  "buffer_load_dwordx4 v46, s[20:23], s26 offen offset:1024 lds\n" \
  "s_add_u32 s26, s26, s27\n" \
  "s_waitcnt vmcnt(36)\n" \
  "ds_read_b64_tr_b16 a[40:41], v47 offset:10240\n" \
  "ds_read_b64_tr_b16 a[42:43], v47 offset:10752\n" \
  "ds_read_b64_tr_b16 a[44:45], v47 offset:10304\n" \
  "ds_read_b64_tr_b16 a[46:47], v47 offset:10816\n" \
  "s_waitcnt lgkmcnt(4)\n" \
  "s_add_u32 m0, s25, 8192\n" \
  "s_nop 0\n" \
  "buffer_load_dwordx4 v45, s[16:19], s26 offen lds\n" \
  "buffer_load_dwordx4 v46, s[20:23], s26 offen offset:1024 lds\n" \
  "s_add_u32 s26, s26, s27\n" \
  "s_waitcnt vmcnt(36)\n" \
  "ds_read_b64_tr_b16 a[48:49], v47 offset:12288\n" \
  "ds_read_b64_tr_b16 a[50:51], v47 offset:12800\n" \
  "ds_read_b64_tr_b16 a[52:53], v47 offset:12352\n" \
  "ds_read_b64_tr_b16 a[54:55], v47 offset:12864\n" \
  "s_waitcnt lgkmcnt(4)\n" \
  "s_add_u32 m0, s25, 10240\n" \
  "s_nop 0\n" \
  "buffer_load_dwordx4 v45, s[16:19], s26 offen lds\n" \
  "buffer_load_dwordx4 v46, s[20:23], s26 offen offset:1024 lds\n" \
  "s_add_u32 s26, s26, s27\n" \
  "s_waitcnt vmcnt(36)\n" \
  "ds_read_b64_tr_b16 a[56:57], v47 offset:14336\n" \
  "ds_read_b64_tr_b16 a[58:59], v47 offset:14848\n" \
  "ds_read_b64_tr_b16 a[60:61], v47 offset:14400\n" \
  "ds_read_b64_tr_b16 a[62:63], v47 offset:14912\n" \
  "s_waitcnt lgkmcnt(4)\n" \
  "s_add_u32 m0, s25, 12288\n" \
  "s_nop 0\n" \
  "buffer_load_dwordx4 v45, s[16:19], s26 offen lds\n" \
  "buffer_load_dwordx4 v46, s[20:23], s26 offen offset:1024 lds\n" \
  "s_add_u32 s26, s26, s27\n" \
  "s_waitcnt vmcnt(36)\n" \
  "ds_read_b64_tr_b16 a[64:65], v47 offset:16384\n" \
  "ds_read_b64_tr_b16 a[66:67], v47 offset:16896\n" \
  "ds_read_b64_tr_b16 a[68:69], v47 offset:16448\n" \
  "ds_read_b64_tr_b16 a[70:71], v47 offset:16960\n" \
  "s_waitcnt lgkmcnt(4)\n" \
  "s_add_u32 m0, s25, 14336\n" \
  "s_nop 0\n" \
  "buffer_load_dwordx4 v45, s[16:19], s26 offen lds\n" \
  "buffer_load_dwordx4 v46, s[20:23], s26 offen offset:1024 lds\n" \
  "s_add_u32 s26, s26, s27\n" \
  "s_waitcnt vmcnt(36)\n" \
  "ds_read_b64_tr_b16 a[72:73], v47 offset:18432\n" \
  "ds_read_b64_tr_b16 a[74:75], v47 offset:18944\n" \
  "ds_read_b64_tr_b16 a[76:77], v47 offset:18496\n" \
  "ds_read_b64_tr_b16 a[78:79], v47 offset:19008\n" \
  "s_waitcnt lgkmcnt(4)\n" \
  "s_add_u32 m0, s25, 16384\n" \
  "s_nop 0\n" \
  "buffer_load_dwordx4 v45, s[16:19], s26 offen lds\n" \
  "buffer_load_dwordx4 v46, s[20:23], s26 offen offset:1024 lds\n" \
  "s_add_u32 s26, s26, s27\n" \
  "s_waitcnt vmcnt(36)\n" \
  "ds_read_b64_tr_b16 a[80:81], v47 offset:20480\n" \
  "ds_read_b64_tr_b16 a[82:83], v47 offset:20992\n" \
  "ds_read_b64_tr_b16 a[84:85], v47 offset:20544\n" \
  "ds_read_b64_tr_b16 a[86:87], v47 offset:21056\n" \
  "s_waitcnt lgkmcnt(4)\n" \
  "s_add_u32 m0, s25, 18432\n" \
  "s_nop 0\n" \
  "buffer_load_dwordx4 v45, s[16:19], s26 offen lds\n" \
  "buffer_load_dwordx4 v46, s[20:23], s26 offen offset:1024 lds\n" \
  "s_add_u32 s26, s26, s27\n" \
  "s_waitcnt vmcnt(36)\n" \
  "ds_read_b64_tr_b16 a[88:89], v47 offset:22528\n" \
  "ds_read_b64_tr_b16 a[90:91], v47 offset:23040\n" \
  "ds_read_b64_tr_b16 a[92:93], v47 offset:22592\n" \
  "ds_read_b64_tr_b16 a[94:95], v47 offset:23104\n" \
  "s_waitcnt lgkmcnt(4)\n" \
  "s_add_u32 m0, s25, 20480\n" \
  "s_nop 0\n" \
  "buffer_load_dwordx4 v45, s[16:19], s26 offen lds\n" \
  "buffer_load_dwordx4 v46, s[20:23], s26 offen offset:1024 lds\n" \
  "s_add_u32 s26, s26, s27\n" \
  "s_waitcnt vmcnt(36)\n" \
  "ds_read_b64_tr_b16 a[96:97], v47 offset:24576\n" \
  "ds_read_b64_tr_b16 a[98:99], v47 offset:25088\n" \
  "ds_read_b64_tr_b16 a[100:101], v47 offset:24640\n" \
  "ds_read_b64_tr_b16 a[102:103], v47 offset:25152\n" \
  "s_waitcnt lgkmcnt(4)\n" \
  "s_add_u32 m0, s25, 22528\n" \
  "s_nop 0\n" \
  "buffer_load_dwordx4 v45, s[16:19], s26 offen lds\n" \
  "buffer_load_dwordx4 v46, s[20:23], s26 offen offset:1024 lds\n" \
  "s_add_u32 s26, s26, s27\n" \
  "s_waitcnt vmcnt(36)\n" \
  "ds_read_b64_tr_b16 a[104:105], v47 offset:26624\n" \
  "ds_read_b64_tr_b16 a[106:107], v47 offset:27136\n" \
  "ds_read_b64_tr_b16 a[108:109], v47 offset:26688\n" \
  "ds_read_b64_tr_b16 a[110:111], v47 offset:27200\n" \
  "s_waitcnt lgkmcnt(4)\n" \
  "s_add_u32 m0, s25, 24576\n" \
  "s_nop 0\n" \
  "buffer_load_dwordx4 v45, s[16:19], s26 offen lds\n" \
  "buffer_load_dwordx4 v46, s[20:23], s26 offen offset:1024 lds\n" \
  "s_add_u32 s26, s26, s27\n" \
  "s_waitcnt vmcnt(36)\n" \
  "ds_read_b64_tr_b16 a[112:113], v47 offset:28672\n" \
  "ds_read_b64_tr_b16 a[114:115], v47 offset:29184\n" \
  "ds_read_b64_tr_b16 a[116:117], v47 offset:28736\n" \
  "ds_read_b64_tr_b16 a[118:119], v47 offset:29248\n" \
  "s_waitcnt lgkmcnt(4)\n" \
  "s_add_u32 m0, s25, 26624\n" \
  "s_nop 0\n" \
  "buffer_load_dwordx4 v45, s[16:19], s26 offen lds\n" \
  "buffer_load_dwordx4 v46, s[20:23], s26 offen offset:1024 lds\n" \
  "s_add_u32 s26, s26, s27\n" \
  "s_waitcnt vmcnt(26)\n" \
  "ds_read_b64_tr_b16 a[120:121], v47\n" \
  "ds_read_b64_tr_b16 a[122:123], v47 offset:512\n" \
  "ds_read_b64_tr_b16 a[124:125], v47 offset:64\n" \
  "ds_read_b64_tr_b16 a[126:127], v47 offset:576\n" \
  "s_waitcnt lgkmcnt(4)\n" \
  "s_add_u32 m0, s25, 28672\n" \
  "s_nop 0\n" \
  "buffer_load_dwordx4 v45, s[16:19], s26 offen lds\n" \
  "buffer_load_dwordx4 v46, s[20:23], s26 offen offset:1024 lds\n" \
  "s_add_u32 s26, s26, s27\n" \
  "s_barrier\n" \
  "ds_read_b128 v[48:51], v34\n" \
  "ds_read_b128 v[52:55], v35\n" \
  "ds_read_b128 v[56:59], v36\n" \
  "ds_read_b128 v[60:63], v37\n" \
  "ds_read_b128 v[64:67], v38\n" \
  "s_waitcnt lgkmcnt(4)\n" \
  "v_mfma_f32_32x32x16_bf16 v[68:83], v[48:51], a[0:3], 0\n" \
  "v_mfma_f32_32x32x16_bf16 v[84:99], v[48:51], a[4:7], 0\n" \
  "ds_read_b128 v[48:51], v39\n" \
  "s_waitcnt lgkmcnt(4)\n" \
  "v_mfma_f32_32x32x16_bf16 v[68:83], v[52:55], a[8:11], v[68:83]\n" \
  "v_mfma_f32_32x32x16_bf16 v[84:99], v[52:55], a[12:15], v[84:99]\n" \
  "s_waitcnt vmcnt(26)\n" \
  "ds_read_b64_tr_b16 a[128:129], v47 offset:2048\n" \
  "ds_read_b64_tr_b16 a[130:131], v47 offset:2560\n" \
  "ds_read_b64_tr_b16 a[132:133], v47 offset:2112\n" \
  "ds_read_b64_tr_b16 a[134:135], v47 offset:2624\n" \
  "s_add_u32 m0, s25, 0\n" \
  "s_nop 0\n" \
  "buffer_load_dwordx4 v45, s[16:19], s26 offen lds\n" \
  "buffer_load_dwordx4 v46, s[20:23], s26 offen offset:1024 lds\n" \
  "s_add_u32 s26, s26, s27\n" \
  "ds_read_b128 v[52:55], v40\n" \
  "s_waitcnt lgkmcnt(8)\n" \
  "v_mfma_f32_32x32x16_bf16 v[68:83], v[56:59], a[16:19], v[68:83]\n" \
  "v_mfma_f32_32x32x16_bf16 v[84:99], v[56:59], a[20:23], v[84:99]\n" \
  "ds_read_b128 v[56:59], v41\n" \
  "s_waitcnt lgkmcnt(8)\n" \
  "v_mfma_f32_32x32x16_bf16 v[68:83], v[60:63], a[24:27], v[68:83]\n" \
  "v_mfma_f32_32x32x16_bf16 v[84:99], v[60:63], a[28:31], v[84:99]\n" \
  "s_waitcnt vmcnt(26)\n" \
  "ds_read_b64_tr_b16 a[136:137], v47 offset:4096\n" \
  "ds_read_b64_tr_b16 a[138:139], v47 offset:4608\n" \
  "ds_read_b64_tr_b16 a[140:141], v47 offset:4160\n" \
  "ds_read_b64_tr_b16 a[142:143], v47 offset:4672\n" \
  "s_waitcnt lgkmcnt(6)\n" \
  "s_add_u32 m0, s25, 2048\n" \
  "s_nop 0\n" \
  "buffer_load_dwordx4 v45, s[16:19], s26 offen lds\n" \
  "buffer_load_dwordx4 v46, s[20:23], s26 offen offset:1024 lds\n" \
  "s_add_u32 s26, s26, s27\n" \
  "ds_read_b128 v[60:63], v34 offset:256\n" \
  "v_mfma_f32_32x32x16_bf16 v[68:83], v[64:67], a[32:35], v[68:83]\n" \
  "v_mfma_f32_32x32x16_bf16 v[84:99], v[64:67], a[36:39], v[84:99]\n" \
  "ds_read_b128 v[64:67], v35 offset:256\n" \
  "v_mfma_f32_32x32x16_bf16 v[68:83], v[48:51], a[40:43], v[68:83]\n" \
  "v_mfma_f32_32x32x16_bf16 v[84:99], v[48:51], a[44:47], v[84:99]\n" \
  "s_waitcnt vmcnt(26)\n" \
  "ds_read_b64_tr_b16 a[144:145], v47 offset:6144\n" \
  "ds_read_b64_tr_b16 a[146:147], v47 offset:6656\n" \
  "ds_read_b64_tr_b16 a[148:149], v47 offset:6208\n" \
  "ds_read_b64_tr_b16 a[150:151], v47 offset:6720\n" \
  "s_waitcnt lgkmcnt(6)\n" \
  "s_add_u32 m0, s25, 4096\n" \
  "s_nop 0\n" \
  "buffer_load_dwordx4 v45, s[16:19], s26 offen lds\n" \
  "buffer_load_dwordx4 v46, s[20:23], s26 offen offset:1024 lds\n" \
  "s_add_u32 s26, s26, s27\n" \
  "ds_read_b128 v[48:51], v36 offset:256\n" \
  "v_mfma_f32_32x32x16_bf16 v[68:83], v[52:55], a[48:51], v[68:83]\n" \
  "v_mfma_f32_32x32x16_bf16 v[84:99], v[52:55], a[52:55], v[84:99]\n" \
  "ds_read_b128 v[52:55], v37 offset:256\n" \
  "v_mfma_f32_32x32x16_bf16 v[68:83], v[56:59], a[56:59], v[68:83]\n" \
  "v_mfma_f32_32x32x16_bf16 v[84:99], v[56:59], a[60:63], v[84:99]\n" \
  "s_waitcnt vmcnt(26)\n" \
  "ds_read_b64_tr_b16 a[152:153], v47 offset:8192\n" \
  "ds_read_b64_tr_b16 a[154:155], v47 offset:8704\n" \
  "ds_read_b64_tr_b16 a[156:157], v47 offset:8256\n" \
  "ds_read_b64_tr_b16 a[158:159], v47 offset:8768\n" \
  "s_waitcnt lgkmcnt(6)\n" \
  "s_add_u32 m0, s25, 6144\n" \
  "s_nop 0\n" \
  "buffer_load_dwordx4 v45, s[16:19], s26 offen lds\n" \
  "buffer_load_dwordx4 v46, s[20:23], s26 offen offset:1024 lds\n" \
  "s_add_u32 s26, s26, s27\n" \
  "ds_read_b128 v[56:59], v38 offset:256\n" \
  "v_mfma_f32_32x32x16_bf16 v[68:83], v[60:63], a[64:67], v[68:83]\n" \
  "v_mfma_f32_32x32x16_bf16 v[84:99], v[60:63], a[68:71], v[84:99]\n" \
  "ds_read_b128 v[60:63], v39 offset:256\n" \
  "v_mfma_f32_32x32x16_bf16 v[68:83], v[64:67], a[72:75], v[68:83]\n" \
  "v_mfma_f32_32x32x16_bf16 v[84:99], v[64:67], a[76:79], v[84:99]\n" \
  "s_waitcnt vmcnt(26)\n" \
  "ds_read_b64_tr_b16 a[160:161], v47 offset:10240\n" \
  "ds_read_b64_tr_b16 a[162:163], v47 offset:10752\n" \
  "ds_read_b64_tr_b16 a[164:165], v47 offset:10304\n" \
  "ds_read_b64_tr_b16 a[166:167], v47 offset:10816\n" \
  "s_waitcnt lgkmcnt(6)\n" \
  "s_add_u32 m0, s25, 8192\n" \
  "s_nop 0\n" \
  "buffer_load_dwordx4 v45, s[16:19], s26 offen lds\n" \
  "buffer_load_dwordx4 v46, s[20:23], s26 offen offset:1024 lds\n" \
  "s_add_u32 s26, s26, s27\n" \
  "ds_read_b128 v[64:67], v40 offset:256\n" \
  "v_mfma_f32_32x32x16_bf16 v[68:83], v[48:51], a[80:83], v[68:83]\n" \
  "v_mfma_f32_32x32x16_bf16 v[84:99], v[48:51], a[84:87], v[84:99]\n" \
  "ds_read_b128 v[48:51], v41 offset:256\n" \
  "v_mfma_f32_32x32x16_bf16 v[68:83], v[52:55], a[88:91], v[68:83]\n" \
  "v_mfma_f32_32x32x16_bf16 v[84:99], v[52:55], a[92:95], v[84:99]\n" \
  "s_waitcnt vmcnt(26)\n" \
  "ds_read_b64_tr_b16 a[168:169], v47 offset:12288\n" \
  "ds_read_b64_tr_b16 a[170:171], v47 offset:12800\n" \
  "ds_read_b64_tr_b16 a[172:173], v47 offset:12352\n" \
  "ds_read_b64_tr_b16 a[174:175], v47 offset:12864\n" \
  "s_waitcnt lgkmcnt(6)\n" \
  "s_add_u32 m0, s25, 10240\n" \
  "s_nop 0\n" \
  "buffer_load_dwordx4 v45, s[16:19], s26 offen lds\n" \
  "buffer_load_dwordx4 v46, s[20:23], s26 offen offset:1024 lds\n" \
  "s_add_u32 s26, s26, s27\n" \
  "ds_read_b128 v[52:55], v34 offset:512\n" \
  "v_mfma_f32_32x32x16_bf16 v[68:83], v[56:59], a[96:99], v[68:83]\n" \
  "v_mfma_f32_32x32x16_bf16 v[84:99], v[56:59], a[100:103], v[84:99]\n" \
  "ds_read_b128 v[56:59], v35 offset:512\n" \
  "v_mfma_f32_32x32x16_bf16 v[68:83], v[60:63], a[104:107], v[68:83]\n" \
  "v_mfma_f32_32x32x16_bf16 v[84:99], v[60:63], a[108:111], v[84:99]\n" \
  "s_waitcnt vmcnt(26)\n" \
  "ds_read_b64_tr_b16 a[176:177], v47 offset:14336\n" \
  "ds_read_b64_tr_b16 a[178:179], v47 offset:14848\n" \
  "ds_read_b64_tr_b16 a[180:181], v47 offset:14400\n" \
  "ds_read_b64_tr_b16 a[182:183], v47 offset:14912\n" \
  "s_waitcnt lgkmcnt(6)\n" \
  "s_add_u32 m0, s25, 12288\n" \
  "s_nop 0\n" \
  "buffer_load_dwordx4 v45, s[16:19], s26 offen lds\n" \
  "buffer_load_dwordx4 v46, s[20:23], s26 offen offset:1024 lds\n" \
  "s_add_u32 s26, s26, s27\n" \
  "ds_read_b128 v[60:63], v36 offset:512\n" \
  "v_mfma_f32_32x32x16_bf16 v[68:83], v[64:67], a[112:115], v[68:83]\n" \
  "v_mfma_f32_32x32x16_bf16 v[84:99], v[64:67], a[116:119], v[84:99]\n" \
  "ds_read_b128 v[64:67], v37 offset:512\n" \
  "v_mfma_f32_32x32x16_bf16 v[68:83], v[48:51], a[120:123], v[68:83]\n" \
  "v_mfma_f32_32x32x16_bf16 v[84:99], v[48:51], a[124:127], v[84:99]\n" \
  "s_waitcnt vmcnt(26)\n" \
  "ds_read_b64_tr_b16 a[184:185], v47 offset:16384\n" \
  "ds_read_b64_tr_b16 a[186:187], v47 offset:16896\n" \
  "ds_read_b64_tr_b16 a[188:189], v47 offset:16448\n" \
  "ds_read_b64_tr_b16 a[190:191], v47 offset:16960\n" \
  "s_waitcnt lgkmcnt(6)\n" \
  "s_add_u32 m0, s25, 14336\n" \
  "s_nop 0\n" \
  "buffer_load_dwordx4 v45, s[16:19], s26 offen lds\n" \
  "buffer_load_dwordx4 v46, s[20:23], s26 offen offset:1024 lds\n" \
  "s_add_u32 s26, s26, s27\n" \
  "ds_read_b128 v[48:51], v38 offset:512\n" \
  "v_mfma_f32_32x32x16_bf16 v[68:83], v[52:55], a[128:131], v[68:83]\n" \
  "v_mfma_f32_32x32x16_bf16 v[84:99], v[52:55], a[132:135], v[84:99]\n" \
  "ds_read_b128 v[52:55], v39 offset:512\n" \
  "v_mfma_f32_32x32x16_bf16 v[68:83], v[56:59], a[136:139], v[68:83]\n" \
  "v_mfma_f32_32x32x16_bf16 v[84:99], v[56:59], a[140:143], v[84:99]\n" \
  "s_waitcnt vmcnt(26)\n" \
  "ds_read_b64_tr_b16 a[192:193], v47 offset:18432\n" \
  "ds_read_b64_tr_b16 a[194:195], v47 offset:18944\n" \
  "ds_read_b64_tr_b16 a[196:197], v47 offset:18496\n" \
  "ds_read_b64_tr_b16 a[198:199], v47 offset:19008\n" \
  "s_waitcnt lgkmcnt(6)\n" \
  "s_add_u32 m0, s25, 16384\n" \
  "s_nop 0\n" \
  "buffer_load_dwordx4 v45, s[16:19], s26 offen lds\n" \
  "buffer_load_dwordx4 v46, s[20:23], s26 offen offset:1024 lds\n" \
  "s_add_u32 s26, s26, s27\n" \
  "ds_read_b128 v[56:59], v40 offset:512\n" \
  "v_mfma_f32_32x32x16_bf16 v[68:83], v[60:63], a[144:147], v[68:83]\n" \
  "v_mfma_f32_32x32x16_bf16 v[84:99], v[60:63], a[148:151], v[84:99]\n" \
  "ds_read_b128 v[60:63], v41 offset:512\n" \
  "v_mfma_f32_32x32x16_bf16 v[68:83], v[64:67], a[152:155], v[68:83]\n" \
  "v_mfma_f32_32x32x16_bf16 v[84:99], v[64:67], a[156:159], v[84:99]\n" \
  "s_waitcnt vmcnt(26)\n" \
  "ds_read_b64_tr_b16 a[200:201], v47 offset:20480\n" \
  "ds_read_b64_tr_b16 a[202:203], v47 offset:20992\n" \
  "ds_read_b64_tr_b16 a[204:205], v47 offset:20544\n" \
  "ds_read_b64_tr_b16 a[206:207], v47 offset:21056\n" \
  "s_waitcnt lgkmcnt(6)\n" \
  "s_add_u32 m0, s25, 18432\n" \
  "s_nop 0\n" \
  "buffer_load_dwordx4 v45, s[16:19], s26 offen lds\n" \
  "buffer_load_dwordx4 v46, s[20:23], s26 offen offset:1024 lds\n" \
  "s_add_u32 s26, s26, s27\n" \
  "ds_read_b128 v[64:67], v34 offset:768\n" \
  "v_mfma_f32_32x32x16_bf16 v[68:83], v[48:51], a[160:163], v[68:83]\n" \
  "v_mfma_f32_32x32x16_bf16 v[84:99], v[48:51], a[164:167], v[84:99]\n" \
  "ds_read_b128 v[48:51], v35 offset:768\n" \
  "v_mfma_f32_32x32x16_bf16 v[68:83], v[52:55], a[168:171], v[68:83]\n" \
  "v_mfma_f32_32x32x16_bf16 v[84:99], v[52:55], a[172:175], v[84:99]\n" \
  "s_waitcnt vmcnt(26)\n" \
  "ds_read_b64_tr_b16 a[208:209], v47 offset:22528\n" \
  "ds_read_b64_tr_b16 a[210:211], v47 offset:23040\n" \
  "ds_read_b64_tr_b16 a[212:213], v47 offset:22592\n" \
  "ds_read_b64_tr_b16 a[214:215], v47 offset:23104\n" \
  "ds_read_b128 v[52:55], v36 offset:768\n" \
  "s_waitcnt lgkmcnt(12)\n" \
  "v_mfma_f32_32x32x16_bf16 v[68:83], v[56:59], a[176:179], v[68:83]\n" \
  "v_mfma_f32_32x32x16_bf16 v[84:99], v[56:59], a[180:183], v[84:99]\n" \
  "ds_read_b128 v[56:59], v37 offset:768\n" \
  "s_waitcnt lgkmcnt(12)\n" \
  "v_mfma_f32_32x32x16_bf16 v[68:83], v[60:63], a[184:187], v[68:83]\n" \
  "v_mfma_f32_32x32x16_bf16 v[84:99], v[60:63], a[188:191], v[84:99]\n" \
  "s_waitcnt vmcnt(24)\n" \
  "ds_read_b64_tr_b16 a[216:217], v47 offset:24576\n" \
  "ds_read_b64_tr_b16 a[218:219], v47 offset:25088\n" \
  "ds_read_b64_tr_b16 a[220:221], v47 offset:24640\n" \
  "ds_read_b64_tr_b16 a[222:223], v47 offset:25152\n" \
  "ds_read_b128 v[60:63], v38 offset:768\n" \
  "s_waitcnt lgkmcnt(12)\n" \
  "v_mfma_f32_32x32x16_bf16 v[68:83], v[64:67], a[192:195], v[68:83]\n" \
  "v_mfma_f32_32x32x16_bf16 v[84:99], v[64:67], a[196:199], v[84:99]\n" \
  "ds_read_b128 v[64:67], v39 offset:768\n" \
  "s_waitcnt lgkmcnt(12)\n" \
  "v_mfma_f32_32x32x16_bf16 v[68:83], v[48:51], a[200:203], v[68:83]\n" \
  "v_mfma_f32_32x32x16_bf16 v[84:99], v[48:51], a[204:207], v[84:99]\n" \
  "s_waitcnt vmcnt(22)\n" \
  "ds_read_b64_tr_b16 a[224:225], v47 offset:26624\n" \
  "ds_read_b64_tr_b16 a[226:227], v47 offset:27136\n" \
  "ds_read_b64_tr_b16 a[228:229], v47 offset:26688\n" \
  "ds_read_b64_tr_b16 a[230:231], v47 offset:27200\n" \
  "ds_read_b128 v[48:51], v40 offset:768\n" \
  "s_waitcnt lgkmcnt(12)\n" \
  "v_mfma_f32_32x32x16_bf16 v[68:83], v[52:55], a[208:211], v[68:83]\n" \
  "v_mfma_f32_32x32x16_bf16 v[84:99], v[52:55], a[212:215], v[84:99]\n" \
  "ds_read_b128 v[52:55], v41 offset:768\n" \
  "s_waitcnt lgkmcnt(10)\n" \
  "v_mfma_f32_32x32x16_bf16 v[68:83], v[56:59], a[216:219], v[68:83]\n" \
  "s_waitcnt lgkmcnt(8)\n" \
  "v_mfma_f32_32x32x16_bf16 v[84:99], v[56:59], a[220:223], v[84:99]\n" \
  "s_waitcnt vmcnt(20)\n" \
  "ds_read_b64_tr_b16 a[232:233], v47 offset:28672\n" \
  "ds_read_b64_tr_b16 a[234:235], v47 offset:29184\n" \
  "ds_read_b64_tr_b16 a[236:237], v47 offset:28736\n" \
  "ds_read_b64_tr_b16 a[238:239], v47 offset:29248\n" \
  "ds_read_b128 v[56:59], v34 offset:1024\n" \
  "v_add_u32 v34, s31, v34\n" \
  "s_waitcnt lgkmcnt(9)\n" \
  "v_mfma_f32_32x32x16_bf16 v[68:83], v[60:63], a[224:227], v[68:83]\n" \
  "s_waitcnt lgkmcnt(7)\n" \
  "v_mfma_f32_32x32x16_bf16 v[84:99], v[60:63], a[228:231], v[84:99]\n" \
  "s_waitcnt vmcnt(18)\n" \
  "ds_read_b64_tr_b16 a[240:241], v47\n" \
  "ds_read_b64_tr_b16 a[242:243], v47 offset:512\n" \
  "ds_read_b64_tr_b16 a[244:245], v47 offset:64\n" \
  "ds_read_b64_tr_b16 a[246:247], v47 offset:576\n" \
  "ds_read_b128 v[60:63], v35 offset:1024\n" \
  "v_add_u32 v35, s31, v35\n" \
  "s_waitcnt lgkmcnt(8)\n" \
  "v_mfma_f32_32x32x16_bf16 v[68:83], v[64:67], a[232:235], v[68:83]\n" \
  "s_waitcnt lgkmcnt(6)\n" \
  "v_mfma_f32_32x32x16_bf16 v[84:99], v[64:67], a[236:239], v[84:99]\n" \
  "s_waitcnt vmcnt(16)\n" \
  "ds_read_b64_tr_b16 a[248:249], v47 offset:2048\n" \
  "ds_read_b64_tr_b16 a[250:251], v47 offset:2560\n" \
  "ds_read_b64_tr_b16 a[252:253], v47 offset:2112\n" \
  "ds_read_b64_tr_b16 a[254:255], v47 offset:2624\n" \
  "ds_read_b128 v[64:67], v36 offset:1024\n" \
  "v_add_u32 v36, s31, v36\n" \
  "s_waitcnt lgkmcnt(8)\n" \
  "v_mfma_f32_32x32x16_bf16 v[68:83], v[48:51], a[240:243], v[68:83]\n" \
  "s_waitcnt lgkmcnt(6)\n" \
  "v_mfma_f32_32x32x16_bf16 v[84:99], v[48:51], a[244:247], v[84:99]\n" \
  "s_waitcnt vmcnt(14)\n" \
  "ds_read_b64_tr_b16 v[192:193], v47 offset:4096\n" \
  "ds_read_b64_tr_b16 v[194:195], v47 offset:4608\n" \
  "ds_read_b64_tr_b16 v[196:197], v47 offset:4160\n" \
  "ds_read_b64_tr_b16 v[198:199], v47 offset:4672\n" \
  "ds_read_b128 v[48:51], v37 offset:1024\n" \
  "v_add_u32 v37, s31, v37\n" \
  "s_waitcnt lgkmcnt(8)\n" \
  "v_mfma_f32_32x32x16_bf16 v[68:83], v[52:55], a[248:251], v[68:83]\n" \
  "s_waitcnt lgkmcnt(6)\n" \
  "v_mfma_f32_32x32x16_bf16 v[84:99], v[52:55], a[252:255], v[84:99]\n" \
  "s_waitcnt vmcnt(12)\n" \
  "ds_read_b64_tr_b16 v[200:201], v47 offset:6144\n" \
  "ds_read_b64_tr_b16 v[202:203], v47 offset:6656\n" \
  "ds_read_b64_tr_b16 v[204:205], v47 offset:6208\n" \
  "ds_read_b64_tr_b16 v[206:207], v47 offset:6720\n" \
  "ds_read_b128 v[52:55], v38 offset:1024\n" \
  "v_add_u32 v38, s31, v38\n" \
  "s_waitcnt lgkmcnt(8)\n" \
  "v_mfma_f32_32x32x16_bf16 v[68:83], v[56:59], v[192:195], v[68:83]\n" \
  "s_waitcnt lgkmcnt(6)\n" \
  "v_mfma_f32_32x32x16_bf16 v[84:99], v[56:59], v[196:199], v[84:99]\n" \
  "s_waitcnt vmcnt(10)\n" \
  "ds_read_b64_tr_b16 v[208:209], v47 offset:8192\n" \
  "ds_read_b64_tr_b16 v[210:211], v47 offset:8704\n" \
  "ds_read_b64_tr_b16 v[212:213], v47 offset:8256\n" \
  "ds_read_b64_tr_b16 v[214:215], v47 offset:8768\n" \
  "ds_read_b128 v[56:59], v39 offset:1024\n" \
  "v_add_u32 v39, s31, v39\n" \
  "s_waitcnt lgkmcnt(8)\n" \
  "v_mfma_f32_32x32x16_bf16 v[68:83], v[60:63], v[200:203], v[68:83]\n" \
  "s_waitcnt lgkmcnt(6)\n" \
  "v_mfma_f32_32x32x16_bf16 v[84:99], v[60:63], v[204:207], v[84:99]\n" \
  "s_waitcnt vmcnt(8)\n" \
  "ds_read_b64_tr_b16 v[216:217], v47 offset:10240\n" \
  "ds_read_b64_tr_b16 v[218:219], v47 offset:10752\n" \
  "ds_read_b64_tr_b16 v[220:221], v47 offset:10304\n" \
  "ds_read_b64_tr_b16 v[222:223], v47 offset:10816\n" \
  "ds_read_b128 v[60:63], v40 offset:1024\n" \
  "v_add_u32 v40, s31, v40\n" \
  "s_waitcnt lgkmcnt(8)\n" \
  "v_mfma_f32_32x32x16_bf16 v[68:83], v[64:67], v[208:211], v[68:83]\n" \
  "s_waitcnt lgkmcnt(6)\n" \
  "v_mfma_f32_32x32x16_bf16 v[84:99], v[64:67], v[212:215], v[84:99]\n" \
  "s_waitcnt vmcnt(6)\n" \
  "ds_read_b64_tr_b16 v[224:225], v47 offset:12288\n" \
  "ds_read_b64_tr_b16 v[226:227], v47 offset:12800\n" \
  "ds_read_b64_tr_b16 v[228:229], v47 offset:12352\n" \
  "ds_read_b64_tr_b16 v[230:231], v47 offset:12864\n" \
  "ds_read_b128 v[64:67], v41 offset:1024\n" \
  "v_add_u32 v41, s31, v41\n" \
  "s_waitcnt lgkmcnt(8)\n" \
  "v_mfma_f32_32x32x16_bf16 v[68:83], v[48:51], v[216:219], v[68:83]\n" \
  "s_waitcnt lgkmcnt(6)\n" \
  "v_mfma_f32_32x32x16_bf16 v[84:99], v[48:51], v[220:223], v[84:99]\n" \
  "s_waitcnt vmcnt(4)\n" \
  "ds_read_b64_tr_b16 v[232:233], v47 offset:14336\n" \
  "ds_read_b64_tr_b16 v[234:235], v47 offset:14848\n" \
  "ds_read_b64_tr_b16 v[236:237], v47 offset:14400\n" \
  "ds_read_b64_tr_b16 v[238:239], v47 offset:14912\n" \
  "s_waitcnt lgkmcnt(7)\n" \
  "v_mfma_f32_32x32x16_bf16 v[68:83], v[52:55], v[224:227], v[68:83]\n" \
  "s_waitcnt lgkmcnt(5)\n" \
  "v_mfma_f32_32x32x16_bf16 v[84:99], v[52:55], v[228:231], v[84:99]\n" \
  "s_waitcnt vmcnt(2)\n" \
  "ds_read_b64_tr_b16 v[240:241], v47 offset:16384\n" \
  "ds_read_b64_tr_b16 v[242:243], v47 offset:16896\n" \
  "ds_read_b64_tr_b16 v[244:245], v47 offset:16448\n" \
  "ds_read_b64_tr_b16 v[246:247], v47 offset:16960\n" \
  "s_waitcnt lgkmcnt(6)\n" \
  "v_mfma_f32_32x32x16_bf16 v[68:83], v[56:59], v[232:235], v[68:83]\n" \
  "s_waitcnt lgkmcnt(4)\n" \
  "v_mfma_f32_32x32x16_bf16 v[84:99], v[56:59], v[236:239], v[84:99]\n" \
  "s_waitcnt vmcnt(0)\n" \
  "ds_read_b64_tr_b16 v[248:249], v47 offset:18432\n" \
  "ds_read_b64_tr_b16 v[250:251], v47 offset:18944\n" \
  "ds_read_b64_tr_b16 v[252:253], v47 offset:18496\n" \
  "ds_read_b64_tr_b16 v[254:255], v47 offset:19008\n" \
  "s_waitcnt lgkmcnt(6)\n" \
  "v_mfma_f32_32x32x16_bf16 v[68:83], v[60:63], v[240:243], v[68:83]\n" \
  "s_waitcnt lgkmcnt(4)\n" \
  "v_mfma_f32_32x32x16_bf16 v[84:99], v[60:63], v[244:247], v[84:99]\n" \
  "s_waitcnt lgkmcnt(2)\n" \
  "v_mfma_f32_32x32x16_bf16 v[68:83], v[64:67], v[248:251], v[68:83]\n" \
  "s_waitcnt lgkmcnt(0)\n" \
  "v_mfma_f32_32x32x16_bf16 v[84:99], v[64:67], v[252:255], v[84:99]\n" \
  "s_mov_b32 s37, 647168\n" \
  "buffer_load_dwordx2 v[20:21], off, s[8:11], s37 sc1\n" \
  "s_nop 1\n" \
  "v_cvt_pk_bf16_f32 v0, v68, v69\n" \
  "v_cvt_pk_bf16_f32 v1, v70, v71\n" \
  "v_cvt_pk_bf16_f32 v2, v72, v73\n" \
  "v_cvt_pk_bf16_f32 v3, v74, v75\n" \
  "buffer_store_dwordx4 v[0:3], v42, s[8:11], s33 offen sc1\n" \
  "v_cvt_pk_bf16_f32 v4, v76, v77\n" \
  "v_cvt_pk_bf16_f32 v5, v78, v79\n" \
  "v_cvt_pk_bf16_f32 v6, v80, v81\n" \
  "v_cvt_pk_bf16_f32 v7, v82, v83\n" \
  "buffer_store_dwordx4 v[4:7], v42, s[8:11], s33 offen offset:512 sc1\n" \
  "v_cvt_pk_bf16_f32 v8, v84, v85\n" \
  "v_cvt_pk_bf16_f32 v9, v86, v87\n" \
  "v_cvt_pk_bf16_f32 v10, v88, v89\n" \
  "v_cvt_pk_bf16_f32 v11, v90, v91\n" \
  "buffer_store_dwordx4 v[8:11], v42, s[8:11], s33 offen offset:2048 sc1\n" \
  "v_cvt_pk_bf16_f32 v12, v92, v93\n" \
  "v_cvt_pk_bf16_f32 v13, v94, v95\n" \
  "v_cvt_pk_bf16_f32 v14, v96, v97\n" \
  "v_cvt_pk_bf16_f32 v15, v98, v99\n" \
  "buffer_store_dwordx4 v[12:15], v42, s[8:11], s33 offen offset:2560 sc1\n" \
  "s_barrier\n" \
  "s_mov_b32 s30, 40960\n" \
  "s_mov_b32 s32, 40960\n" \
  "s_add_u32 s37, s24, s30\n" \
  "s_add_u32 m0, s37, 0\n" \
  "s_nop 0\n" \
  "buffer_load_dwordx4 v24, s[12:15], s32 offen lds\n" \
  "buffer_load_dwordx4 v25, s[12:15], s32 offen offset:1024 lds\n" \
  "buffer_load_dwordx4 v26, s[12:15], s32 offen offset:2048 lds\n" \
  "buffer_load_dwordx4 v27, s[12:15], s32 offen offset:3072 lds\n" \
  "s_add_u32 s37, s24, s30\n" \
  "s_add_u32 m0, s37, 4096\n" \
  "s_nop 0\n" \
  "buffer_load_dwordx4 v28, s[12:15], s32 offen lds\n" \
  "buffer_load_dwordx4 v29, s[12:15], s32 offen offset:1024 lds\n" \
  "buffer_load_dwordx4 v30, s[12:15], s32 offen offset:2048 lds\n" \
  "buffer_load_dwordx4 v31, s[12:15], s32 offen offset:3072 lds\n" \
  "s_add_u32 s37, s24, s30\n" \
  "s_add_u32 m0, s37, 8192\n" \
  "s_nop 0\n" \
  "buffer_load_dwordx4 v32, s[12:15], s32 offen lds\n" \
  "buffer_load_dwordx4 v33, s[12:15], s32 offen offset:1024 lds\n" \
  "s_mov_b32 s30, 81920\n" \
  "s_mov_b32 s32, 81920\n" \
  "s_add_u32 s37, s24, s30\n" \
  "s_add_u32 m0, s37, 0\n" \
  "s_nop 0\n" \
  "buffer_load_dwordx4 v24, s[12:15], s32 offen lds\n" \
  "buffer_load_dwordx4 v25, s[12:15], s32 offen offset:1024 lds\n" \
  "buffer_load_dwordx4 v26, s[12:15], s32 offen offset:2048 lds\n" \
  "buffer_load_dwordx4 v27, s[12:15], s32 offen offset:3072 lds\n" \
  "s_add_u32 s37, s24, s30\n" \
  "s_add_u32 m0, s37, 4096\n" \
  "s_nop 0\n" \
  "buffer_load_dwordx4 v28, s[12:15], s32 offen lds\n" \
  "buffer_load_dwordx4 v29, s[12:15], s32 offen offset:1024 lds\n" \
  "buffer_load_dwordx4 v30, s[12:15], s32 offen offset:2048 lds\n" \
  "buffer_load_dwordx4 v31, s[12:15], s32 offen offset:3072 lds\n" \
  "s_add_u32 s37, s24, s30\n" \
  "s_add_u32 m0, s37, 8192\n" \
  "s_nop 0\n" \
  "buffer_load_dwordx4 v32, s[12:15], s32 offen lds\n" \
  "buffer_load_dwordx4 v33, s[12:15], s32 offen offset:1024 lds\n" \
  "s_mov_b32 s30, 0\n" \
  "s_mov_b32 s32, 122880\n" \
  "s_mov_b32 s29, 40960\n" \
  "s_mov_b32 s37, 647168\n" \
  "s_waitcnt vmcnt(20)\n" \
  "v_mov_b32 v42, 0x80000000\n" \
  "GA_%=:\n" \
  "v_or_b32 v21, 0x3fff, v21\n" \
  "v_add_u32 v21, 1, v21\n" \
  "v_cmp_ne_u32 vcc, v20, v21\n" \
  "s_cbranch_vccz GAG_%=\n" \
  "s_sleep 2\n" \
  "buffer_load_dwordx2 v[20:21], off, s[8:11], s37 sc1\n" \
  "s_waitcnt vmcnt(0)\n" \
  "s_sub_u32 s38, s38, 1\n" \
  "s_cbranch_scc0 GA_%=\n" \
  "GAG_%=:\n" \
  "v_cmp_ne_u32 vcc, 0, v132\n" \
  "v_cndmask_b32 v44, v44, v20, vcc\n" \
  "s_mov_b32 exec_hi, 0\n" \
  "buffer_store_dword v44, v132, s[8:11], s35 offen sc1\n" \
  "s_mov_b32 exec_hi, -1\n" \
  "s_waitcnt vmcnt(1)\n" \
  "s_barrier\n" \
  "ds_read_b128 v[48:51], v34\n" \
  "ds_read_b128 v[52:55], v35\n" \
  "ds_read_b128 v[56:59], v36\n" \
  "ds_read_b128 v[60:63], v37\n" \
  "GL_%=:\n" \
  "s_add_u32 s37, s29, 40960\n" \
  "s_cmp_eq_u32 s37, 122880\n" \
  "s_cselect_b32 s37, 0, s37\n" \
  "s_sub_u32 s31, s37, s29\n" \
  "ds_read_b128 v[64:67], v38\n" \
  "s_waitcnt lgkmcnt(4)\n" \
  "v_mfma_f32_32x32x16_bf16 v[100:115], v[48:51], a[0:3], 0\n" \
  "v_mfma_f32_32x32x16_bf16 v[116:131], v[48:51], a[4:7], 0\n" \
  "ds_read_b128 v[48:51], v39\n" \
  "s_waitcnt lgkmcnt(4)\n" \
  "v_mfma_f32_32x32x16_bf16 v[100:115], v[52:55], a[8:11], v[100:115]\n" \
  "v_cvt_pk_bf16_f32 v0, v68, v69\n" \
  "v_cvt_pk_bf16_f32 v1, v70, v71\n" \
  "v_mfma_f32_32x32x16_bf16 v[116:131], v[52:55], a[12:15], v[116:131]\n" \
  "v_cvt_pk_bf16_f32 v2, v72, v73\n" \
  "v_cvt_pk_bf16_f32 v3, v74, v75\n" \
  "ds_read_b128 v[52:55], v40\n" \
  "s_waitcnt lgkmcnt(4)\n" \
  "v_mfma_f32_32x32x16_bf16 v[100:115], v[56:59], a[16:19], v[100:115]\n" \
  "buffer_store_dwordx4 v[0:3], v42, s[8:11], s33 offen sc1\n" \
  "v_cvt_pk_bf16_f32 v4, v76, v77\n" \
  "v_mfma_f32_32x32x16_bf16 v[116:131], v[56:59], a[20:23], v[116:131]\n" \
  "v_cvt_pk_bf16_f32 v5, v78, v79\n" \
  "v_cvt_pk_bf16_f32 v6, v80, v81\n" \
  "ds_read_b128 v[56:59], v41\n" \
  "s_waitcnt lgkmcnt(4)\n" \
  "v_mfma_f32_32x32x16_bf16 v[100:115], v[60:63], a[24:27], v[100:115]\n" \
  "v_cvt_pk_bf16_f32 v7, v82, v83\n" \
  "buffer_store_dwordx4 v[4:7], v42, s[8:11], s33 offen offset:512 sc1\n" \
  "v_mfma_f32_32x32x16_bf16 v[116:131], v[60:63], a[28:31], v[116:131]\n" \
  "v_cvt_pk_bf16_f32 v8, v84, v85\n" \
  "v_cvt_pk_bf16_f32 v9, v86, v87\n" \
  "ds_read_b128 v[60:63], v34 offset:256\n" \
  "s_waitcnt lgkmcnt(4)\n" \
  "v_mfma_f32_32x32x16_bf16 v[100:115], v[64:67], a[32:35], v[100:115]\n" \
  "v_cvt_pk_bf16_f32 v10, v88, v89\n" \
  "v_cvt_pk_bf16_f32 v11, v90, v91\n" \
  "v_mfma_f32_32x32x16_bf16 v[116:131], v[64:67], a[36:39], v[116:131]\n" \
  "buffer_store_dwordx4 v[8:11], v42, s[8:11], s33 offen offset:2048 sc1\n" \
  "v_cvt_pk_bf16_f32 v12, v92, v93\n" \
  "ds_read_b128 v[64:67], v35 offset:256\n" \
  "s_waitcnt lgkmcnt(4)\n" \
  "v_mfma_f32_32x32x16_bf16 v[100:115], v[48:51], a[40:43], v[100:115]\n" \
  "v_cvt_pk_bf16_f32 v13, v94, v95\n" \
  "v_cvt_pk_bf16_f32 v14, v96, v97\n" \
  "v_mfma_f32_32x32x16_bf16 v[116:131], v[48:51], a[44:47], v[116:131]\n" \
  "v_cvt_pk_bf16_f32 v15, v98, v99\n" \
  "buffer_store_dwordx4 v[12:15], v42, s[8:11], s33 offen offset:2560 sc1\n" \
  "ds_read_b128 v[48:51], v36 offset:256\n" \
  "s_waitcnt lgkmcnt(4)\n" \
  "v_mfma_f32_32x32x16_bf16 v[100:115], v[52:55], a[48:51], v[100:115]\n" \
  "v_mfma_f32_32x32x16_bf16 v[116:131], v[52:55], a[52:55], v[116:131]\n" \
  "ds_read_b128 v[52:55], v37 offset:256\n" \
  "s_waitcnt lgkmcnt(4)\n" \
  "v_mfma_f32_32x32x16_bf16 v[100:115], v[56:59], a[56:59], v[100:115]\n" \
  "v_mfma_f32_32x32x16_bf16 v[116:131], v[56:59], a[60:63], v[116:131]\n" \
  "ds_read_b128 v[56:59], v38 offset:256\n" \
  "s_waitcnt lgkmcnt(4)\n" \
  "v_mfma_f32_32x32x16_bf16 v[100:115], v[60:63], a[64:67], v[100:115]\n" \
  "s_add_u32 s37, s24, s30\n" \
  "s_add_u32 m0, s37, 0\n" \
  "s_nop 0\n" \
  "buffer_load_dwordx4 v24, s[12:15], s32 offen lds\n" \
  "v_mfma_f32_32x32x16_bf16 v[116:131], v[60:63], a[68:71], v[116:131]\n" \
  "ds_read_b128 v[60:63], v39 offset:256\n" \
  "s_waitcnt lgkmcnt(4)\n" \
  "v_mfma_f32_32x32x16_bf16 v[100:115], v[64:67], a[72:75], v[100:115]\n" \
  "v_mfma_f32_32x32x16_bf16 v[116:131], v[64:67], a[76:79], v[116:131]\n" \
  "ds_read_b128 v[64:67], v40 offset:256\n" \
  "s_waitcnt lgkmcnt(4)\n" \
  "v_mfma_f32_32x32x16_bf16 v[100:115], v[48:51], a[80:83], v[100:115]\n" \
  "buffer_load_dwordx4 v25, s[12:15], s32 offen offset:1024 lds\n" \
  "v_mfma_f32_32x32x16_bf16 v[116:131], v[48:51], a[84:87], v[116:131]\n" \
  "ds_read_b128 v[48:51], v41 offset:256\n" \
  "s_waitcnt lgkmcnt(4)\n" \
  "v_mfma_f32_32x32x16_bf16 v[100:115], v[52:55], a[88:91], v[100:115]\n" \
  "v_mfma_f32_32x32x16_bf16 v[116:131], v[52:55], a[92:95], v[116:131]\n" \
  "ds_read_b128 v[52:55], v34 offset:512\n" \
  "s_waitcnt lgkmcnt(4)\n" \
  "v_mfma_f32_32x32x16_bf16 v[100:115], v[56:59], a[96:99], v[100:115]\n" \
  "buffer_load_dwordx4 v26, s[12:15], s32 offen offset:2048 lds\n" \
  "v_mfma_f32_32x32x16_bf16 v[116:131], v[56:59], a[100:103], v[116:131]\n" \
  "ds_read_b128 v[56:59], v35 offset:512\n" \
  "s_waitcnt lgkmcnt(4)\n" \
  "v_mfma_f32_32x32x16_bf16 v[100:115], v[60:63], a[104:107], v[100:115]\n" \
  "v_mfma_f32_32x32x16_bf16 v[116:131], v[60:63], a[108:111], v[116:131]\n" \
  "ds_read_b128 v[60:63], v36 offset:512\n" \
  "s_waitcnt lgkmcnt(4)\n" \
  "v_mfma_f32_32x32x16_bf16 v[100:115], v[64:67], a[112:115], v[100:115]\n" \
  "buffer_load_dwordx4 v27, s[12:15], s32 offen offset:3072 lds\n" \
  "v_mfma_f32_32x32x16_bf16 v[116:131], v[64:67], a[116:119], v[116:131]\n" \
  "ds_read_b128 v[64:67], v37 offset:512\n" \
  "s_waitcnt lgkmcnt(4)\n" \
  "v_mfma_f32_32x32x16_bf16 v[100:115], v[48:51], a[120:123], v[100:115]\n" \
  "v_mfma_f32_32x32x16_bf16 v[116:131], v[48:51], a[124:127], v[116:131]\n" \
  "ds_read_b128 v[48:51], v38 offset:512\n" \
  "s_waitcnt lgkmcnt(4)\n" \
  "v_mfma_f32_32x32x16_bf16 v[100:115], v[52:55], a[128:131], v[100:115]\n" \
  "s_add_u32 s37, s24, s30\n" \
  "s_add_u32 m0, s37, 4096\n" \
  "s_nop 0\n" \
  "buffer_load_dwordx4 v28, s[12:15], s32 offen lds\n" \
  "v_mfma_f32_32x32x16_bf16 v[116:131], v[52:55], a[132:135], v[116:131]\n" \
  "ds_read_b128 v[52:55], v39 offset:512\n" \
  "s_waitcnt lgkmcnt(4)\n" \
  "v_mfma_f32_32x32x16_bf16 v[100:115], v[56:59], a[136:139], v[100:115]\n" \
  "v_mfma_f32_32x32x16_bf16 v[116:131], v[56:59], a[140:143], v[116:131]\n" \
  "ds_read_b128 v[56:59], v40 offset:512\n" \
  "s_waitcnt lgkmcnt(4)\n" \
  "v_mfma_f32_32x32x16_bf16 v[100:115], v[60:63], a[144:147], v[100:115]\n" \
  "buffer_load_dwordx4 v29, s[12:15], s32 offen offset:1024 lds\n" \
  "v_mfma_f32_32x32x16_bf16 v[116:131], v[60:63], a[148:151], v[116:131]\n" \
  "ds_read_b128 v[60:63], v41 offset:512\n" \
  "s_waitcnt lgkmcnt(4)\n" \
  "v_mfma_f32_32x32x16_bf16 v[100:115], v[64:67], a[152:155], v[100:115]\n" \
  "v_mfma_f32_32x32x16_bf16 v[116:131], v[64:67], a[156:159], v[116:131]\n" \
  "ds_read_b128 v[64:67], v34 offset:768\n" \
  "s_waitcnt lgkmcnt(4)\n" \
  "v_mfma_f32_32x32x16_bf16 v[100:115], v[48:51], a[160:163], v[100:115]\n" \
  "buffer_load_dwordx4 v30, s[12:15], s32 offen offset:2048 lds\n" \
  "v_mfma_f32_32x32x16_bf16 v[116:131], v[48:51], a[164:167], v[116:131]\n" \
  "ds_read_b128 v[48:51], v35 offset:768\n" \
  "s_waitcnt lgkmcnt(4)\n" \
  "v_mfma_f32_32x32x16_bf16 v[100:115], v[52:55], a[168:171], v[100:115]\n" \
  "v_mfma_f32_32x32x16_bf16 v[116:131], v[52:55], a[172:175], v[116:131]\n" \
  "ds_read_b128 v[52:55], v36 offset:768\n" \
  "s_waitcnt lgkmcnt(4)\n" \
  "v_mfma_f32_32x32x16_bf16 v[100:115], v[56:59], a[176:179], v[100:115]\n" \
  "buffer_load_dwordx4 v31, s[12:15], s32 offen offset:3072 lds\n" \
  "v_mfma_f32_32x32x16_bf16 v[116:131], v[56:59], a[180:183], v[116:131]\n" \
  "ds_read_b128 v[56:59], v37 offset:768\n" \
  "s_waitcnt lgkmcnt(4)\n" \
  "v_mfma_f32_32x32x16_bf16 v[100:115], v[60:63], a[184:187], v[100:115]\n" \
  "v_mfma_f32_32x32x16_bf16 v[116:131], v[60:63], a[188:191], v[116:131]\n" \
  "ds_read_b128 v[60:63], v38 offset:768\n" \
  "s_waitcnt lgkmcnt(4)\n" \
  "v_mfma_f32_32x32x16_bf16 v[100:115], v[64:67], a[192:195], v[100:115]\n" \
  "s_add_u32 s37, s24, s30\n" \
  "s_add_u32 m0, s37, 8192\n" \
  "s_nop 0\n" \
  "buffer_load_dwordx4 v32, s[12:15], s32 offen lds\n" \
  "v_mfma_f32_32x32x16_bf16 v[116:131], v[64:67], a[196:199], v[116:131]\n" \
  "ds_read_b128 v[64:67], v39 offset:768\n" \
  "s_waitcnt lgkmcnt(4)\n" \
  "v_mfma_f32_32x32x16_bf16 v[100:115], v[48:51], a[200:203], v[100:115]\n" \
  "v_mfma_f32_32x32x16_bf16 v[116:131], v[48:51], a[204:207], v[116:131]\n" \
  "ds_read_b128 v[48:51], v40 offset:768\n" \
  "s_waitcnt lgkmcnt(4)\n" \
  "v_mfma_f32_32x32x16_bf16 v[100:115], v[52:55], a[208:211], v[100:115]\n" \
  "buffer_load_dwordx4 v33, s[12:15], s32 offen offset:1024 lds\n" \
  "v_mfma_f32_32x32x16_bf16 v[116:131], v[52:55], a[212:215], v[116:131]\n" \
  "ds_read_b128 v[52:55], v41 offset:768\n" \
  "s_waitcnt lgkmcnt(4)\n" \
  "v_mfma_f32_32x32x16_bf16 v[100:115], v[56:59], a[216:219], v[100:115]\n" \
  "v_mfma_f32_32x32x16_bf16 v[116:131], v[56:59], a[220:223], v[116:131]\n" \
  "ds_read_b128 v[56:59], v34 offset:1024\n" \
  "v_add_u32 v34, s31, v34\n" \
  "s_waitcnt lgkmcnt(4)\n" \
  "v_mfma_f32_32x32x16_bf16 v[100:115], v[60:63], a[224:227], v[100:115]\n" \
  "v_mfma_f32_32x32x16_bf16 v[116:131], v[60:63], a[228:231], v[116:131]\n" \
  "ds_read_b128 v[60:63], v35 offset:1024\n" \
  "v_add_u32 v35, s31, v35\n" \
  "s_waitcnt lgkmcnt(4)\n" \
  "v_mfma_f32_32x32x16_bf16 v[100:115], v[64:67], a[232:235], v[100:115]\n" \
  "v_mfma_f32_32x32x16_bf16 v[116:131], v[64:67], a[236:239], v[116:131]\n" \
  "ds_read_b128 v[64:67], v36 offset:1024\n" \
  "v_add_u32 v36, s31, v36\n" \
  "s_waitcnt lgkmcnt(4)\n" \
  "v_mfma_f32_32x32x16_bf16 v[100:115], v[48:51], a[240:243], v[100:115]\n" \
  "v_mfma_f32_32x32x16_bf16 v[116:131], v[48:51], a[244:247], v[116:131]\n" \
  "ds_read_b128 v[48:51], v37 offset:1024\n" \
  "v_add_u32 v37, s31, v37\n" \
  "s_waitcnt lgkmcnt(4)\n" \
  "v_mfma_f32_32x32x16_bf16 v[100:115], v[52:55], a[248:251], v[100:115]\n" \
  "v_mfma_f32_32x32x16_bf16 v[116:131], v[52:55], a[252:255], v[116:131]\n" \
  "ds_read_b128 v[52:55], v38 offset:1024\n" \
  "v_add_u32 v38, s31, v38\n" \
  "s_waitcnt lgkmcnt(4)\n" \
  "v_mfma_f32_32x32x16_bf16 v[100:115], v[56:59], v[192:195], v[100:115]\n" \
  "v_mfma_f32_32x32x16_bf16 v[116:131], v[56:59], v[196:199], v[116:131]\n" \
  "ds_read_b128 v[56:59], v39 offset:1024\n" \
  "v_add_u32 v39, s31, v39\n" \
  "s_waitcnt lgkmcnt(4)\n" \
  "v_mfma_f32_32x32x16_bf16 v[100:115], v[60:63], v[200:203], v[100:115]\n" \
  "v_mfma_f32_32x32x16_bf16 v[116:131], v[60:63], v[204:207], v[116:131]\n" \
  "ds_read_b128 v[60:63], v40 offset:1024\n" \
  "v_add_u32 v40, s31, v40\n" \
  "s_waitcnt lgkmcnt(4)\n" \
  "v_mfma_f32_32x32x16_bf16 v[100:115], v[64:67], v[208:211], v[100:115]\n" \
  "v_mfma_f32_32x32x16_bf16 v[116:131], v[64:67], v[212:215], v[116:131]\n" \
  "s_add_u32 s33, s33, s34\n" \
  "v_mov_b32 v42, v43\n" \
  "ds_read_b128 v[64:67], v41 offset:1024\n" \
  "v_add_u32 v41, s31, v41\n" \
  "s_waitcnt lgkmcnt(4)\n" \
  "v_mfma_f32_32x32x16_bf16 v[100:115], v[48:51], v[216:219], v[100:115]\n" \
  "v_mfma_f32_32x32x16_bf16 v[116:131], v[48:51], v[220:223], v[116:131]\n" \
  "s_waitcnt lgkmcnt(0)\n" \
  "s_waitcnt vmcnt(10)\n" \
  "s_barrier\n" \
  "s_mov_b32 exec_hi, 0\n" \
  "buffer_store_dword v44, v132, s[8:11], s35 offen sc1\n" \
  "s_mov_b32 exec_hi, -1\n" \
  "v_add_u32 v44, 1, v44\n" \
  "ds_read_b128 v[48:51], v34\n" \
  "v_mfma_f32_32x32x16_bf16 v[100:115], v[52:55], v[224:227], v[100:115]\n" \
  "v_mfma_f32_32x32x16_bf16 v[116:131], v[52:55], v[228:231], v[116:131]\n" \
  "ds_read_b128 v[52:55], v35\n" \
  "v_mfma_f32_32x32x16_bf16 v[100:115], v[56:59], v[232:235], v[100:115]\n" \
  "v_mfma_f32_32x32x16_bf16 v[116:131], v[56:59], v[236:239], v[116:131]\n" \
  "ds_read_b128 v[56:59], v36\n" \
  "v_mfma_f32_32x32x16_bf16 v[100:115], v[60:63], v[240:243], v[100:115]\n" \
  "v_mfma_f32_32x32x16_bf16 v[116:131], v[60:63], v[244:247], v[116:131]\n" \
  "ds_read_b128 v[60:63], v37\n" \
  "v_mfma_f32_32x32x16_bf16 v[100:115], v[64:67], v[248:251], v[100:115]\n" \
  "v_mfma_f32_32x32x16_bf16 v[116:131], v[64:67], v[252:255], v[116:131]\n" \
  "s_mov_b32 s30, s29\n" \
  "s_add_u32 s29, s29, s31\n" \
  "s_add_u32 s32, s32, 40960\n" \
  "s_min_u32 s32, s32, 2580480\n" \
  "s_cmp_eq_u32 s28, 62\n" \
  "s_cbranch_scc1 GE_%=\n" \
  "s_add_u32 s37, s29, 40960\n" \
  "s_cmp_eq_u32 s37, 122880\n" \
  "s_cselect_b32 s37, 0, s37\n" \
  "s_sub_u32 s31, s37, s29\n" \
  "ds_read_b128 v[64:67], v38\n" \
  "s_waitcnt lgkmcnt(4)\n" \
  "v_mfma_f32_32x32x16_bf16 v[68:83], v[48:51], a[0:3], 0\n" \
  "v_mfma_f32_32x32x16_bf16 v[84:99], v[48:51], a[4:7], 0\n" \
  "ds_read_b128 v[48:51], v39\n" \
  "s_waitcnt lgkmcnt(4)\n" \
  "v_mfma_f32_32x32x16_bf16 v[68:83], v[52:55], a[8:11], v[68:83]\n" \
  "v_cvt_pk_bf16_f32 v0, v100, v101\n" \
  "v_cvt_pk_bf16_f32 v1, v102, v103\n" \
  "v_mfma_f32_32x32x16_bf16 v[84:99], v[52:55], a[12:15], v[84:99]\n" \
  "v_cvt_pk_bf16_f32 v2, v104, v105\n" \
  "v_cvt_pk_bf16_f32 v3, v106, v107\n" \
  "ds_read_b128 v[52:55], v40\n" \
  "s_waitcnt lgkmcnt(4)\n" \
  "v_mfma_f32_32x32x16_bf16 v[68:83], v[56:59], a[16:19], v[68:83]\n" \
  "buffer_store_dwordx4 v[0:3], v42, s[8:11], s33 offen sc1\n" \
  "v_cvt_pk_bf16_f32 v4, v108, v109\n" \
  "v_mfma_f32_32x32x16_bf16 v[84:99], v[56:59], a[20:23], v[84:99]\n" \
  "v_cvt_pk_bf16_f32 v5, v110, v111\n" \
  "v_cvt_pk_bf16_f32 v6, v112, v113\n" \
  "ds_read_b128 v[56:59], v41\n" \
  "s_waitcnt lgkmcnt(4)\n" \
  "v_mfma_f32_32x32x16_bf16 v[68:83], v[60:63], a[24:27], v[68:83]\n" \
  "v_cvt_pk_bf16_f32 v7, v114, v115\n" \
  "buffer_store_dwordx4 v[4:7], v42, s[8:11], s33 offen offset:512 sc1\n" \
  "v_mfma_f32_32x32x16_bf16 v[84:99], v[60:63], a[28:31], v[84:99]\n" \
  "v_cvt_pk_bf16_f32 v8, v116, v117\n" \
  "v_cvt_pk_bf16_f32 v9, v118, v119\n" \
  "ds_read_b128 v[60:63], v34 offset:256\n" \
  "s_waitcnt lgkmcnt(4)\n" \
  "v_mfma_f32_32x32x16_bf16 v[68:83], v[64:67], a[32:35], v[68:83]\n" \
  "v_cvt_pk_bf16_f32 v10, v120, v121\n" \
  "v_cvt_pk_bf16_f32 v11, v122, v123\n" \
  "v_mfma_f32_32x32x16_bf16 v[84:99], v[64:67], a[36:39], v[84:99]\n" \
  "buffer_store_dwordx4 v[8:11], v42, s[8:11], s33 offen offset:2048 sc1\n" \
  "v_cvt_pk_bf16_f32 v12, v124, v125\n" \
  "ds_read_b128 v[64:67], v35 offset:256\n" \
  "s_waitcnt lgkmcnt(4)\n" \
  "v_mfma_f32_32x32x16_bf16 v[68:83], v[48:51], a[40:43], v[68:83]\n" \
  "v_cvt_pk_bf16_f32 v13, v126, v127\n" \
  "v_cvt_pk_bf16_f32 v14, v128, v129\n" \
  "v_mfma_f32_32x32x16_bf16 v[84:99], v[48:51], a[44:47], v[84:99]\n" \
  "v_cvt_pk_bf16_f32 v15, v130, v131\n" \
  "buffer_store_dwordx4 v[12:15], v42, s[8:11], s33 offen offset:2560 sc1\n" \
  "ds_read_b128 v[48:51], v36 offset:256\n" \
  "s_waitcnt lgkmcnt(4)\n" \
  "v_mfma_f32_32x32x16_bf16 v[68:83], v[52:55], a[48:51], v[68:83]\n" \
  "v_mfma_f32_32x32x16_bf16 v[84:99], v[52:55], a[52:55], v[84:99]\n" \
  "ds_read_b128 v[52:55], v37 offset:256\n" \
  "s_waitcnt lgkmcnt(4)\n" \
  "v_mfma_f32_32x32x16_bf16 v[68:83], v[56:59], a[56:59], v[68:83]\n" \
  "v_mfma_f32_32x32x16_bf16 v[84:99], v[56:59], a[60:63], v[84:99]\n" \
  "ds_read_b128 v[56:59], v38 offset:256\n" \
  "s_waitcnt lgkmcnt(4)\n" \
  "v_mfma_f32_32x32x16_bf16 v[68:83], v[60:63], a[64:67], v[68:83]\n" \
  "s_add_u32 s37, s24, s30\n" \
  "s_add_u32 m0, s37, 0\n" \
  "s_nop 0\n" \
  "buffer_load_dwordx4 v24, s[12:15], s32 offen lds\n" \
  "v_mfma_f32_32x32x16_bf16 v[84:99], v[60:63], a[68:71], v[84:99]\n" \
  "ds_read_b128 v[60:63], v39 offset:256\n" \
  "s_waitcnt lgkmcnt(4)\n" \
  "v_mfma_f32_32x32x16_bf16 v[68:83], v[64:67], a[72:75], v[68:83]\n" \
  "v_mfma_f32_32x32x16_bf16 v[84:99], v[64:67], a[76:79], v[84:99]\n" \
  "ds_read_b128 v[64:67], v40 offset:256\n" \
  "s_waitcnt lgkmcnt(4)\n" \
  "v_mfma_f32_32x32x16_bf16 v[68:83], v[48:51], a[80:83], v[68:83]\n" \
  "buffer_load_dwordx4 v25, s[12:15], s32 offen offset:1024 lds\n" \
  "v_mfma_f32_32x32x16_bf16 v[84:99], v[48:51], a[84:87], v[84:99]\n" \
  "ds_read_b128 v[48:51], v41 offset:256\n" \
  "s_waitcnt lgkmcnt(4)\n" \
  "v_mfma_f32_32x32x16_bf16 v[68:83], v[52:55], a[88:91], v[68:83]\n" \
  "v_mfma_f32_32x32x16_bf16 v[84:99], v[52:55], a[92:95], v[84:99]\n" \
  "ds_read_b128 v[52:55], v34 offset:512\n" \
  "s_waitcnt lgkmcnt(4)\n" \
  "v_mfma_f32_32x32x16_bf16 v[68:83], v[56:59], a[96:99], v[68:83]\n" \
  "buffer_load_dwordx4 v26, s[12:15], s32 offen offset:2048 lds\n" \
  "v_mfma_f32_32x32x16_bf16 v[84:99], v[56:59], a[100:103], v[84:99]\n" \
  "ds_read_b128 v[56:59], v35 offset:512\n" \
  "s_waitcnt lgkmcnt(4)\n" \
  "v_mfma_f32_32x32x16_bf16 v[68:83], v[60:63], a[104:107], v[68:83]\n" \
  "v_mfma_f32_32x32x16_bf16 v[84:99], v[60:63], a[108:111], v[84:99]\n" \
  "ds_read_b128 v[60:63], v36 offset:512\n" \
  "s_waitcnt lgkmcnt(4)\n" \
  "v_mfma_f32_32x32x16_bf16 v[68:83], v[64:67], a[112:115], v[68:83]\n" \
  "buffer_load_dwordx4 v27, s[12:15], s32 offen offset:3072 lds\n" \
  "v_mfma_f32_32x32x16_bf16 v[84:99], v[64:67], a[116:119], v[84:99]\n" \
  "ds_read_b128 v[64:67], v37 offset:512\n" \
  "s_waitcnt lgkmcnt(4)\n" \
  "v_mfma_f32_32x32x16_bf16 v[68:83], v[48:51], a[120:123], v[68:83]\n" \
  "v_mfma_f32_32x32x16_bf16 v[84:99], v[48:51], a[124:127], v[84:99]\n" \
  "ds_read_b128 v[48:51], v38 offset:512\n" \
  "s_waitcnt lgkmcnt(4)\n" \
  "v_mfma_f32_32x32x16_bf16 v[68:83], v[52:55], a[128:131], v[68:83]\n" \
  "s_add_u32 s37, s24, s30\n" \
  "s_add_u32 m0, s37, 4096\n" \
  "s_nop 0\n" \
  "buffer_load_dwordx4 v28, s[12:15], s32 offen lds\n" \
  "v_mfma_f32_32x32x16_bf16 v[84:99], v[52:55], a[132:135], v[84:99]\n" \
  "ds_read_b128 v[52:55], v39 offset:512\n" \
  "s_waitcnt lgkmcnt(4)\n" \
  "v_mfma_f32_32x32x16_bf16 v[68:83], v[56:59], a[136:139], v[68:83]\n" \
  "v_mfma_f32_32x32x16_bf16 v[84:99], v[56:59], a[140:143], v[84:99]\n" \
  "ds_read_b128 v[56:59], v40 offset:512\n" \
  "s_waitcnt lgkmcnt(4)\n" \
  "v_mfma_f32_32x32x16_bf16 v[68:83], v[60:63], a[144:147], v[68:83]\n" \
  "buffer_load_dwordx4 v29, s[12:15], s32 offen offset:1024 lds\n" \
  "v_mfma_f32_32x32x16_bf16 v[84:99], v[60:63], a[148:151], v[84:99]\n" \
  "ds_read_b128 v[60:63], v41 offset:512\n" \
  "s_waitcnt lgkmcnt(4)\n" \
  "v_mfma_f32_32x32x16_bf16 v[68:83], v[64:67], a[152:155], v[68:83]\n" \
  "v_mfma_f32_32x32x16_bf16 v[84:99], v[64:67], a[156:159], v[84:99]\n" \
  "ds_read_b128 v[64:67], v34 offset:768\n" \
  "s_waitcnt lgkmcnt(4)\n" \
  "v_mfma_f32_32x32x16_bf16 v[68:83], v[48:51], a[160:163], v[68:83]\n" \
  "buffer_load_dwordx4 v30, s[12:15], s32 offen offset:2048 lds\n" \
  "v_mfma_f32_32x32x16_bf16 v[84:99], v[48:51], a[164:167], v[84:99]\n" \
  "ds_read_b128 v[48:51], v35 offset:768\n" \
  "s_waitcnt lgkmcnt(4)\n" \
  "v_mfma_f32_32x32x16_bf16 v[68:83], v[52:55], a[168:171], v[68:83]\n" \
  "v_mfma_f32_32x32x16_bf16 v[84:99], v[52:55], a[172:175], v[84:99]\n" \
  "ds_read_b128 v[52:55], v36 offset:768\n" \
  "s_waitcnt lgkmcnt(4)\n" \
  "v_mfma_f32_32x32x16_bf16 v[68:83], v[56:59], a[176:179], v[68:83]\n" \
  "buffer_load_dwordx4 v31, s[12:15], s32 offen offset:3072 lds\n" \
  "v_mfma_f32_32x32x16_bf16 v[84:99], v[56:59], a[180:183], v[84:99]\n" \
  "ds_read_b128 v[56:59], v37 offset:768\n" \
  "s_waitcnt lgkmcnt(4)\n" \
  "v_mfma_f32_32x32x16_bf16 v[68:83], v[60:63], a[184:187], v[68:83]\n" \
  "v_mfma_f32_32x32x16_bf16 v[84:99], v[60:63], a[188:191], v[84:99]\n" \
  "ds_read_b128 v[60:63], v38 offset:768\n" \
  "s_waitcnt lgkmcnt(4)\n" \
  "v_mfma_f32_32x32x16_bf16 v[68:83], v[64:67], a[192:195], v[68:83]\n" \
  "s_add_u32 s37, s24, s30\n" \
  "s_add_u32 m0, s37, 8192\n" \
  "s_nop 0\n" \
  "buffer_load_dwordx4 v32, s[12:15], s32 offen lds\n" \
  "v_mfma_f32_32x32x16_bf16 v[84:99], v[64:67], a[196:199], v[84:99]\n" \
  "ds_read_b128 v[64:67], v39 offset:768\n" \
  "s_waitcnt lgkmcnt(4)\n" \
  "v_mfma_f32_32x32x16_bf16 v[68:83], v[48:51], a[200:203], v[68:83]\n" \
  "v_mfma_f32_32x32x16_bf16 v[84:99], v[48:51], a[204:207], v[84:99]\n" \
  "ds_read_b128 v[48:51], v40 offset:768\n" \
  "s_waitcnt lgkmcnt(4)\n" \
  "v_mfma_f32_32x32x16_bf16 v[68:83], v[52:55], a[208:211], v[68:83]\n" \
  "buffer_load_dwordx4 v33, s[12:15], s32 offen offset:1024 lds\n" \
  "v_mfma_f32_32x32x16_bf16 v[84:99], v[52:55], a[212:215], v[84:99]\n" \
  "ds_read_b128 v[52:55], v41 offset:768\n" \
  "s_waitcnt lgkmcnt(4)\n" \
  "v_mfma_f32_32x32x16_bf16 v[68:83], v[56:59], a[216:219], v[68:83]\n" \
  "v_mfma_f32_32x32x16_bf16 v[84:99], v[56:59], a[220:223], v[84:99]\n" \
  "ds_read_b128 v[56:59], v34 offset:1024\n" \
  "v_add_u32 v34, s31, v34\n" \
  "s_waitcnt lgkmcnt(4)\n" \
  "v_mfma_f32_32x32x16_bf16 v[68:83], v[60:63], a[224:227], v[68:83]\n" \
  "v_mfma_f32_32x32x16_bf16 v[84:99], v[60:63], a[228:231], v[84:99]\n" \
  "ds_read_b128 v[60:63], v35 offset:1024\n" \
  "v_add_u32 v35, s31, v35\n" \
  "s_waitcnt lgkmcnt(4)\n" \
  "v_mfma_f32_32x32x16_bf16 v[68:83], v[64:67], a[232:235], v[68:83]\n" \
  "v_mfma_f32_32x32x16_bf16 v[84:99], v[64:67], a[236:239], v[84:99]\n" \
  "ds_read_b128 v[64:67], v36 offset:1024\n" \
  "v_add_u32 v36, s31, v36\n" \
  "s_waitcnt lgkmcnt(4)\n" \
  "v_mfma_f32_32x32x16_bf16 v[68:83], v[48:51], a[240:243], v[68:83]\n" \
  "v_mfma_f32_32x32x16_bf16 v[84:99], v[48:51], a[244:247], v[84:99]\n" \
  "ds_read_b128 v[48:51], v37 offset:1024\n" \
  "v_add_u32 v37, s31, v37\n" \
  "s_waitcnt lgkmcnt(4)\n" \
  "v_mfma_f32_32x32x16_bf16 v[68:83], v[52:55], a[248:251], v[68:83]\n" \
  "v_mfma_f32_32x32x16_bf16 v[84:99], v[52:55], a[252:255], v[84:99]\n" \
  "ds_read_b128 v[52:55], v38 offset:1024\n" \
  "v_add_u32 v38, s31, v38\n" \
  "s_waitcnt lgkmcnt(4)\n" \
  "v_mfma_f32_32x32x16_bf16 v[68:83], v[56:59], v[192:195], v[68:83]\n" \
  "v_mfma_f32_32x32x16_bf16 v[84:99], v[56:59], v[196:199], v[84:99]\n" \
  "ds_read_b128 v[56:59], v39 offset:1024\n" \
  "v_add_u32 v39, s31, v39\n" \
  "s_waitcnt lgkmcnt(4)\n" \
  "v_mfma_f32_32x32x16_bf16 v[68:83], v[60:63], v[200:203], v[68:83]\n" \
  "v_mfma_f32_32x32x16_bf16 v[84:99], v[60:63], v[204:207], v[84:99]\n" \
  "ds_read_b128 v[60:63], v40 offset:1024\n" \
  "v_add_u32 v40, s31, v40\n" \
  "s_waitcnt lgkmcnt(4)\n" \
  "v_mfma_f32_32x32x16_bf16 v[68:83], v[64:67], v[208:211], v[68:83]\n" \
  "v_mfma_f32_32x32x16_bf16 v[84:99], v[64:67], v[212:215], v[84:99]\n" \
  "s_add_u32 s33, s33, s34\n" \
  "v_mov_b32 v42, v43\n" \
  "ds_read_b128 v[64:67], v41 offset:1024\n" \
  "v_add_u32 v41, s31, v41\n" \
  "s_waitcnt lgkmcnt(4)\n" \
  "v_mfma_f32_32x32x16_bf16 v[68:83], v[48:51], v[216:219], v[68:83]\n" \
  "v_mfma_f32_32x32x16_bf16 v[84:99], v[48:51], v[220:223], v[84:99]\n" \
  "s_waitcnt lgkmcnt(0)\n" \
  "s_waitcnt vmcnt(10)\n" \
  "s_barrier\n" \
  "s_mov_b32 exec_hi, 0\n" \
  "buffer_store_dword v44, v132, s[8:11], s35 offen sc1\n" \
  "s_mov_b32 exec_hi, -1\n" \
  "v_add_u32 v44, 1, v44\n" \
  "ds_read_b128 v[48:51], v34\n" \
  "v_mfma_f32_32x32x16_bf16 v[68:83], v[52:55], v[224:227], v[68:83]\n" \
  "v_mfma_f32_32x32x16_bf16 v[84:99], v[52:55], v[228:231], v[84:99]\n" \
  "ds_read_b128 v[52:55], v35\n" \
  "v_mfma_f32_32x32x16_bf16 v[68:83], v[56:59], v[232:235], v[68:83]\n" \
  "v_mfma_f32_32x32x16_bf16 v[84:99], v[56:59], v[236:239], v[84:99]\n" \
  "ds_read_b128 v[56:59], v36\n" \
  "v_mfma_f32_32x32x16_bf16 v[68:83], v[60:63], v[240:243], v[68:83]\n" \
  "v_mfma_f32_32x32x16_bf16 v[84:99], v[60:63], v[244:247], v[84:99]\n" \
  "ds_read_b128 v[60:63], v37\n" \
  "v_mfma_f32_32x32x16_bf16 v[68:83], v[64:67], v[248:251], v[68:83]\n" \
  "v_mfma_f32_32x32x16_bf16 v[84:99], v[64:67], v[252:255], v[84:99]\n" \
  "s_mov_b32 s30, s29\n" \
  "s_add_u32 s29, s29, s31\n" \
  "s_add_u32 s32, s32, 40960\n" \
  "s_min_u32 s32, s32, 2580480\n" \
  "s_add_u32 s28, s28, 2\n" \
  "s_branch GL_%=\n" \
  "GE_%=:\n" \
  "s_nop 7\n" \
  "s_nop 3\n" \
  "v_cvt_pk_bf16_f32 v0, v100, v101\n" \
  "v_cvt_pk_bf16_f32 v1, v102, v103\n" \
  "v_cvt_pk_bf16_f32 v2, v104, v105\n" \
  "v_cvt_pk_bf16_f32 v3, v106, v107\n" \
  "buffer_store_dwordx4 v[0:3], v42, s[8:11], s33 offen sc1\n" \
  "v_cvt_pk_bf16_f32 v4, v108, v109\n" \
  "v_cvt_pk_bf16_f32 v5, v110, v111\n" \
  "v_cvt_pk_bf16_f32 v6, v112, v113\n" \
  "v_cvt_pk_bf16_f32 v7, v114, v115\n" \
  "buffer_store_dwordx4 v[4:7], v42, s[8:11], s33 offen offset:512 sc1\n" \
  "v_cvt_pk_bf16_f32 v8, v116, v117\n" \
  "v_cvt_pk_bf16_f32 v9, v118, v119\n" \
  "v_cvt_pk_bf16_f32 v10, v120, v121\n" \
  "v_cvt_pk_bf16_f32 v11, v122, v123\n" \
  "buffer_store_dwordx4 v[8:11], v42, s[8:11], s33 offen offset:2048 sc1\n" \
  "v_cvt_pk_bf16_f32 v12, v124, v125\n" \
  "v_cvt_pk_bf16_f32 v13, v126, v127\n" \
  "v_cvt_pk_bf16_f32 v14, v128, v129\n" \
  "v_cvt_pk_bf16_f32 v15, v130, v131\n" \
  "buffer_store_dwordx4 v[12:15], v42, s[8:11], s33 offen offset:2560 sc1\n" \
  "s_waitcnt vmcnt(0)\n" \
  "s_waitcnt lgkmcnt(0)\n" \
  "s_mov_b32 exec_hi, 0\n" \
  "buffer_store_dword v44, v132, s[8:11], s35 offen sc1\n" \
  "s_mov_b32 exec_hi, -1\n" \
  "s_waitcnt vmcnt(0)\n"

#define ASM_GF \
  "v_mbcnt_lo_u32_b32 v132, -1, 0\n" \
  "v_lshlrev_b32 v132, 2, v132\n" \
  "s_add_u32 s37, s24, s30\n" \
  "s_add_u32 m0, s37, 0\n" \
  "s_nop 0\n" \
  "buffer_load_dwordx4 v24, s[12:15], s32 offen lds\n" \
  "buffer_load_dwordx4 v25, s[12:15], s32 offen offset:1024 lds\n" \
  "buffer_load_dwordx4 v26, s[12:15], s32 offen offset:2048 lds\n" \
  "buffer_load_dwordx4 v27, s[12:15], s32 offen offset:3072 lds\n" \
  "s_add_u32 s37, s24, s30\n" \
  "s_add_u32 m0, s37, 4096\n" \
  "s_nop 0\n" \
  "buffer_load_dwordx4 v28, s[12:15], s32 offen lds\n" \
  "buffer_load_dwordx4 v29, s[12:15], s32 offen offset:1024 lds\n" \
  "buffer_load_dwordx4 v30, s[12:15], s32 offen offset:2048 lds\n" \
  "buffer_load_dwordx4 v31, s[12:15], s32 offen offset:3072 lds\n" \
  "s_add_u32 s37, s24, s30\n" \
  "s_add_u32 m0, s37, 8192\n" \
  "s_nop 0\n" \
  "buffer_load_dwordx4 v32, s[12:15], s32 offen lds\n" \
  "buffer_load_dwordx4 v33, s[12:15], s32 offen offset:1024 lds\n" \
  "s_mov_b32 s31, 40960\n" \
  "s_waitcnt vmcnt(38)\n" \
  "ds_read_b64_tr_b16 a[0:1], v47\n" \
  "ds_read_b64_tr_b16 a[2:3], v47 offset:256\n" \
  "ds_read_b64_tr_b16 a[4:5], v47 offset:1024\n" \
  "ds_read_b64_tr_b16 a[6:7], v47 offset:1280\n" \
  "s_waitcnt vmcnt(36)\n" \
  "ds_read_b64_tr_b16 a[8:9], v47 offset:2048\n" \
  "ds_read_b64_tr_b16 a[10:11], v47 offset:2304\n" \
  "ds_read_b64_tr_b16 a[12:13], v47 offset:3072\n" \
  "ds_read_b64_tr_b16 a[14:15], v47 offset:3328\n" \
  "s_waitcnt lgkmcnt(4)\n" \
  "s_add_u32 m0, s25, 0\n" \
  "s_nop 0\n" \
  "buffer_load_dwordx4 v45, s[16:19], s26 offen lds\n" \
  "buffer_load_dwordx4 v46, s[20:23], s26 offen offset:1024 lds\n" \
  "s_add_u32 s26, s26, s27\n" \
  "s_waitcnt vmcnt(36)\n" \
  "ds_read_b64_tr_b16 a[16:17], v47 offset:4096\n" \
  "ds_read_b64_tr_b16 a[18:19], v47 offset:4352\n" \
  "ds_read_b64_tr_b16 a[20:21], v47 offset:5120\n" \
  "ds_read_b64_tr_b16 a[22:23], v47 offset:5376\n" \
  "s_waitcnt lgkmcnt(4)\n" \
  "s_add_u32 m0, s25, 2048\n" \
  "s_nop 0\n" \
  "buffer_load_dwordx4 v45, s[16:19], s26 offen lds\n" \
  "buffer_load_dwordx4 v46, s[20:23], s26 offen offset:1024 lds\n" \
  "s_add_u32 s26, s26, s27\n" \
  "s_waitcnt vmcnt(36)\n" \
  "ds_read_b64_tr_b16 a[24:25], v47 offset:6144\n" \
  "ds_read_b64_tr_b16 a[26:27], v47 offset:6400\n" \
  "ds_read_b64_tr_b16 a[28:29], v47 offset:7168\n" \
  "ds_read_b64_tr_b16 a[30:31], v47 offset:7424\n" \
  "s_waitcnt lgkmcnt(4)\n" \
  "s_add_u32 m0, s25, 4096\n" \
  "s_nop 0\n" \
  "buffer_load_dwordx4 v45, s[16:19], s26 offen lds\n" \
  "buffer_load_dwordx4 v46, s[20:23], s26 offen offset:1024 lds\n" \
  "s_add_u32 s26, s26, s27\n" \
  "s_waitcnt vmcnt(36)\n" \
  "ds_read_b64_tr_b16 a[32:33], v47 offset:8192\n" \
  "ds_read_b64_tr_b16 a[34:35], v47 offset:8448\n" \
  "ds_read_b64_tr_b16 a[36:37], v47 offset:9216\n" \
  "ds_read_b64_tr_b16 a[38:39], v47 offset:9472\n" \
  "s_waitcnt lgkmcnt(4)\n" \
  "s_add_u32 m0, s25, 6144\n" \
  "s_nop 0\n" \
  "buffer_load_dwordx4 v45, s[16:19], s26 offen lds\n" \
  "buffer_load_dwordx4 v46, s[20:23], s26 offen offset:1024 lds\n" \
  "s_add_u32 s26, s26, s27\n" \
  "s_waitcnt vmcnt(36)\n" \
  "ds_read_b64_tr_b16 a[40:41], v47 offset:10240\n" \
  "ds_read_b64_tr_b16 a[42:43], v47 offset:10496\n" \
  "ds_read_b64_tr_b16 a[44:45], v47 offset:11264\n" \
  "ds_read_b64_tr_b16 a[46:47], v47 offset:11520\n" \
  "s_waitcnt lgkmcnt(4)\n" \
  "s_add_u32 m0, s25, 8192\n" \
  "s_nop 0\n" \
  "buffer_load_dwordx4 v45, s[16:19], s26 offen lds\n" \
  "buffer_load_dwordx4 v46, s[20:23], s26 offen offset:1024 lds\n" \
  "s_add_u32 s26, s26, s27\n" \
  "s_waitcnt vmcnt(36)\n" \
  "ds_read_b64_tr_b16 a[48:49], v47 offset:12288\n" \
  "ds_read_b64_tr_b16 a[50:51], v47 offset:12544\n" \
  "ds_read_b64_tr_b16 a[52:53], v47 offset:13312\n" \
  "ds_read_b64_tr_b16 a[54:55], v47 offset:13568\n" \
  "s_waitcnt lgkmcnt(4)\n" \
  "s_add_u32 m0, s25, 10240\n" \
  "s_nop 0\n" \
  "buffer_load_dwordx4 v45, s[16:19], s26 offen lds\n" \
  "buffer_load_dwordx4 v46, s[20:23], s26 offen offset:1024 lds\n" \
  "s_add_u32 s26, s26, s27\n" \
  "s_waitcnt vmcnt(36)\n" \
  "ds_read_b64_tr_b16 a[56:57], v47 offset:14336\n" \
  "ds_read_b64_tr_b16 a[58:59], v47 offset:14592\n" \
  "ds_read_b64_tr_b16 a[60:61], v47 offset:15360\n" \
  "ds_read_b64_tr_b16 a[62:63], v47 offset:15616\n" \
  "s_waitcnt lgkmcnt(4)\n" \
  "s_add_u32 m0, s25, 12288\n" \
  "s_nop 0\n" \
  "buffer_load_dwordx4 v45, s[16:19], s26 offen lds\n" \
  "buffer_load_dwordx4 v46, s[20:23], s26 offen offset:1024 lds\n" \
  "s_add_u32 s26, s26, s27\n" \
  "s_waitcnt vmcnt(36)\n" \
  "ds_read_b64_tr_b16 a[64:65], v47 offset:16384\n" \
  "ds_read_b64_tr_b16 a[66:67], v47 offset:16640\n" \
  "ds_read_b64_tr_b16 a[68:69], v47 offset:17408\n" \
  "ds_read_b64_tr_b16 a[70:71], v47 offset:17664\n" \
  "s_waitcnt lgkmcnt(4)\n" \
  "s_add_u32 m0, s25, 14336\n" \
  "s_nop 0\n" \
  "buffer_load_dwordx4 v45, s[16:19], s26 offen lds\n" \
  "buffer_load_dwordx4 v46, s[20:23], s26 offen offset:1024 lds\n" \
  "s_add_u32 s26, s26, s27\n" \
  "s_waitcnt vmcnt(36)\n" \
  "ds_read_b64_tr_b16 a[72:73], v47 offset:18432\n" \
  "ds_read_b64_tr_b16 a[74:75], v47 offset:18688\n" \
  "ds_read_b64_tr_b16 a[76:77], v47 offset:19456\n" \
  "ds_read_b64_tr_b16 a[78:79], v47 offset:19712\n" \
  "s_waitcnt lgkmcnt(4)\n" \
  "s_add_u32 m0, s25, 16384\n" \
  "s_nop 0\n" \
  "buffer_load_dwordx4 v45, s[16:19], s26 offen lds\n" \
  "buffer_load_dwordx4 v46, s[20:23], s26 offen offset:1024 lds\n" \
  "s_add_u32 s26, s26, s27\n" \
  "s_waitcnt vmcnt(36)\n" \
  "ds_read_b64_tr_b16 a[80:81], v47 offset:20480\n" \
  "ds_read_b64_tr_b16 a[82:83], v47 offset:20736\n" \
  "ds_read_b64_tr_b16 a[84:85], v47 offset:21504\n" \
  "ds_read_b64_tr_b16 a[86:87], v47 offset:21760\n" \
  "s_waitcnt lgkmcnt(4)\n" \
  "s_add_u32 m0, s25, 18432\n" \
  "s_nop 0\n" \
  "buffer_load_dwordx4 v45, s[16:19], s26 offen lds\n" \
  "buffer_load_dwordx4 v46, s[20:23], s26 offen offset:1024 lds\n" \
  "s_add_u32 s26, s26, s27\n" \
  "s_waitcnt vmcnt(36)\n" \
  "ds_read_b64_tr_b16 a[88:89], v47 offset:22528\n" \
  "ds_read_b64_tr_b16 a[90:91], v47 offset:22784\n" \
  "ds_read_b64_tr_b16 a[92:93], v47 offset:23552\n" \
  "ds_read_b64_tr_b16 a[94:95], v47 offset:23808\n" \
  "s_waitcnt lgkmcnt(4)\n" \
  "s_add_u32 m0, s25, 20480\n" \
  "s_nop 0\n" \
  "buffer_load_dwordx4 v45, s[16:19], s26 offen lds\n" \
  "buffer_load_dwordx4 v46, s[20:23], s26 offen offset:1024 lds\n" \
  "s_add_u32 s26, s26, s27\n" \
  "s_waitcnt vmcnt(36)\n" \
  "ds_read_b64_tr_b16 a[96:97], v47 offset:24576\n" \
  "ds_read_b64_tr_b16 a[98:99], v47 offset:24832\n" \
  "ds_read_b64_tr_b16 a[100:101], v47 offset:25600\n" \
  "ds_read_b64_tr_b16 a[102:103], v47 offset:25856\n" \
  "s_waitcnt lgkmcnt(4)\n" \
  "s_add_u32 m0, s25, 22528\n" \
  "s_nop 0\n" \
  "buffer_load_dwordx4 v45, s[16:19], s26 offen lds\n" \
  "buffer_load_dwordx4 v46, s[20:23], s26 offen offset:1024 lds\n" \
  "s_add_u32 s26, s26, s27\n" \
  "s_waitcnt vmcnt(36)\n" \
  "ds_read_b64_tr_b16 a[104:105], v47 offset:26624\n" \
  "ds_read_b64_tr_b16 a[106:107], v47 offset:26880\n" \
  "ds_read_b64_tr_b16 a[108:109], v47 offset:27648\n" \
  "ds_read_b64_tr_b16 a[110:111], v47 offset:27904\n" \
  "s_waitcnt lgkmcnt(4)\n" \
  "s_add_u32 m0, s25, 24576\n" \
  "s_nop 0\n" \
  "buffer_load_dwordx4 v45, s[16:19], s26 offen lds\n" \
  "buffer_load_dwordx4 v46, s[20:23], s26 offen offset:1024 lds\n" \
  "s_add_u32 s26, s26, s27\n" \
  "s_waitcnt vmcnt(36)\n" \
  "ds_read_b64_tr_b16 a[112:113], v47 offset:28672\n" \
  "ds_read_b64_tr_b16 a[114:115], v47 offset:28928\n" \
  "ds_read_b64_tr_b16 a[116:117], v47 offset:29696\n" \
  "ds_read_b64_tr_b16 a[118:119], v47 offset:29952\n" \
  "s_waitcnt lgkmcnt(4)\n" \
  "s_add_u32 m0, s25, 26624\n" \
  "s_nop 0\n" \
  "buffer_load_dwordx4 v45, s[16:19], s26 offen lds\n" \
  "buffer_load_dwordx4 v46, s[20:23], s26 offen offset:1024 lds\n" \
  "s_add_u32 s26, s26, s27\n" \
  "s_waitcnt vmcnt(26)\n" \
  "ds_read_b64_tr_b16 a[120:121], v47\n" \
  "ds_read_b64_tr_b16 a[122:123], v47 offset:256\n" \
  "ds_read_b64_tr_b16 a[124:125], v47 offset:1024\n" \
  "ds_read_b64_tr_b16 a[126:127], v47 offset:1280\n" \
  "s_waitcnt lgkmcnt(4)\n" \
  "s_add_u32 m0, s25, 28672\n" \
  "s_nop 0\n" \
  "buffer_load_dwordx4 v45, s[16:19], s26 offen lds\n" \
  "buffer_load_dwordx4 v46, s[20:23], s26 offen offset:1024 lds\n" \
  "s_add_u32 s26, s26, s27\n" \
  "s_barrier\n" \
  "ds_read_b128 v[48:51], v34\n" \
  "ds_read_b128 v[52:55], v35\n" \
  "ds_read_b128 v[56:59], v36\n" \
  "ds_read_b128 v[60:63], v37\n" \
  "ds_read_b128 v[64:67], v38\n" \
  "s_waitcnt lgkmcnt(4)\n" \
  "v_mfma_f32_32x32x16_bf16 v[68:83], a[0:3], v[48:51], 0\n" \
  "v_mfma_f32_32x32x16_bf16 v[84:99], a[4:7], v[48:51], 0\n" \
  "ds_read_b128 v[48:51], v39\n" \
  "s_waitcnt lgkmcnt(4)\n" \
  "v_mfma_f32_32x32x16_bf16 v[68:83], a[8:11], v[52:55], v[68:83]\n" \
  "v_mfma_f32_32x32x16_bf16 v[84:99], a[12:15], v[52:55], v[84:99]\n" \
  "s_waitcnt vmcnt(26)\n" \
  "ds_read_b64_tr_b16 a[128:129], v47 offset:2048\n" \
  "ds_read_b64_tr_b16 a[130:131], v47 offset:2304\n" \
  "ds_read_b64_tr_b16 a[132:133], v47 offset:3072\n" \
  "ds_read_b64_tr_b16 a[134:135], v47 offset:3328\n" \
  "s_add_u32 m0, s25, 0\n" \
  "s_nop 0\n" \
  "buffer_load_dwordx4 v45, s[16:19], s26 offen lds\n" \
  "buffer_load_dwordx4 v46, s[20:23], s26 offen offset:1024 lds\n" \
  "s_add_u32 s26, s26, s27\n" \
  "ds_read_b128 v[52:55], v40\n" \
  "s_waitcnt lgkmcnt(8)\n" \
  "v_mfma_f32_32x32x16_bf16 v[68:83], a[16:19], v[56:59], v[68:83]\n" \
  "v_mfma_f32_32x32x16_bf16 v[84:99], a[20:23], v[56:59], v[84:99]\n" \
  "ds_read_b128 v[56:59], v41\n" \
  "s_waitcnt lgkmcnt(8)\n" \
  "v_mfma_f32_32x32x16_bf16 v[68:83], a[24:27], v[60:63], v[68:83]\n" \
  "v_mfma_f32_32x32x16_bf16 v[84:99], a[28:31], v[60:63], v[84:99]\n" \
  "s_waitcnt vmcnt(26)\n" \
  "ds_read_b64_tr_b16 a[136:137], v47 offset:4096\n" \
  "ds_read_b64_tr_b16 a[138:139], v47 offset:4352\n" \
  "ds_read_b64_tr_b16 a[140:141], v47 offset:5120\n" \
  "ds_read_b64_tr_b16 a[142:143], v47 offset:5376\n" \
  "s_waitcnt lgkmcnt(6)\n" \
  "s_add_u32 m0, s25, 2048\n" \
  "s_nop 0\n" \
  "buffer_load_dwordx4 v45, s[16:19], s26 offen lds\n" \
  "buffer_load_dwordx4 v46, s[20:23], s26 offen offset:1024 lds\n" \
  "s_add_u32 s26, s26, s27\n" \
  "ds_read_b128 v[60:63], v34 offset:256\n" \
  "v_mfma_f32_32x32x16_bf16 v[68:83], a[32:35], v[64:67], v[68:83]\n" \
  "v_mfma_f32_32x32x16_bf16 v[84:99], a[36:39], v[64:67], v[84:99]\n" \
  "ds_read_b128 v[64:67], v35 offset:256\n" \
  "v_mfma_f32_32x32x16_bf16 v[68:83], a[40:43], v[48:51], v[68:83]\n" \
  "v_mfma_f32_32x32x16_bf16 v[84:99], a[44:47], v[48:51], v[84:99]\n" \
  "s_waitcnt vmcnt(26)\n" \
  "ds_read_b64_tr_b16 a[144:145], v47 offset:6144\n" \
  "ds_read_b64_tr_b16 a[146:147], v47 offset:6400\n" \
  "ds_read_b64_tr_b16 a[148:149], v47 offset:7168\n" \
  "ds_read_b64_tr_b16 a[150:151], v47 offset:7424\n" \
  "s_waitcnt lgkmcnt(6)\n" \
  "s_add_u32 m0, s25, 4096\n" \
  "s_nop 0\n" \
  "buffer_load_dwordx4 v45, s[16:19], s26 offen lds\n" \
  "buffer_load_dwordx4 v46, s[20:23], s26 offen offset:1024 lds\n" \
  "s_add_u32 s26, s26, s27\n" \
  "ds_read_b128 v[48:51], v36 offset:256\n" \
  "v_mfma_f32_32x32x16_bf16 v[68:83], a[48:51], v[52:55], v[68:83]\n" \
  "v_mfma_f32_32x32x16_bf16 v[84:99], a[52:55], v[52:55], v[84:99]\n" \
  "ds_read_b128 v[52:55], v37 offset:256\n" \
  "v_mfma_f32_32x32x16_bf16 v[68:83], a[56:59], v[56:59], v[68:83]\n" \
  "v_mfma_f32_32x32x16_bf16 v[84:99], a[60:63], v[56:59], v[84:99]\n" \
  "s_waitcnt vmcnt(26)\n" \
  "ds_read_b64_tr_b16 a[152:153], v47 offset:8192\n" \
  "ds_read_b64_tr_b16 a[154:155], v47 offset:8448\n" \
  "ds_read_b64_tr_b16 a[156:157], v47 offset:9216\n" \
  "ds_read_b64_tr_b16 a[158:159], v47 offset:9472\n" \
  "s_waitcnt lgkmcnt(6)\n" \
  "s_add_u32 m0, s25, 6144\n" \
  "s_nop 0\n" \
  "buffer_load_dwordx4 v45, s[16:19], s26 offen lds\n" \
  "buffer_load_dwordx4 v46, s[20:23], s26 offen offset:1024 lds\n" \
  "s_add_u32 s26, s26, s27\n" \
  "ds_read_b128 v[56:59], v38 offset:256\n" \
  "v_mfma_f32_32x32x16_bf16 v[68:83], a[64:67], v[60:63], v[68:83]\n" \
  "v_mfma_f32_32x32x16_bf16 v[84:99], a[68:71], v[60:63], v[84:99]\n" \
  "ds_read_b128 v[60:63], v39 offset:256\n" \
  "v_mfma_f32_32x32x16_bf16 v[68:83], a[72:75], v[64:67], v[68:83]\n" \
  "v_mfma_f32_32x32x16_bf16 v[84:99], a[76:79], v[64:67], v[84:99]\n" \
  "s_waitcnt vmcnt(26)\n" \
  "ds_read_b64_tr_b16 a[160:161], v47 offset:10240\n" \
  "ds_read_b64_tr_b16 a[162:163], v47 offset:10496\n" \
  "ds_read_b64_tr_b16 a[164:165], v47 offset:11264\n" \
  "ds_read_b64_tr_b16 a[166:167], v47 offset:11520\n" \
  "s_waitcnt lgkmcnt(6)\n" \
  "s_add_u32 m0, s25, 8192\n" \
  "s_nop 0\n" \
  "buffer_load_dwordx4 v45, s[16:19], s26 offen lds\n" \
  "buffer_load_dwordx4 v46, s[20:23], s26 offen offset:1024 lds\n" \
  "s_add_u32 s26, s26, s27\n" \
  "ds_read_b128 v[64:67], v40 offset:256\n" \
  "v_mfma_f32_32x32x16_bf16 v[68:83], a[80:83], v[48:51], v[68:83]\n" \
  "v_mfma_f32_32x32x16_bf16 v[84:99], a[84:87], v[48:51], v[84:99]\n" \
  "ds_read_b128 v[48:51], v41 offset:256\n" \
  "v_mfma_f32_32x32x16_bf16 v[68:83], a[88:91], v[52:55], v[68:83]\n" \
  "v_mfma_f32_32x32x16_bf16 v[84:99], a[92:95], v[52:55], v[84:99]\n" \
  "s_waitcnt vmcnt(26)\n" \
  "ds_read_b64_tr_b16 a[168:169], v47 offset:12288\n" \
  "ds_read_b64_tr_b16 a[170:171], v47 offset:12544\n" \
  "ds_read_b64_tr_b16 a[172:173], v47 offset:13312\n" \
  "ds_read_b64_tr_b16 a[174:175], v47 offset:13568\n" \
  "s_waitcnt lgkmcnt(6)\n" \
  "s_add_u32 m0, s25, 10240\n" \
  "s_nop 0\n" \
  "buffer_load_dwordx4 v45, s[16:19], s26 offen lds\n" \
  "buffer_load_dwordx4 v46, s[20:23], s26 offen offset:1024 lds\n" \
  "s_add_u32 s26, s26, s27\n" \
  "ds_read_b128 v[52:55], v34 offset:512\n" \
  "v_mfma_f32_32x32x16_bf16 v[68:83], a[96:99], v[56:59], v[68:83]\n" \
  "v_mfma_f32_32x32x16_bf16 v[84:99], a[100:103], v[56:59], v[84:99]\n" \
  "ds_read_b128 v[56:59], v35 offset:512\n" \
  "v_mfma_f32_32x32x16_bf16 v[68:83], a[104:107], v[60:63], v[68:83]\n" \
  "v_mfma_f32_32x32x16_bf16 v[84:99], a[108:111], v[60:63], v[84:99]\n" \
  "s_waitcnt vmcnt(26)\n" \
  "ds_read_b64_tr_b16 a[176:177], v47 offset:14336\n" \
  "ds_read_b64_tr_b16 a[178:179], v47 offset:14592\n" \
  "ds_read_b64_tr_b16 a[180:181], v47 offset:15360\n" \
  "ds_read_b64_tr_b16 a[182:183], v47 offset:15616\n" \
  "s_waitcnt lgkmcnt(6)\n" \
  "s_add_u32 m0, s25, 12288\n" \
  "s_nop 0\n" \
  "buffer_load_dwordx4 v45, s[16:19], s26 offen lds\n" \
  "buffer_load_dwordx4 v46, s[20:23], s26 offen offset:1024 lds\n" \
  "s_add_u32 s26, s26, s27\n" \
  "ds_read_b128 v[60:63], v36 offset:512\n" \
  "v_mfma_f32_32x32x16_bf16 v[68:83], a[112:115], v[64:67], v[68:83]\n" \
  "v_mfma_f32_32x32x16_bf16 v[84:99], a[116:119], v[64:67], v[84:99]\n" \
  "ds_read_b128 v[64:67], v37 offset:512\n" \
  "v_mfma_f32_32x32x16_bf16 v[68:83], a[120:123], v[48:51], v[68:83]\n" \
  "v_mfma_f32_32x32x16_bf16 v[84:99], a[124:127], v[48:51], v[84:99]\n" \
  "s_waitcnt vmcnt(26)\n" \
  "ds_read_b64_tr_b16 a[184:185], v47 offset:16384\n" \
  "ds_read_b64_tr_b16 a[186:187], v47 offset:16640\n" \
  "ds_read_b64_tr_b16 a[188:189], v47 offset:17408\n" \
  "ds_read_b64_tr_b16 a[190:191], v47 offset:17664\n" \
  "s_waitcnt lgkmcnt(6)\n" \
  "s_add_u32 m0, s25, 14336\n" \
  "s_nop 0\n" \
  "buffer_load_dwordx4 v45, s[16:19], s26 offen lds\n" \
  "buffer_load_dwordx4 v46, s[20:23], s26 offen offset:1024 lds\n" \
  "s_add_u32 s26, s26, s27\n" \
  "ds_read_b128 v[48:51], v38 offset:512\n" \
  "v_mfma_f32_32x32x16_bf16 v[68:83], a[128:131], v[52:55], v[68:83]\n" \
  "v_mfma_f32_32x32x16_bf16 v[84:99], a[132:135], v[52:55], v[84:99]\n" \
  "ds_read_b128 v[52:55], v39 offset:512\n" \
  "v_mfma_f32_32x32x16_bf16 v[68:83], a[136:139], v[56:59], v[68:83]\n" \
  "v_mfma_f32_32x32x16_bf16 v[84:99], a[140:143], v[56:59], v[84:99]\n" \
  "s_waitcnt vmcnt(26)\n" \
  "ds_read_b64_tr_b16 a[192:193], v47 offset:18432\n" \
  "ds_read_b64_tr_b16 a[194:195], v47 offset:18688\n" \
  "ds_read_b64_tr_b16 a[196:197], v47 offset:19456\n" \
  "ds_read_b64_tr_b16 a[198:199], v47 offset:19712\n" \
  "s_waitcnt lgkmcnt(6)\n" \
  "s_add_u32 m0, s25, 16384\n" \
  "s_nop 0\n" \
  "buffer_load_dwordx4 v45, s[16:19], s26 offen lds\n" \
  "buffer_load_dwordx4 v46, s[20:23], s26 offen offset:1024 lds\n" \
  "s_add_u32 s26, s26, s27\n" \
  "ds_read_b128 v[56:59], v40 offset:512\n" \
  "v_mfma_f32_32x32x16_bf16 v[68:83], a[144:147], v[60:63], v[68:83]\n" \
  "v_mfma_f32_32x32x16_bf16 v[84:99], a[148:151], v[60:63], v[84:99]\n" \
  "ds_read_b128 v[60:63], v41 offset:512\n" \
  "v_mfma_f32_32x32x16_bf16 v[68:83], a[152:155], v[64:67], v[68:83]\n" \
  "v_mfma_f32_32x32x16_bf16 v[84:99], a[156:159], v[64:67], v[84:99]\n" \
  "s_waitcnt vmcnt(26)\n" \
  "ds_read_b64_tr_b16 a[200:201], v47 offset:20480\n" \
  "ds_read_b64_tr_b16 a[202:203], v47 offset:20736\n" \
  "ds_read_b64_tr_b16 a[204:205], v47 offset:21504\n" \
  "ds_read_b64_tr_b16 a[206:207], v47 offset:21760\n" \
  "s_waitcnt lgkmcnt(6)\n" \
  "s_add_u32 m0, s25, 18432\n" \
  "s_nop 0\n" \
  "buffer_load_dwordx4 v45, s[16:19], s26 offen lds\n" \
  "buffer_load_dwordx4 v46, s[20:23], s26 offen offset:1024 lds\n" \
  "s_add_u32 s26, s26, s27\n" \
  "ds_read_b128 v[64:67], v34 offset:768\n" \
  "v_mfma_f32_32x32x16_bf16 v[68:83], a[160:163], v[48:51], v[68:83]\n" \
  "v_mfma_f32_32x32x16_bf16 v[84:99], a[164:167], v[48:51], v[84:99]\n" \
  "ds_read_b128 v[48:51], v35 offset:768\n" \
  "v_mfma_f32_32x32x16_bf16 v[68:83], a[168:171], v[52:55], v[68:83]\n" \
  "v_mfma_f32_32x32x16_bf16 v[84:99], a[172:175], v[52:55], v[84:99]\n" \
  "s_waitcnt vmcnt(26)\n" \
  "ds_read_b64_tr_b16 a[208:209], v47 offset:22528\n" \
  "ds_read_b64_tr_b16 a[210:211], v47 offset:22784\n" \
  "ds_read_b64_tr_b16 a[212:213], v47 offset:23552\n" \
  "ds_read_b64_tr_b16 a[214:215], v47 offset:23808\n" \
  "ds_read_b128 v[52:55], v36 offset:768\n" \
  "s_waitcnt lgkmcnt(12)\n" \
  "v_mfma_f32_32x32x16_bf16 v[68:83], a[176:179], v[56:59], v[68:83]\n" \
  "v_mfma_f32_32x32x16_bf16 v[84:99], a[180:183], v[56:59], v[84:99]\n" \
  "ds_read_b128 v[56:59], v37 offset:768\n" \
  "s_waitcnt lgkmcnt(12)\n" \
  "v_mfma_f32_32x32x16_bf16 v[68:83], a[184:187], v[60:63], v[68:83]\n" \
  "v_mfma_f32_32x32x16_bf16 v[84:99], a[188:191], v[60:63], v[84:99]\n" \
  "s_waitcnt vmcnt(24)\n" \
  "ds_read_b64_tr_b16 a[216:217], v47 offset:24576\n" \
  "ds_read_b64_tr_b16 a[218:219], v47 offset:24832\n" \
  "ds_read_b64_tr_b16 a[220:221], v47 offset:25600\n" \
  "ds_read_b64_tr_b16 a[222:223], v47 offset:25856\n" \
  "ds_read_b128 v[60:63], v38 offset:768\n" \
  "s_waitcnt lgkmcnt(12)\n" \
  "v_mfma_f32_32x32x16_bf16 v[68:83], a[192:195], v[64:67], v[68:83]\n" \
  "v_mfma_f32_32x32x16_bf16 v[84:99], a[196:199], v[64:67], v[84:99]\n" \
  "ds_read_b128 v[64:67], v39 offset:768\n" \
  "s_waitcnt lgkmcnt(12)\n" \
  "v_mfma_f32_32x32x16_bf16 v[68:83], a[200:203], v[48:51], v[68:83]\n" \
  "v_mfma_f32_32x32x16_bf16 v[84:99], a[204:207], v[48:51], v[84:99]\n" \
  "s_waitcnt vmcnt(22)\n" \
  "ds_read_b64_tr_b16 a[224:225], v47 offset:26624\n" \
  "ds_read_b64_tr_b16 a[226:227], v47 offset:26880\n" \
  "ds_read_b64_tr_b16 a[228:229], v47 offset:27648\n" \
  "ds_read_b64_tr_b16 a[230:231], v47 offset:27904\n" \
  "ds_read_b128 v[48:51], v40 offset:768\n" \
  "s_waitcnt lgkmcnt(12)\n" \
  "v_mfma_f32_32x32x16_bf16 v[68:83], a[208:211], v[52:55], v[68:83]\n" \
  "v_mfma_f32_32x32x16_bf16 v[84:99], a[212:215], v[52:55], v[84:99]\n" \
  "ds_read_b128 v[52:55], v41 offset:768\n" \
  "s_waitcnt lgkmcnt(10)\n" \
  "v_mfma_f32_32x32x16_bf16 v[68:83], a[216:219], v[56:59], v[68:83]\n" \
  "s_waitcnt lgkmcnt(8)\n" \
  "v_mfma_f32_32x32x16_bf16 v[84:99], a[220:223], v[56:59], v[84:99]\n" \
  "s_waitcnt vmcnt(20)\n" \
  "ds_read_b64_tr_b16 a[232:233], v47 offset:28672\n" \
  "ds_read_b64_tr_b16 a[234:235], v47 offset:28928\n" \
  "ds_read_b64_tr_b16 a[236:237], v47 offset:29696\n" \
  "ds_read_b64_tr_b16 a[238:239], v47 offset:29952\n" \
  "ds_read_b128 v[56:59], v34 offset:1024\n" \
  "v_add_u32 v34, s31, v34\n" \
  "s_waitcnt lgkmcnt(9)\n" \
  "v_mfma_f32_32x32x16_bf16 v[68:83], a[224:227], v[60:63], v[68:83]\n" \
  "s_waitcnt lgkmcnt(7)\n" \
  "v_mfma_f32_32x32x16_bf16 v[84:99], a[228:231], v[60:63], v[84:99]\n" \
  "s_waitcnt vmcnt(18)\n" \
  "ds_read_b64_tr_b16 a[240:241], v47\n" \
  "ds_read_b64_tr_b16 a[242:243], v47 offset:256\n" \
  "ds_read_b64_tr_b16 a[244:245], v47 offset:1024\n" \
  "ds_read_b64_tr_b16 a[246:247], v47 offset:1280\n" \
  "ds_read_b128 v[60:63], v35 offset:1024\n" \
  "v_add_u32 v35, s31, v35\n" \
  "s_waitcnt lgkmcnt(8)\n" \
  "v_mfma_f32_32x32x16_bf16 v[68:83], a[232:235], v[64:67], v[68:83]\n" \
  "s_waitcnt lgkmcnt(6)\n" \
  "v_mfma_f32_32x32x16_bf16 v[84:99], a[236:239], v[64:67], v[84:99]\n" \
  "s_waitcnt vmcnt(16)\n" \
  "ds_read_b64_tr_b16 a[248:249], v47 offset:2048\n" \
  "ds_read_b64_tr_b16 a[250:251], v47 offset:2304\n" \
  "ds_read_b64_tr_b16 a[252:253], v47 offset:3072\n" \
  "ds_read_b64_tr_b16 a[254:255], v47 offset:3328\n" \
  "ds_read_b128 v[64:67], v36 offset:1024\n" \
  "v_add_u32 v36, s31, v36\n" \
  "s_waitcnt lgkmcnt(8)\n" \
  "v_mfma_f32_32x32x16_bf16 v[68:83], a[240:243], v[48:51], v[68:83]\n" \
  "s_waitcnt lgkmcnt(6)\n" \
  "v_mfma_f32_32x32x16_bf16 v[84:99], a[244:247], v[48:51], v[84:99]\n" \
  "s_waitcnt vmcnt(14)\n" \
  "ds_read_b64_tr_b16 v[192:193], v47 offset:4096\n" \
  "ds_read_b64_tr_b16 v[194:195], v47 offset:4352\n" \
  "ds_read_b64_tr_b16 v[196:197], v47 offset:5120\n" \
  "ds_read_b64_tr_b16 v[198:199], v47 offset:5376\n" \
  "ds_read_b128 v[48:51], v37 offset:1024\n" \
  "v_add_u32 v37, s31, v37\n" \
  "s_waitcnt lgkmcnt(8)\n" \
  "v_mfma_f32_32x32x16_bf16 v[68:83], a[248:251], v[52:55], v[68:83]\n" \
  "s_waitcnt lgkmcnt(6)\n" \
  "v_mfma_f32_32x32x16_bf16 v[84:99], a[252:255], v[52:55], v[84:99]\n" \
  "s_waitcnt vmcnt(12)\n" \
  "ds_read_b64_tr_b16 v[200:201], v47 offset:6144\n" \
  "ds_read_b64_tr_b16 v[202:203], v47 offset:6400\n" \
  "ds_read_b64_tr_b16 v[204:205], v47 offset:7168\n" \
  "ds_read_b64_tr_b16 v[206:207], v47 offset:7424\n" \
  "ds_read_b128 v[52:55], v38 offset:1024\n" \
  "v_add_u32 v38, s31, v38\n" \
  "s_waitcnt lgkmcnt(8)\n" \
  "v_mfma_f32_32x32x16_bf16 v[68:83], v[192:195], v[56:59], v[68:83]\n" \
  "s_waitcnt lgkmcnt(6)\n" \
  "v_mfma_f32_32x32x16_bf16 v[84:99], v[196:199], v[56:59], v[84:99]\n" \
  "s_waitcnt vmcnt(10)\n" \
  "ds_read_b64_tr_b16 v[208:209], v47 offset:8192\n" \
  "ds_read_b64_tr_b16 v[210:211], v47 offset:8448\n" \
  "ds_read_b64_tr_b16 v[212:213], v47 offset:9216\n" \
  "ds_read_b64_tr_b16 v[214:215], v47 offset:9472\n" \
  "ds_read_b128 v[56:59], v39 offset:1024\n" \
  "v_add_u32 v39, s31, v39\n" \
  "s_waitcnt lgkmcnt(8)\n" \
  "v_mfma_f32_32x32x16_bf16 v[68:83], v[200:203], v[60:63], v[68:83]\n" \
  "s_waitcnt lgkmcnt(6)\n" \
  "v_mfma_f32_32x32x16_bf16 v[84:99], v[204:207], v[60:63], v[84:99]\n" \
  "s_waitcnt vmcnt(8)\n" \
  "ds_read_b64_tr_b16 v[216:217], v47 offset:10240\n" \
  "ds_read_b64_tr_b16 v[218:219], v47 offset:10496\n" \
  "ds_read_b64_tr_b16 v[220:221], v47 offset:11264\n" \
  "ds_read_b64_tr_b16 v[222:223], v47 offset:11520\n" \
  "ds_read_b128 v[60:63], v40 offset:1024\n" \
  "v_add_u32 v40, s31, v40\n" \
  "s_waitcnt lgkmcnt(8)\n" \
  "v_mfma_f32_32x32x16_bf16 v[68:83], v[208:211], v[64:67], v[68:83]\n" \
  "s_waitcnt lgkmcnt(6)\n" \
  "v_mfma_f32_32x32x16_bf16 v[84:99], v[212:215], v[64:67], v[84:99]\n" \
  "s_waitcnt vmcnt(6)\n" \
  "ds_read_b64_tr_b16 v[224:225], v47 offset:12288\n" \
  "ds_read_b64_tr_b16 v[226:227], v47 offset:12544\n" \
  "ds_read_b64_tr_b16 v[228:229], v47 offset:13312\n" \
  "ds_read_b64_tr_b16 v[230:231], v47 offset:13568\n" \
  "ds_read_b128 v[64:67], v41 offset:1024\n" \
  "v_add_u32 v41, s31, v41\n" \
  "s_waitcnt lgkmcnt(8)\n" \
  "v_mfma_f32_32x32x16_bf16 v[68:83], v[216:219], v[48:51], v[68:83]\n" \
  "s_waitcnt lgkmcnt(6)\n" \
  "v_mfma_f32_32x32x16_bf16 v[84:99], v[220:223], v[48:51], v[84:99]\n" \
  "s_waitcnt vmcnt(4)\n" \
  "ds_read_b64_tr_b16 v[232:233], v47 offset:14336\n" \
  "ds_read_b64_tr_b16 v[234:235], v47 offset:14592\n" \
  "ds_read_b64_tr_b16 v[236:237], v47 offset:15360\n" \
  "ds_read_b64_tr_b16 v[238:239], v47 offset:15616\n" \
  "s_waitcnt lgkmcnt(7)\n" \
  "v_mfma_f32_32x32x16_bf16 v[68:83], v[224:227], v[52:55], v[68:83]\n" \
  "s_waitcnt lgkmcnt(5)\n" \
  "v_mfma_f32_32x32x16_bf16 v[84:99], v[228:231], v[52:55], v[84:99]\n" \
  "s_waitcnt vmcnt(2)\n" \
  "ds_read_b64_tr_b16 v[240:241], v47 offset:16384\n" \
  "ds_read_b64_tr_b16 v[242:243], v47 offset:16640\n" \
  "ds_read_b64_tr_b16 v[244:245], v47 offset:17408\n" \
  "ds_read_b64_tr_b16 v[246:247], v47 offset:17664\n" \
  "s_waitcnt lgkmcnt(6)\n" \
  "v_mfma_f32_32x32x16_bf16 v[68:83], v[232:235], v[56:59], v[68:83]\n" \
  "s_waitcnt lgkmcnt(4)\n" \
  "v_mfma_f32_32x32x16_bf16 v[84:99], v[236:239], v[56:59], v[84:99]\n" \
  "s_waitcnt vmcnt(0)\n" \
  "ds_read_b64_tr_b16 v[248:249], v47 offset:18432\n" \
  "ds_read_b64_tr_b16 v[250:251], v47 offset:18688\n" \
  "ds_read_b64_tr_b16 v[252:253], v47 offset:19456\n" \
  "ds_read_b64_tr_b16 v[254:255], v47 offset:19712\n" \
  "s_waitcnt lgkmcnt(6)\n" \
  "v_mfma_f32_32x32x16_bf16 v[68:83], v[240:243], v[60:63], v[68:83]\n" \
  "s_waitcnt lgkmcnt(4)\n" \
  "v_mfma_f32_32x32x16_bf16 v[84:99], v[244:247], v[60:63], v[84:99]\n" \
  "s_waitcnt lgkmcnt(2)\n" \
  "v_mfma_f32_32x32x16_bf16 v[68:83], v[248:251], v[64:67], v[68:83]\n" \
  "s_waitcnt lgkmcnt(0)\n" \
  "v_mfma_f32_32x32x16_bf16 v[84:99], v[252:255], v[64:67], v[84:99]\n" \
  "s_mov_b32 s37, 647168\n" \
  "buffer_load_dwordx2 v[20:21], off, s[8:11], s37 sc1\n" \
  "s_nop 1\n" \
  "v_mul_f32 v16, s36, v68\n" \
  "v_mul_f32 v17, s36, v69\n" \
  "v_exp_f32 v16, v16\n" \
  "v_exp_f32 v17, v17\n" \
  "v_add_f32 v16, 1.0, v16\n" \
  "v_add_f32 v17, 1.0, v17\n" \
  "v_rcp_f32 v16, v16\n" \
  "v_rcp_f32 v17, v17\n" \
  "v_mul_f32 v68, v68, v16\n" \
  "v_mul_f32 v69, v69, v17\n" \
  "v_mul_f32 v68, v68, v84\n" \
  "v_mul_f32 v69, v69, v85\n" \
  "v_cvt_pk_bf16_f32 v0, v68, v69\n" \
  "v_mul_f32 v18, s36, v70\n" \
  "v_mul_f32 v19, s36, v71\n" \
  "v_exp_f32 v18, v18\n" \
  "v_exp_f32 v19, v19\n" \
  "v_add_f32 v18, 1.0, v18\n" \
  "v_add_f32 v19, 1.0, v19\n" \
  "v_rcp_f32 v18, v18\n" \
  "v_rcp_f32 v19, v19\n" \
  "v_mul_f32 v70, v70, v18\n" \
  "v_mul_f32 v71, v71, v19\n" \
  "v_mul_f32 v70, v70, v86\n" \
  "v_mul_f32 v71, v71, v87\n" \
  "v_cvt_pk_bf16_f32 v1, v70, v71\n" \
  "v_mul_f32 v16, s36, v72\n" \
  "v_mul_f32 v17, s36, v73\n" \
  "v_exp_f32 v16, v16\n" \
  "v_exp_f32 v17, v17\n" \
  "v_add_f32 v16, 1.0, v16\n" \
  "v_add_f32 v17, 1.0, v17\n" \
  "v_rcp_f32 v16, v16\n" \
  "v_rcp_f32 v17, v17\n" \
  "v_mul_f32 v72, v72, v16\n" \
  "v_mul_f32 v73, v73, v17\n" \
  "v_mul_f32 v72, v72, v88\n" \
  "v_mul_f32 v73, v73, v89\n" \
  "v_cvt_pk_bf16_f32 v2, v72, v73\n" \
  "v_mul_f32 v18, s36, v74\n" \
  "v_mul_f32 v19, s36, v75\n" \
  "v_exp_f32 v18, v18\n" \
  "v_exp_f32 v19, v19\n" \
  "v_add_f32 v18, 1.0, v18\n" \
  "v_add_f32 v19, 1.0, v19\n" \
  "v_rcp_f32 v18, v18\n" \
  "v_rcp_f32 v19, v19\n" \
  "v_mul_f32 v74, v74, v18\n" \
  "v_mul_f32 v75, v75, v19\n" \
  "v_mul_f32 v74, v74, v90\n" \
  "v_mul_f32 v75, v75, v91\n" \
  "v_cvt_pk_bf16_f32 v3, v74, v75\n" \
  "buffer_store_dwordx4 v[0:3], v42, s[8:11], s33 offen sc1\n" \
  "v_mul_f32 v16, s36, v76\n" \
  "v_mul_f32 v17, s36, v77\n" \
  "v_exp_f32 v16, v16\n" \
  "v_exp_f32 v17, v17\n" \
  "v_add_f32 v16, 1.0, v16\n" \
  "v_add_f32 v17, 1.0, v17\n" \
  "v_rcp_f32 v16, v16\n" \
  "v_rcp_f32 v17, v17\n" \
  "v_mul_f32 v76, v76, v16\n" \
  "v_mul_f32 v77, v77, v17\n" \
  "v_mul_f32 v76, v76, v92\n" \
  "v_mul_f32 v77, v77, v93\n" \
  "v_cvt_pk_bf16_f32 v4, v76, v77\n" \
  "v_mul_f32 v18, s36, v78\n" \
  "v_mul_f32 v19, s36, v79\n" \
  "v_exp_f32 v18, v18\n" \
  "v_exp_f32 v19, v19\n" \
  "v_add_f32 v18, 1.0, v18\n" \
  "v_add_f32 v19, 1.0, v19\n" \
  "v_rcp_f32 v18, v18\n" \
  "v_rcp_f32 v19, v19\n" \
  "v_mul_f32 v78, v78, v18\n" \
  "v_mul_f32 v79, v79, v19\n" \
  "v_mul_f32 v78, v78, v94\n" \
  "v_mul_f32 v79, v79, v95\n" \
  "v_cvt_pk_bf16_f32 v5, v78, v79\n" \
  "v_mul_f32 v16, s36, v80\n" \
  "v_mul_f32 v17, s36, v81\n" \
  "v_exp_f32 v16, v16\n" \
  "v_exp_f32 v17, v17\n" \
  "v_add_f32 v16, 1.0, v16\n" \
  "v_add_f32 v17, 1.0, v17\n" \
  "v_rcp_f32 v16, v16\n" \
  "v_rcp_f32 v17, v17\n" \
  "v_mul_f32 v80, v80, v16\n" \
  "v_mul_f32 v81, v81, v17\n" \
  "v_mul_f32 v80, v80, v96\n" \
  "v_mul_f32 v81, v81, v97\n" \
  "v_cvt_pk_bf16_f32 v6, v80, v81\n" \
  "v_mul_f32 v18, s36, v82\n" \
  "v_mul_f32 v19, s36, v83\n" \
  "v_exp_f32 v18, v18\n" \
  "v_exp_f32 v19, v19\n" \
  "v_add_f32 v18, 1.0, v18\n" \
  "v_add_f32 v19, 1.0, v19\n" \
  "v_rcp_f32 v18, v18\n" \
  "v_rcp_f32 v19, v19\n" \
  "v_mul_f32 v82, v82, v18\n" \
  "v_mul_f32 v83, v83, v19\n" \
  "v_mul_f32 v82, v82, v98\n" \
  "v_mul_f32 v83, v83, v99\n" \
  "v_cvt_pk_bf16_f32 v7, v82, v83\n" \
  "buffer_store_dwordx4 v[4:7], v42, s[8:11], s33 offen offset:1024 sc1\n" \
  "s_barrier\n" \
  "s_mov_b32 s30, 40960\n" \
  "s_mov_b32 s32, 40960\n" \
  "s_add_u32 s37, s24, s30\n" \
  "s_add_u32 m0, s37, 0\n" \
  "s_nop 0\n" \
  "buffer_load_dwordx4 v24, s[12:15], s32 offen lds\n" \
  "buffer_load_dwordx4 v25, s[12:15], s32 offen offset:1024 lds\n" \
  "buffer_load_dwordx4 v26, s[12:15], s32 offen offset:2048 lds\n" \
  "buffer_load_dwordx4 v27, s[12:15], s32 offen offset:3072 lds\n" \
  "s_add_u32 s37, s24, s30\n" \
  "s_add_u32 m0, s37, 4096\n" \
  "s_nop 0\n" \
  "buffer_load_dwordx4 v28, s[12:15], s32 offen lds\n" \
  "buffer_load_dwordx4 v29, s[12:15], s32 offen offset:1024 lds\n" \
  "buffer_load_dwordx4 v30, s[12:15], s32 offen offset:2048 lds\n" \
  "buffer_load_dwordx4 v31, s[12:15], s32 offen offset:3072 lds\n" \
  "s_add_u32 s37, s24, s30\n" \
  "s_add_u32 m0, s37, 8192\n" \
  "s_nop 0\n" \
  "buffer_load_dwordx4 v32, s[12:15], s32 offen lds\n" \
  "buffer_load_dwordx4 v33, s[12:15], s32 offen offset:1024 lds\n" \
  "s_mov_b32 s30, 81920\n" \
  "s_mov_b32 s32, 81920\n" \
  "s_add_u32 s37, s24, s30\n" \
  "s_add_u32 m0, s37, 0\n" \
  "s_nop 0\n" \
  "buffer_load_dwordx4 v24, s[12:15], s32 offen lds\n" \
  "buffer_load_dwordx4 v25, s[12:15], s32 offen offset:1024 lds\n" \
  "buffer_load_dwordx4 v26, s[12:15], s32 offen offset:2048 lds\n" \
  "buffer_load_dwordx4 v27, s[12:15], s32 offen offset:3072 lds\n" \
  "s_add_u32 s37, s24, s30\n" \
  "s_add_u32 m0, s37, 4096\n" \
  "s_nop 0\n" \
  "buffer_load_dwordx4 v28, s[12:15], s32 offen lds\n" \
  "buffer_load_dwordx4 v29, s[12:15], s32 offen offset:1024 lds\n" \
  "buffer_load_dwordx4 v30, s[12:15], s32 offen offset:2048 lds\n" \
  "buffer_load_dwordx4 v31, s[12:15], s32 offen offset:3072 lds\n" \
  "s_add_u32 s37, s24, s30\n" \
  "s_add_u32 m0, s37, 8192\n" \
  "s_nop 0\n" \
  "buffer_load_dwordx4 v32, s[12:15], s32 offen lds\n" \
  "buffer_load_dwordx4 v33, s[12:15], s32 offen offset:1024 lds\n" \
  "s_mov_b32 s30, 0\n" \
  "s_mov_b32 s32, 122880\n" \
  "s_mov_b32 s29, 40960\n" \
  "s_mov_b32 s37, 647168\n" \
  "s_waitcnt vmcnt(20)\n" \
  "v_mov_b32 v42, 0x80000000\n" \
  "GA_%=:\n" \
  "v_or_b32 v21, 0x3fff, v21\n" \
  "v_add_u32 v21, 1, v21\n" \
  "v_cmp_ne_u32 vcc, v20, v21\n" \
  "s_cbranch_vccz GAG_%=\n" \
  "s_sleep 2\n" \
  "buffer_load_dwordx2 v[20:21], off, s[8:11], s37 sc1\n" \
  "s_waitcnt vmcnt(0)\n" \
  "s_sub_u32 s38, s38, 1\n" \
  "s_cbranch_scc0 GA_%=\n" \
  "GAG_%=:\n" \
  "v_cmp_ne_u32 vcc, 0, v132\n" \
  "v_cndmask_b32 v44, v44, v20, vcc\n" \
  "s_mov_b32 exec_hi, 0\n" \
  "buffer_store_dword v44, v132, s[8:11], s35 offen sc1\n" \
  "s_mov_b32 exec_hi, -1\n" \
  "s_waitcnt vmcnt(1)\n" \
  "s_barrier\n" \
  "ds_read_b128 v[48:51], v34\n" \
  "ds_read_b128 v[52:55], v35\n" \
  "ds_read_b128 v[56:59], v36\n" \
  "ds_read_b128 v[60:63], v37\n" \
  "GL_%=:\n" \
  "s_add_u32 s37, s29, 40960\n" \
  "s_cmp_eq_u32 s37, 122880\n" \
  "s_cselect_b32 s37, 0, s37\n" \
  "s_sub_u32 s31, s37, s29\n" \
  "ds_read_b128 v[64:67], v38\n" \
  "s_waitcnt lgkmcnt(4)\n" \
  "v_mfma_f32_32x32x16_bf16 v[100:115], a[0:3], v[48:51], 0\n" \
  "v_mfma_f32_32x32x16_bf16 v[116:131], a[4:7], v[48:51], 0\n" \
  "ds_read_b128 v[48:51], v39\n" \
  "s_waitcnt lgkmcnt(4)\n" \
  "v_mfma_f32_32x32x16_bf16 v[100:115], a[8:11], v[52:55], v[100:115]\n" \
  "v_mul_f32 v16, s36, v68\n" \
  "v_mul_f32 v17, s36, v69\n" \
  "v_exp_f32 v16, v16\n" \
  "v_exp_f32 v17, v17\n" \
  "v_mfma_f32_32x32x16_bf16 v[116:131], a[12:15], v[52:55], v[116:131]\n" \
  "v_add_f32 v16, 1.0, v16\n" \
  "v_add_f32 v17, 1.0, v17\n" \
  "v_rcp_f32 v16, v16\n" \
  "v_rcp_f32 v17, v17\n" \
  "ds_read_b128 v[52:55], v40\n" \
  "s_waitcnt lgkmcnt(4)\n" \
  "v_mfma_f32_32x32x16_bf16 v[100:115], a[16:19], v[56:59], v[100:115]\n" \
  "v_mul_f32 v68, v68, v16\n" \
  "v_mul_f32 v69, v69, v17\n" \
  "v_mul_f32 v68, v68, v84\n" \
  "v_mul_f32 v69, v69, v85\n" \
  "v_mfma_f32_32x32x16_bf16 v[116:131], a[20:23], v[56:59], v[116:131]\n" \
  "v_cvt_pk_bf16_f32 v0, v68, v69\n" \
  "v_mul_f32 v18, s36, v70\n" \
  "v_mul_f32 v19, s36, v71\n" \
  "v_exp_f32 v18, v18\n" \
  "ds_read_b128 v[56:59], v41\n" \
  "s_waitcnt lgkmcnt(4)\n" \
  "v_mfma_f32_32x32x16_bf16 v[100:115], a[24:27], v[60:63], v[100:115]\n" \
  "v_exp_f32 v19, v19\n" \
  "v_add_f32 v18, 1.0, v18\n" \
  "v_add_f32 v19, 1.0, v19\n" \
  "v_rcp_f32 v18, v18\n" \
  "v_mfma_f32_32x32x16_bf16 v[116:131], a[28:31], v[60:63], v[116:131]\n" \
  "v_rcp_f32 v19, v19\n" \
  "v_mul_f32 v70, v70, v18\n" \
  "v_mul_f32 v71, v71, v19\n" \
  "v_mul_f32 v70, v70, v86\n" \
  "ds_read_b128 v[60:63], v34 offset:256\n" \
  "s_waitcnt lgkmcnt(4)\n" \
  "v_mfma_f32_32x32x16_bf16 v[100:115], a[32:35], v[64:67], v[100:115]\n" \
  "v_mul_f32 v71, v71, v87\n" \
  "v_cvt_pk_bf16_f32 v1, v70, v71\n" \
  "v_mul_f32 v16, s36, v72\n" \
  "v_mul_f32 v17, s36, v73\n" \
  "v_mfma_f32_32x32x16_bf16 v[116:131], a[36:39], v[64:67], v[116:131]\n" \
  "v_exp_f32 v16, v16\n" \
  "v_exp_f32 v17, v17\n" \
  "v_add_f32 v16, 1.0, v16\n" \
  "v_add_f32 v17, 1.0, v17\n" \
  "ds_read_b128 v[64:67], v35 offset:256\n" \
  "s_waitcnt lgkmcnt(4)\n" \
  "v_mfma_f32_32x32x16_bf16 v[100:115], a[40:43], v[48:51], v[100:115]\n" \
  "v_rcp_f32 v16, v16\n" \
  "v_rcp_f32 v17, v17\n" \
  "v_mul_f32 v72, v72, v16\n" \
  "v_mul_f32 v73, v73, v17\n" \
  "v_mfma_f32_32x32x16_bf16 v[116:131], a[44:47], v[48:51], v[116:131]\n" \
  "v_mul_f32 v72, v72, v88\n" \
  "v_mul_f32 v73, v73, v89\n" \
  "v_cvt_pk_bf16_f32 v2, v72, v73\n" \
  "v_mul_f32 v18, s36, v74\n" \
  "ds_read_b128 v[48:51], v36 offset:256\n" \
  "s_waitcnt lgkmcnt(4)\n" \
  "v_mfma_f32_32x32x16_bf16 v[100:115], a[48:51], v[52:55], v[100:115]\n" \
  "v_mul_f32 v19, s36, v75\n" \
  "v_exp_f32 v18, v18\n" \
  "v_exp_f32 v19, v19\n" \
  "v_add_f32 v18, 1.0, v18\n" \
  "v_mfma_f32_32x32x16_bf16 v[116:131], a[52:55], v[52:55], v[116:131]\n" \
  "v_add_f32 v19, 1.0, v19\n" \
  "v_rcp_f32 v18, v18\n" \
  "v_rcp_f32 v19, v19\n" \
  "v_mul_f32 v74, v74, v18\n" \
  "ds_read_b128 v[52:55], v37 offset:256\n" \
  "s_waitcnt lgkmcnt(4)\n" \
  "v_mfma_f32_32x32x16_bf16 v[100:115], a[56:59], v[56:59], v[100:115]\n" \
  "v_mul_f32 v75, v75, v19\n" \
  "v_mul_f32 v74, v74, v90\n" \
  "v_mul_f32 v75, v75, v91\n" \
  "v_cvt_pk_bf16_f32 v3, v74, v75\n" \
  "v_mfma_f32_32x32x16_bf16 v[116:131], a[60:63], v[56:59], v[116:131]\n" \
  "buffer_store_dwordx4 v[0:3], v42, s[8:11], s33 offen sc1\n" \
  "v_mul_f32 v16, s36, v76\n" \
  "v_mul_f32 v17, s36, v77\n" \
  "v_exp_f32 v16, v16\n" \
  "ds_read_b128 v[56:59], v38 offset:256\n" \
  "s_waitcnt lgkmcnt(4)\n" \
  "v_mfma_f32_32x32x16_bf16 v[100:115], a[64:67], v[60:63], v[100:115]\n" \
  "v_exp_f32 v17, v17\n" \
  "v_add_f32 v16, 1.0, v16\n" \
  "v_add_f32 v17, 1.0, v17\n" \
  "v_rcp_f32 v16, v16\n" \
  "v_mfma_f32_32x32x16_bf16 v[116:131], a[68:71], v[60:63], v[116:131]\n" \
  "v_rcp_f32 v17, v17\n" \
  "v_mul_f32 v76, v76, v16\n" \
  "v_mul_f32 v77, v77, v17\n" \
  "v_mul_f32 v76, v76, v92\n" \
  "ds_read_b128 v[60:63], v39 offset:256\n" \
  "s_waitcnt lgkmcnt(4)\n" \
  "v_mfma_f32_32x32x16_bf16 v[100:115], a[72:75], v[64:67], v[100:115]\n" \
  "v_mul_f32 v77, v77, v93\n" \
  "v_cvt_pk_bf16_f32 v4, v76, v77\n" \
  "v_mul_f32 v18, s36, v78\n" \
  "v_mul_f32 v19, s36, v79\n" \
  "v_mfma_f32_32x32x16_bf16 v[116:131], a[76:79], v[64:67], v[116:131]\n" \
  "v_exp_f32 v18, v18\n" \
  "v_exp_f32 v19, v19\n" \
  "v_add_f32 v18, 1.0, v18\n" \
  "v_add_f32 v19, 1.0, v19\n" \
  "ds_read_b128 v[64:67], v40 offset:256\n" \
  "s_waitcnt lgkmcnt(4)\n" \
  "v_mfma_f32_32x32x16_bf16 v[100:115], a[80:83], v[48:51], v[100:115]\n" \
  "v_rcp_f32 v18, v18\n" \
  "v_rcp_f32 v19, v19\n" \
  "v_mul_f32 v78, v78, v18\n" \
  "v_mul_f32 v79, v79, v19\n" \
  "v_mfma_f32_32x32x16_bf16 v[116:131], a[84:87], v[48:51], v[116:131]\n" \
  "v_mul_f32 v78, v78, v94\n" \
  "v_mul_f32 v79, v79, v95\n" \
  "v_cvt_pk_bf16_f32 v5, v78, v79\n" \
  "v_mul_f32 v16, s36, v80\n" \
  "ds_read_b128 v[48:51], v41 offset:256\n" \
  "s_waitcnt lgkmcnt(4)\n" \
  "v_mfma_f32_32x32x16_bf16 v[100:115], a[88:91], v[52:55], v[100:115]\n" \
  "v_mul_f32 v17, s36, v81\n" \
  "v_exp_f32 v16, v16\n" \
  "v_exp_f32 v17, v17\n" \
  "v_add_f32 v16, 1.0, v16\n" \
  "v_mfma_f32_32x32x16_bf16 v[116:131], a[92:95], v[52:55], v[116:131]\n" \
  "v_add_f32 v17, 1.0, v17\n" \
  "v_rcp_f32 v16, v16\n" \
  "v_rcp_f32 v17, v17\n" \
  "v_mul_f32 v80, v80, v16\n" \
  "ds_read_b128 v[52:55], v34 offset:512\n" \
  "s_waitcnt lgkmcnt(4)\n" \
  "v_mfma_f32_32x32x16_bf16 v[100:115], a[96:99], v[56:59], v[100:115]\n" \
  "v_mul_f32 v81, v81, v17\n" \
  "v_mul_f32 v80, v80, v96\n" \
  "v_mul_f32 v81, v81, v97\n" \
  "v_cvt_pk_bf16_f32 v6, v80, v81\n" \
  "v_mfma_f32_32x32x16_bf16 v[116:131], a[100:103], v[56:59], v[116:131]\n" \
  "v_mul_f32 v18, s36, v82\n" \
  "v_mul_f32 v19, s36, v83\n" \
  "v_exp_f32 v18, v18\n" \
  "v_exp_f32 v19, v19\n" \
  "ds_read_b128 v[56:59], v35 offset:512\n" \
  "s_waitcnt lgkmcnt(4)\n" \
  "v_mfma_f32_32x32x16_bf16 v[100:115], a[104:107], v[60:63], v[100:115]\n" \
  "v_add_f32 v18, 1.0, v18\n" \
  "v_add_f32 v19, 1.0, v19\n" \
  "v_rcp_f32 v18, v18\n" \
  "v_rcp_f32 v19, v19\n" \
  "v_mfma_f32_32x32x16_bf16 v[116:131], a[108:111], v[60:63], v[116:131]\n" \
  "v_mul_f32 v82, v82, v18\n" \
  "v_mul_f32 v83, v83, v19\n" \
  "v_mul_f32 v82, v82, v98\n" \
  "v_mul_f32 v83, v83, v99\n" \
  "ds_read_b128 v[60:63], v36 offset:512\n" \
  "s_waitcnt lgkmcnt(4)\n" \
  "v_mfma_f32_32x32x16_bf16 v[100:115], a[112:115], v[64:67], v[100:115]\n" \
  "v_cvt_pk_bf16_f32 v7, v82, v83\n" \
  "buffer_store_dwordx4 v[4:7], v42, s[8:11], s33 offen offset:1024 sc1\n" \
  "v_mfma_f32_32x32x16_bf16 v[116:131], a[116:119], v[64:67], v[116:131]\n" \
  "ds_read_b128 v[64:67], v37 offset:512\n" \
  "s_waitcnt lgkmcnt(4)\n" \
  "v_mfma_f32_32x32x16_bf16 v[100:115], a[120:123], v[48:51], v[100:115]\n" \
  "v_mfma_f32_32x32x16_bf16 v[116:131], a[124:127], v[48:51], v[116:131]\n" \
  "ds_read_b128 v[48:51], v38 offset:512\n" \
  "s_waitcnt lgkmcnt(4)\n" \
  "v_mfma_f32_32x32x16_bf16 v[100:115], a[128:131], v[52:55], v[100:115]\n" \
  "s_add_u32 s37, s24, s30\n" \
  "s_add_u32 m0, s37, 0\n" \
  "s_nop 0\n" \
  "buffer_load_dwordx4 v24, s[12:15], s32 offen lds\n" \
  "v_mfma_f32_32x32x16_bf16 v[116:131], a[132:135], v[52:55], v[116:131]\n" \
  "ds_read_b128 v[52:55], v39 offset:512\n" \
  "s_waitcnt lgkmcnt(4)\n" \
  "v_mfma_f32_32x32x16_bf16 v[100:115], a[136:139], v[56:59], v[100:115]\n" \
  "v_mfma_f32_32x32x16_bf16 v[116:131], a[140:143], v[56:59], v[116:131]\n" \
  "ds_read_b128 v[56:59], v40 offset:512\n" \
  "s_waitcnt lgkmcnt(4)\n" \
  "v_mfma_f32_32x32x16_bf16 v[100:115], a[144:147], v[60:63], v[100:115]\n" \
  "buffer_load_dwordx4 v25, s[12:15], s32 offen offset:1024 lds\n" \
  "v_mfma_f32_32x32x16_bf16 v[116:131], a[148:151], v[60:63], v[116:131]\n" \
  "ds_read_b128 v[60:63], v41 offset:512\n" \
  "s_waitcnt lgkmcnt(4)\n" \
  "v_mfma_f32_32x32x16_bf16 v[100:115], a[152:155], v[64:67], v[100:115]\n" \
  "v_mfma_f32_32x32x16_bf16 v[116:131], a[156:159], v[64:67], v[116:131]\n" \
  "ds_read_b128 v[64:67], v34 offset:768\n" \
  "s_waitcnt lgkmcnt(4)\n" \
  "v_mfma_f32_32x32x16_bf16 v[100:115], a[160:163], v[48:51], v[100:115]\n" \
  "buffer_load_dwordx4 v26, s[12:15], s32 offen offset:2048 lds\n" \
  "v_mfma_f32_32x32x16_bf16 v[116:131], a[164:167], v[48:51], v[116:131]\n" \
  "ds_read_b128 v[48:51], v35 offset:768\n" \
  "s_waitcnt lgkmcnt(4)\n" \
  "v_mfma_f32_32x32x16_bf16 v[100:115], a[168:171], v[52:55], v[100:115]\n" \
  "v_mfma_f32_32x32x16_bf16 v[116:131], a[172:175], v[52:55], v[116:131]\n" \
  "ds_read_b128 v[52:55], v36 offset:768\n" \
  "s_waitcnt lgkmcnt(4)\n" \
  "v_mfma_f32_32x32x16_bf16 v[100:115], a[176:179], v[56:59], v[100:115]\n" \
  "buffer_load_dwordx4 v27, s[12:15], s32 offen offset:3072 lds\n" \
  "v_mfma_f32_32x32x16_bf16 v[116:131], a[180:183], v[56:59], v[116:131]\n" \
  "ds_read_b128 v[56:59], v37 offset:768\n" \
  "s_waitcnt lgkmcnt(4)\n" \
  "v_mfma_f32_32x32x16_bf16 v[100:115], a[184:187], v[60:63], v[100:115]\n" \
  "v_mfma_f32_32x32x16_bf16 v[116:131], a[188:191], v[60:63], v[116:131]\n" \
  "ds_read_b128 v[60:63], v38 offset:768\n" \
  "s_waitcnt lgkmcnt(4)\n" \
  "v_mfma_f32_32x32x16_bf16 v[100:115], a[192:195], v[64:67], v[100:115]\n" \
  "s_add_u32 s37, s24, s30\n" \
  "s_add_u32 m0, s37, 4096\n" \
  "s_nop 0\n" \
  "buffer_load_dwordx4 v28, s[12:15], s32 offen lds\n" \
  "v_mfma_f32_32x32x16_bf16 v[116:131], a[196:199], v[64:67], v[116:131]\n" \
  "ds_read_b128 v[64:67], v39 offset:768\n" \
  "s_waitcnt lgkmcnt(4)\n" \
  "v_mfma_f32_32x32x16_bf16 v[100:115], a[200:203], v[48:51], v[100:115]\n" \
  "v_mfma_f32_32x32x16_bf16 v[116:131], a[204:207], v[48:51], v[116:131]\n" \
  "ds_read_b128 v[48:51], v40 offset:768\n" \
  "s_waitcnt lgkmcnt(4)\n" \
  "v_mfma_f32_32x32x16_bf16 v[100:115], a[208:211], v[52:55], v[100:115]\n" \
  "buffer_load_dwordx4 v29, s[12:15], s32 offen offset:1024 lds\n" \
  "v_mfma_f32_32x32x16_bf16 v[116:131], a[212:215], v[52:55], v[116:131]\n" \
  "ds_read_b128 v[52:55], v41 offset:768\n" \
  "s_waitcnt lgkmcnt(4)\n" \
  "v_mfma_f32_32x32x16_bf16 v[100:115], a[216:219], v[56:59], v[100:115]\n" \
  "v_mfma_f32_32x32x16_bf16 v[116:131], a[220:223], v[56:59], v[116:131]\n" \
  "ds_read_b128 v[56:59], v34 offset:1024\n" \
  "v_add_u32 v34, s31, v34\n" \
  "s_waitcnt lgkmcnt(4)\n" \
  "v_mfma_f32_32x32x16_bf16 v[100:115], a[224:227], v[60:63], v[100:115]\n" \
  "buffer_load_dwordx4 v30, s[12:15], s32 offen offset:2048 lds\n" \
  "v_mfma_f32_32x32x16_bf16 v[116:131], a[228:231], v[60:63], v[116:131]\n" \
  "ds_read_b128 v[60:63], v35 offset:1024\n" \
  "v_add_u32 v35, s31, v35\n" \
  "s_waitcnt lgkmcnt(4)\n" \
  "v_mfma_f32_32x32x16_bf16 v[100:115], a[232:235], v[64:67], v[100:115]\n" \
  "v_mfma_f32_32x32x16_bf16 v[116:131], a[236:239], v[64:67], v[116:131]\n" \
  "ds_read_b128 v[64:67], v36 offset:1024\n" \
  "v_add_u32 v36, s31, v36\n" \
  "s_waitcnt lgkmcnt(4)\n" \
  "v_mfma_f32_32x32x16_bf16 v[100:115], a[240:243], v[48:51], v[100:115]\n" \
  "buffer_load_dwordx4 v31, s[12:15], s32 offen offset:3072 lds\n" \
  "v_mfma_f32_32x32x16_bf16 v[116:131], a[244:247], v[48:51], v[116:131]\n" \
  "ds_read_b128 v[48:51], v37 offset:1024\n" \
  "v_add_u32 v37, s31, v37\n" \
  "s_waitcnt lgkmcnt(4)\n" \
  "v_mfma_f32_32x32x16_bf16 v[100:115], a[248:251], v[52:55], v[100:115]\n" \
  "v_mfma_f32_32x32x16_bf16 v[116:131], a[252:255], v[52:55], v[116:131]\n" \
  "ds_read_b128 v[52:55], v38 offset:1024\n" \
  "v_add_u32 v38, s31, v38\n" \
  "s_waitcnt lgkmcnt(4)\n" \
  "v_mfma_f32_32x32x16_bf16 v[100:115], v[192:195], v[56:59], v[100:115]\n" \
  "s_add_u32 s37, s24, s30\n" \
  "s_add_u32 m0, s37, 8192\n" \
  "s_nop 0\n" \
  "buffer_load_dwordx4 v32, s[12:15], s32 offen lds\n" \
  "v_mfma_f32_32x32x16_bf16 v[116:131], v[196:199], v[56:59], v[116:131]\n" \
  "ds_read_b128 v[56:59], v39 offset:1024\n" \
  "v_add_u32 v39, s31, v39\n" \
  "s_waitcnt lgkmcnt(4)\n" \
  "v_mfma_f32_32x32x16_bf16 v[100:115], v[200:203], v[60:63], v[100:115]\n" \
  "v_mfma_f32_32x32x16_bf16 v[116:131], v[204:207], v[60:63], v[116:131]\n" \
  "ds_read_b128 v[60:63], v40 offset:1024\n" \
  "v_add_u32 v40, s31, v40\n" \
  "s_waitcnt lgkmcnt(4)\n" \
  "v_mfma_f32_32x32x16_bf16 v[100:115], v[208:211], v[64:67], v[100:115]\n" \
  "buffer_load_dwordx4 v33, s[12:15], s32 offen offset:1024 lds\n" \
  "v_mfma_f32_32x32x16_bf16 v[116:131], v[212:215], v[64:67], v[116:131]\n" \
  "s_add_u32 s33, s33, s34\n" \
  "v_mov_b32 v42, v43\n" \
  "ds_read_b128 v[64:67], v41 offset:1024\n" \
  "v_add_u32 v41, s31, v41\n" \
  "s_waitcnt lgkmcnt(4)\n" \
  "v_mfma_f32_32x32x16_bf16 v[100:115], v[216:219], v[48:51], v[100:115]\n" \
  "v_mfma_f32_32x32x16_bf16 v[116:131], v[220:223], v[48:51], v[116:131]\n" \
  "s_waitcnt lgkmcnt(0)\n" \
  "s_waitcnt vmcnt(10)\n" \
  "s_barrier\n" \
  "s_mov_b32 exec_hi, 0\n" \
  "buffer_store_dword v44, v132, s[8:11], s35 offen sc1\n" \
  "s_mov_b32 exec_hi, -1\n" \
  "v_add_u32 v44, 1, v44\n" \
  "ds_read_b128 v[48:51], v34\n" \
  "v_mfma_f32_32x32x16_bf16 v[100:115], v[224:227], v[52:55], v[100:115]\n" \
  "v_mfma_f32_32x32x16_bf16 v[116:131], v[228:231], v[52:55], v[116:131]\n" \
  "ds_read_b128 v[52:55], v35\n" \
  "v_mfma_f32_32x32x16_bf16 v[100:115], v[232:235], v[56:59], v[100:115]\n" \
  "v_mfma_f32_32x32x16_bf16 v[116:131], v[236:239], v[56:59], v[116:131]\n" \
  "ds_read_b128 v[56:59], v36\n" \
  "v_mfma_f32_32x32x16_bf16 v[100:115], v[240:243], v[60:63], v[100:115]\n" \
  "v_mfma_f32_32x32x16_bf16 v[116:131], v[244:247], v[60:63], v[116:131]\n" \
  "ds_read_b128 v[60:63], v37\n" \
  "v_mfma_f32_32x32x16_bf16 v[100:115], v[248:251], v[64:67], v[100:115]\n" \
  "v_mfma_f32_32x32x16_bf16 v[116:131], v[252:255], v[64:67], v[116:131]\n" \
  "s_mov_b32 s30, s29\n" \
  "s_add_u32 s29, s29, s31\n" \
  "s_add_u32 s32, s32, 40960\n" \
  "s_min_u32 s32, s32, 2580480\n" \
  "s_cmp_eq_u32 s28, 62\n" \
  "s_cbranch_scc1 GE_%=\n" \
  "s_add_u32 s37, s29, 40960\n" \
  "s_cmp_eq_u32 s37, 122880\n" \
  "s_cselect_b32 s37, 0, s37\n" \
  "s_sub_u32 s31, s37, s29\n" \
  "ds_read_b128 v[64:67], v38\n" \
  "s_waitcnt lgkmcnt(4)\n" \
  "v_mfma_f32_32x32x16_bf16 v[68:83], a[0:3], v[48:51], 0\n" \
  "v_mfma_f32_32x32x16_bf16 v[84:99], a[4:7], v[48:51], 0\n" \
  "ds_read_b128 v[48:51], v39\n" \
  "s_waitcnt lgkmcnt(4)\n" \
  "v_mfma_f32_32x32x16_bf16 v[68:83], a[8:11], v[52:55], v[68:83]\n" \
  "v_mul_f32 v16, s36, v100\n" \
  "v_mul_f32 v17, s36, v101\n" \
  "v_exp_f32 v16, v16\n" \
  "v_exp_f32 v17, v17\n" \
  "v_mfma_f32_32x32x16_bf16 v[84:99], a[12:15], v[52:55], v[84:99]\n" \
  "v_add_f32 v16, 1.0, v16\n" \
  "v_add_f32 v17, 1.0, v17\n" \
  "v_rcp_f32 v16, v16\n" \
  "v_rcp_f32 v17, v17\n" \
  "ds_read_b128 v[52:55], v40\n" \
  "s_waitcnt lgkmcnt(4)\n" \
  "v_mfma_f32_32x32x16_bf16 v[68:83], a[16:19], v[56:59], v[68:83]\n" \
  "v_mul_f32 v100, v100, v16\n" \
  "v_mul_f32 v101, v101, v17\n" \
  "v_mul_f32 v100, v100, v116\n" \
  "v_mul_f32 v101, v101, v117\n" \
  "v_mfma_f32_32x32x16_bf16 v[84:99], a[20:23], v[56:59], v[84:99]\n" \
  "v_cvt_pk_bf16_f32 v0, v100, v101\n" \
  "v_mul_f32 v18, s36, v102\n" \
  "v_mul_f32 v19, s36, v103\n" \
  "v_exp_f32 v18, v18\n" \
  "ds_read_b128 v[56:59], v41\n" \
  "s_waitcnt lgkmcnt(4)\n" \
  "v_mfma_f32_32x32x16_bf16 v[68:83], a[24:27], v[60:63], v[68:83]\n" \
  "v_exp_f32 v19, v19\n" \
  "v_add_f32 v18, 1.0, v18\n" \
  "v_add_f32 v19, 1.0, v19\n" \
  "v_rcp_f32 v18, v18\n" \
  "v_mfma_f32_32x32x16_bf16 v[84:99], a[28:31], v[60:63], v[84:99]\n" \
  "v_rcp_f32 v19, v19\n" \
  "v_mul_f32 v102, v102, v18\n" \
  "v_mul_f32 v103, v103, v19\n" \
  "v_mul_f32 v102, v102, v118\n" \
  "ds_read_b128 v[60:63], v34 offset:256\n" \
  "s_waitcnt lgkmcnt(4)\n" \
  "v_mfma_f32_32x32x16_bf16 v[68:83], a[32:35], v[64:67], v[68:83]\n" \
  "v_mul_f32 v103, v103, v119\n" \
  "v_cvt_pk_bf16_f32 v1, v102, v103\n" \
  "v_mul_f32 v16, s36, v104\n" \
  "v_mul_f32 v17, s36, v105\n" \
  "v_mfma_f32_32x32x16_bf16 v[84:99], a[36:39], v[64:67], v[84:99]\n" \
  "v_exp_f32 v16, v16\n" \
  "v_exp_f32 v17, v17\n" \
  "v_add_f32 v16, 1.0, v16\n" \
  "v_add_f32 v17, 1.0, v17\n" \
  "ds_read_b128 v[64:67], v35 offset:256\n" \
  "s_waitcnt lgkmcnt(4)\n" \
  "v_mfma_f32_32x32x16_bf16 v[68:83], a[40:43], v[48:51], v[68:83]\n" \
  "v_rcp_f32 v16, v16\n" \
  "v_rcp_f32 v17, v17\n" \
  "v_mul_f32 v104, v104, v16\n" \
  "v_mul_f32 v105, v105, v17\n" \
  "v_mfma_f32_32x32x16_bf16 v[84:99], a[44:47], v[48:51], v[84:99]\n" \
  "v_mul_f32 v104, v104, v120\n" \
  "v_mul_f32 v105, v105, v121\n" \
  "v_cvt_pk_bf16_f32 v2, v104, v105\n" \
  "v_mul_f32 v18, s36, v106\n" \
  "ds_read_b128 v[48:51], v36 offset:256\n" \
  "s_waitcnt lgkmcnt(4)\n" \
  "v_mfma_f32_32x32x16_bf16 v[68:83], a[48:51], v[52:55], v[68:83]\n" \
  "v_mul_f32 v19, s36, v107\n" \
  "v_exp_f32 v18, v18\n" \
  "v_exp_f32 v19, v19\n" \
  "v_add_f32 v18, 1.0, v18\n" \
  "v_mfma_f32_32x32x16_bf16 v[84:99], a[52:55], v[52:55], v[84:99]\n" \
  "v_add_f32 v19, 1.0, v19\n" \
  "v_rcp_f32 v18, v18\n" \
  "v_rcp_f32 v19, v19\n" \
  "v_mul_f32 v106, v106, v18\n" \
  "ds_read_b128 v[52:55], v37 offset:256\n" \
  "s_waitcnt lgkmcnt(4)\n" \
  "v_mfma_f32_32x32x16_bf16 v[68:83], a[56:59], v[56:59], v[68:83]\n" \
  "v_mul_f32 v107, v107, v19\n" \
  "v_mul_f32 v106, v106, v122\n" \
  "v_mul_f32 v107, v107, v123\n" \
  "v_cvt_pk_bf16_f32 v3, v106, v107\n" \
  "v_mfma_f32_32x32x16_bf16 v[84:99], a[60:63], v[56:59], v[84:99]\n" \
  "buffer_store_dwordx4 v[0:3], v42, s[8:11], s33 offen sc1\n" \
  "v_mul_f32 v16, s36, v108\n" \
  "v_mul_f32 v17, s36, v109\n" \
  "v_exp_f32 v16, v16\n" \
  "ds_read_b128 v[56:59], v38 offset:256\n" \
  "s_waitcnt lgkmcnt(4)\n" \
  "v_mfma_f32_32x32x16_bf16 v[68:83], a[64:67], v[60:63], v[68:83]\n" \
  "v_exp_f32 v17, v17\n" \
  "v_add_f32 v16, 1.0, v16\n" \
  "v_add_f32 v17, 1.0, v17\n" \
  "v_rcp_f32 v16, v16\n" \
  "v_mfma_f32_32x32x16_bf16 v[84:99], a[68:71], v[60:63], v[84:99]\n" \
  "v_rcp_f32 v17, v17\n" \
  "v_mul_f32 v108, v108, v16\n" \
  "v_mul_f32 v109, v109, v17\n" \
  "v_mul_f32 v108, v108, v124\n" \
  "ds_read_b128 v[60:63], v39 offset:256\n" \
  "s_waitcnt lgkmcnt(4)\n" \
  "v_mfma_f32_32x32x16_bf16 v[68:83], a[72:75], v[64:67], v[68:83]\n" \
  "v_mul_f32 v109, v109, v125\n" \
  "v_cvt_pk_bf16_f32 v4, v108, v109\n" \
  "v_mul_f32 v18, s36, v110\n" \
  "v_mul_f32 v19, s36, v111\n" \
  "v_mfma_f32_32x32x16_bf16 v[84:99], a[76:79], v[64:67], v[84:99]\n" \
  "v_exp_f32 v18, v18\n" \
  "v_exp_f32 v19, v19\n" \
  "v_add_f32 v18, 1.0, v18\n" \
  "v_add_f32 v19, 1.0, v19\n" \
  "ds_read_b128 v[64:67], v40 offset:256\n" \
  "s_waitcnt lgkmcnt(4)\n" \
  "v_mfma_f32_32x32x16_bf16 v[68:83], a[80:83], v[48:51], v[68:83]\n" \
  "v_rcp_f32 v18, v18\n" \
  "v_rcp_f32 v19, v19\n" \
  "v_mul_f32 v110, v110, v18\n" \
  "v_mul_f32 v111, v111, v19\n" \
  "v_mfma_f32_32x32x16_bf16 v[84:99], a[84:87], v[48:51], v[84:99]\n" \
  "v_mul_f32 v110, v110, v126\n" \
  "v_mul_f32 v111, v111, v127\n" \
  "v_cvt_pk_bf16_f32 v5, v110, v111\n" \
  "v_mul_f32 v16, s36, v112\n" \
  "ds_read_b128 v[48:51], v41 offset:256\n" \
  "s_waitcnt lgkmcnt(4)\n" \
  "v_mfma_f32_32x32x16_bf16 v[68:83], a[88:91], v[52:55], v[68:83]\n" \
  "v_mul_f32 v17, s36, v113\n" \
  "v_exp_f32 v16, v16\n" \
  "v_exp_f32 v17, v17\n" \
  "v_add_f32 v16, 1.0, v16\n" \
  "v_mfma_f32_32x32x16_bf16 v[84:99], a[92:95], v[52:55], v[84:99]\n" \
  "v_add_f32 v17, 1.0, v17\n" \
  "v_rcp_f32 v16, v16\n" \
  "v_rcp_f32 v17, v17\n" \
  "v_mul_f32 v112, v112, v16\n" \
  "ds_read_b128 v[52:55], v34 offset:512\n" \
  "s_waitcnt lgkmcnt(4)\n" \
  "v_mfma_f32_32x32x16_bf16 v[68:83], a[96:99], v[56:59], v[68:83]\n" \
  "v_mul_f32 v113, v113, v17\n" \
  "v_mul_f32 v112, v112, v128\n" \
  "v_mul_f32 v113, v113, v129\n" \
  "v_cvt_pk_bf16_f32 v6, v112, v113\n" \
  "v_mfma_f32_32x32x16_bf16 v[84:99], a[100:103], v[56:59], v[84:99]\n" \
  "v_mul_f32 v18, s36, v114\n" \
  "v_mul_f32 v19, s36, v115\n" \
  "v_exp_f32 v18, v18\n" \
  "v_exp_f32 v19, v19\n" \
  "ds_read_b128 v[56:59], v35 offset:512\n" \
  "s_waitcnt lgkmcnt(4)\n" \
  "v_mfma_f32_32x32x16_bf16 v[68:83], a[104:107], v[60:63], v[68:83]\n" \
  "v_add_f32 v18, 1.0, v18\n" \
  "v_add_f32 v19, 1.0, v19\n" \
  "v_rcp_f32 v18, v18\n" \
  "v_rcp_f32 v19, v19\n" \
  "v_mfma_f32_32x32x16_bf16 v[84:99], a[108:111], v[60:63], v[84:99]\n" \
  "v_mul_f32 v114, v114, v18\n" \
  "v_mul_f32 v115, v115, v19\n" \
  "v_mul_f32 v114, v114, v130\n" \
  "v_mul_f32 v115, v115, v131\n" \
  "ds_read_b128 v[60:63], v36 offset:512\n" \
  "s_waitcnt lgkmcnt(4)\n" \
  "v_mfma_f32_32x32x16_bf16 v[68:83], a[112:115], v[64:67], v[68:83]\n" \
  "v_cvt_pk_bf16_f32 v7, v114, v115\n" \
  "buffer_store_dwordx4 v[4:7], v42, s[8:11], s33 offen offset:1024 sc1\n" \
  "v_mfma_f32_32x32x16_bf16 v[84:99], a[116:119], v[64:67], v[84:99]\n" \
  "ds_read_b128 v[64:67], v37 offset:512\n" \
  "s_waitcnt lgkmcnt(4)\n" \
  "v_mfma_f32_32x32x16_bf16 v[68:83], a[120:123], v[48:51], v[68:83]\n" \
  "v_mfma_f32_32x32x16_bf16 v[84:99], a[124:127], v[48:51], v[84:99]\n" \
  "ds_read_b128 v[48:51], v38 offset:512\n" \
  "s_waitcnt lgkmcnt(4)\n" \
  "v_mfma_f32_32x32x16_bf16 v[68:83], a[128:131], v[52:55], v[68:83]\n" \
  "s_add_u32 s37, s24, s30\n" \
  "s_add_u32 m0, s37, 0\n" \
  "s_nop 0\n" \
  "buffer_load_dwordx4 v24, s[12:15], s32 offen lds\n" \
  "v_mfma_f32_32x32x16_bf16 v[84:99], a[132:135], v[52:55], v[84:99]\n" \
  "ds_read_b128 v[52:55], v39 offset:512\n" \
  "s_waitcnt lgkmcnt(4)\n" \
  "v_mfma_f32_32x32x16_bf16 v[68:83], a[136:139], v[56:59], v[68:83]\n" \
  "v_mfma_f32_32x32x16_bf16 v[84:99], a[140:143], v[56:59], v[84:99]\n" \
  "ds_read_b128 v[56:59], v40 offset:512\n" \
  "s_waitcnt lgkmcnt(4)\n" \
  "v_mfma_f32_32x32x16_bf16 v[68:83], a[144:147], v[60:63], v[68:83]\n" \
  "buffer_load_dwordx4 v25, s[12:15], s32 offen offset:1024 lds\n" \
  "v_mfma_f32_32x32x16_bf16 v[84:99], a[148:151], v[60:63], v[84:99]\n" \
  "ds_read_b128 v[60:63], v41 offset:512\n" \
  "s_waitcnt lgkmcnt(4)\n" \
  "v_mfma_f32_32x32x16_bf16 v[68:83], a[152:155], v[64:67], v[68:83]\n" \
  "v_mfma_f32_32x32x16_bf16 v[84:99], a[156:159], v[64:67], v[84:99]\n" \
  "ds_read_b128 v[64:67], v34 offset:768\n" \
  "s_waitcnt lgkmcnt(4)\n" \
  "v_mfma_f32_32x32x16_bf16 v[68:83], a[160:163], v[48:51], v[68:83]\n" \
  "buffer_load_dwordx4 v26, s[12:15], s32 offen offset:2048 lds\n" \
  "v_mfma_f32_32x32x16_bf16 v[84:99], a[164:167], v[48:51], v[84:99]\n" \
  "ds_read_b128 v[48:51], v35 offset:768\n" \
  "s_waitcnt lgkmcnt(4)\n" \
  "v_mfma_f32_32x32x16_bf16 v[68:83], a[168:171], v[52:55], v[68:83]\n" \
  "v_mfma_f32_32x32x16_bf16 v[84:99], a[172:175], v[52:55], v[84:99]\n" \
  "ds_read_b128 v[52:55], v36 offset:768\n" \
  "s_waitcnt lgkmcnt(4)\n" \
  "v_mfma_f32_32x32x16_bf16 v[68:83], a[176:179], v[56:59], v[68:83]\n" \
  "buffer_load_dwordx4 v27, s[12:15], s32 offen offset:3072 lds\n" \
  "v_mfma_f32_32x32x16_bf16 v[84:99], a[180:183], v[56:59], v[84:99]\n" \
  "ds_read_b128 v[56:59], v37 offset:768\n" \
  "s_waitcnt lgkmcnt(4)\n" \
  "v_mfma_f32_32x32x16_bf16 v[68:83], a[184:187], v[60:63], v[68:83]\n" \
  "v_mfma_f32_32x32x16_bf16 v[84:99], a[188:191], v[60:63], v[84:99]\n" \
  "ds_read_b128 v[60:63], v38 offset:768\n" \
  "s_waitcnt lgkmcnt(4)\n" \
  "v_mfma_f32_32x32x16_bf16 v[68:83], a[192:195], v[64:67], v[68:83]\n" \
  "s_add_u32 s37, s24, s30\n" \
  "s_add_u32 m0, s37, 4096\n" \
  "s_nop 0\n" \
  "buffer_load_dwordx4 v28, s[12:15], s32 offen lds\n" \
  "v_mfma_f32_32x32x16_bf16 v[84:99], a[196:199], v[64:67], v[84:99]\n" \
  "ds_read_b128 v[64:67], v39 offset:768\n" \
  "s_waitcnt lgkmcnt(4)\n" \
  "v_mfma_f32_32x32x16_bf16 v[68:83], a[200:203], v[48:51], v[68:83]\n" \
  "v_mfma_f32_32x32x16_bf16 v[84:99], a[204:207], v[48:51], v[84:99]\n" \
  "ds_read_b128 v[48:51], v40 offset:768\n" \
  "s_waitcnt lgkmcnt(4)\n" \
  "v_mfma_f32_32x32x16_bf16 v[68:83], a[208:211], v[52:55], v[68:83]\n" \
  "buffer_load_dwordx4 v29, s[12:15], s32 offen offset:1024 lds\n" \
  "v_mfma_f32_32x32x16_bf16 v[84:99], a[212:215], v[52:55], v[84:99]\n" \
  "ds_read_b128 v[52:55], v41 offset:768\n" \
  "s_waitcnt lgkmcnt(4)\n" \
  "v_mfma_f32_32x32x16_bf16 v[68:83], a[216:219], v[56:59], v[68:83]\n" \
  "v_mfma_f32_32x32x16_bf16 v[84:99], a[220:223], v[56:59], v[84:99]\n" \
  "ds_read_b128 v[56:59], v34 offset:1024\n" \
  "v_add_u32 v34, s31, v34\n" \
  "s_waitcnt lgkmcnt(4)\n" \
  "v_mfma_f32_32x32x16_bf16 v[68:83], a[224:227], v[60:63], v[68:83]\n" \
  "buffer_load_dwordx4 v30, s[12:15], s32 offen offset:2048 lds\n" \
  "v_mfma_f32_32x32x16_bf16 v[84:99], a[228:231], v[60:63], v[84:99]\n" \
  "ds_read_b128 v[60:63], v35 offset:1024\n" \
  "v_add_u32 v35, s31, v35\n" \
  "s_waitcnt lgkmcnt(4)\n" \
  "v_mfma_f32_32x32x16_bf16 v[68:83], a[232:235], v[64:67], v[68:83]\n" \
  "v_mfma_f32_32x32x16_bf16 v[84:99], a[236:239], v[64:67], v[84:99]\n" \
  "ds_read_b128 v[64:67], v36 offset:1024\n" \
  "v_add_u32 v36, s31, v36\n" \
  "s_waitcnt lgkmcnt(4)\n" \
  "v_mfma_f32_32x32x16_bf16 v[68:83], a[240:243], v[48:51], v[68:83]\n" \
  "buffer_load_dwordx4 v31, s[12:15], s32 offen offset:3072 lds\n" \
  "v_mfma_f32_32x32x16_bf16 v[84:99], a[244:247], v[48:51], v[84:99]\n" \
  "ds_read_b128 v[48:51], v37 offset:1024\n" \
  "v_add_u32 v37, s31, v37\n" \
  "s_waitcnt lgkmcnt(4)\n" \
  "v_mfma_f32_32x32x16_bf16 v[68:83], a[248:251], v[52:55], v[68:83]\n" \
  "v_mfma_f32_32x32x16_bf16 v[84:99], a[252:255], v[52:55], v[84:99]\n" \
  "ds_read_b128 v[52:55], v38 offset:1024\n" \
  "v_add_u32 v38, s31, v38\n" \
  "s_waitcnt lgkmcnt(4)\n" \
  "v_mfma_f32_32x32x16_bf16 v[68:83], v[192:195], v[56:59], v[68:83]\n" \
  "s_add_u32 s37, s24, s30\n" \
  "s_add_u32 m0, s37, 8192\n" \
  "s_nop 0\n" \
  "buffer_load_dwordx4 v32, s[12:15], s32 offen lds\n" \
  "v_mfma_f32_32x32x16_bf16 v[84:99], v[196:199], v[56:59], v[84:99]\n" \
  "ds_read_b128 v[56:59], v39 offset:1024\n" \
  "v_add_u32 v39, s31, v39\n" \
  "s_waitcnt lgkmcnt(4)\n" \
  "v_mfma_f32_32x32x16_bf16 v[68:83], v[200:203], v[60:63], v[68:83]\n" \
  "v_mfma_f32_32x32x16_bf16 v[84:99], v[204:207], v[60:63], v[84:99]\n" \
  "ds_read_b128 v[60:63], v40 offset:1024\n" \
  "v_add_u32 v40, s31, v40\n" \
  "s_waitcnt lgkmcnt(4)\n" \
  "v_mfma_f32_32x32x16_bf16 v[68:83], v[208:211], v[64:67], v[68:83]\n" \
  "buffer_load_dwordx4 v33, s[12:15], s32 offen offset:1024 lds\n" \
  "v_mfma_f32_32x32x16_bf16 v[84:99], v[212:215], v[64:67], v[84:99]\n" \
  "s_add_u32 s33, s33, s34\n" \
  "v_mov_b32 v42, v43\n" \
  "ds_read_b128 v[64:67], v41 offset:1024\n" \
  "v_add_u32 v41, s31, v41\n" \
  "s_waitcnt lgkmcnt(4)\n" \
  "v_mfma_f32_32x32x16_bf16 v[68:83], v[216:219], v[48:51], v[68:83]\n" \
  "v_mfma_f32_32x32x16_bf16 v[84:99], v[220:223], v[48:51], v[84:99]\n" \
  "s_waitcnt lgkmcnt(0)\n" \
  "s_waitcnt vmcnt(10)\n" \
  "s_barrier\n" \
  "s_mov_b32 exec_hi, 0\n" \
  "buffer_store_dword v44, v132, s[8:11], s35 offen sc1\n" \
  "s_mov_b32 exec_hi, -1\n" \
  "v_add_u32 v44, 1, v44\n" \
  "ds_read_b128 v[48:51], v34\n" \
  "v_mfma_f32_32x32x16_bf16 v[68:83], v[224:227], v[52:55], v[68:83]\n" \
  "v_mfma_f32_32x32x16_bf16 v[84:99], v[228:231], v[52:55], v[84:99]\n" \
  "ds_read_b128 v[52:55], v35\n" \
  "v_mfma_f32_32x32x16_bf16 v[68:83], v[232:235], v[56:59], v[68:83]\n" \
  "v_mfma_f32_32x32x16_bf16 v[84:99], v[236:239], v[56:59], v[84:99]\n" \
  "ds_read_b128 v[56:59], v36\n" \
  "v_mfma_f32_32x32x16_bf16 v[68:83], v[240:243], v[60:63], v[68:83]\n" \
  "v_mfma_f32_32x32x16_bf16 v[84:99], v[244:247], v[60:63], v[84:99]\n" \
  "ds_read_b128 v[60:63], v37\n" \
  "v_mfma_f32_32x32x16_bf16 v[68:83], v[248:251], v[64:67], v[68:83]\n" \
  "v_mfma_f32_32x32x16_bf16 v[84:99], v[252:255], v[64:67], v[84:99]\n" \
  "s_mov_b32 s30, s29\n" \
  "s_add_u32 s29, s29, s31\n" \
  "s_add_u32 s32, s32, 40960\n" \
  "s_min_u32 s32, s32, 2580480\n" \
  "s_add_u32 s28, s28, 2\n" \
  "s_branch GL_%=\n" \
  "GE_%=:\n" \
  "s_nop 7\n" \
  "s_nop 3\n" \
  "v_mul_f32 v16, s36, v100\n" \
  "v_mul_f32 v17, s36, v101\n" \
  "v_exp_f32 v16, v16\n" \
  "v_exp_f32 v17, v17\n" \
  "v_add_f32 v16, 1.0, v16\n" \
  "v_add_f32 v17, 1.0, v17\n" \
  "v_rcp_f32 v16, v16\n" \
  "v_rcp_f32 v17, v17\n" \
  "v_mul_f32 v100, v100, v16\n" \
  "v_mul_f32 v101, v101, v17\n" \
  "v_mul_f32 v100, v100, v116\n" \
  "v_mul_f32 v101, v101, v117\n" \
  "v_cvt_pk_bf16_f32 v0, v100, v101\n" \
  "v_mul_f32 v18, s36, v102\n" \
  "v_mul_f32 v19, s36, v103\n" \
  "v_exp_f32 v18, v18\n" \
  "v_exp_f32 v19, v19\n" \
  "v_add_f32 v18, 1.0, v18\n" \
  "v_add_f32 v19, 1.0, v19\n" \
  "v_rcp_f32 v18, v18\n" \
  "v_rcp_f32 v19, v19\n" \
  "v_mul_f32 v102, v102, v18\n" \
  "v_mul_f32 v103, v103, v19\n" \
  "v_mul_f32 v102, v102, v118\n" \
  "v_mul_f32 v103, v103, v119\n" \
  "v_cvt_pk_bf16_f32 v1, v102, v103\n" \
  "v_mul_f32 v16, s36, v104\n" \
  "v_mul_f32 v17, s36, v105\n" \
  "v_exp_f32 v16, v16\n" \
  "v_exp_f32 v17, v17\n" \
  "v_add_f32 v16, 1.0, v16\n" \
  "v_add_f32 v17, 1.0, v17\n" \
  "v_rcp_f32 v16, v16\n" \
  "v_rcp_f32 v17, v17\n" \
  "v_mul_f32 v104, v104, v16\n" \
  "v_mul_f32 v105, v105, v17\n" \
  "v_mul_f32 v104, v104, v120\n" \
  "v_mul_f32 v105, v105, v121\n" \
  "v_cvt_pk_bf16_f32 v2, v104, v105\n" \
  "v_mul_f32 v18, s36, v106\n" \
  "v_mul_f32 v19, s36, v107\n" \
  "v_exp_f32 v18, v18\n" \
  "v_exp_f32 v19, v19\n" \
  "v_add_f32 v18, 1.0, v18\n" \
  "v_add_f32 v19, 1.0, v19\n" \
  "v_rcp_f32 v18, v18\n" \
  "v_rcp_f32 v19, v19\n" \
  "v_mul_f32 v106, v106, v18\n" \
  "v_mul_f32 v107, v107, v19\n" \
  "v_mul_f32 v106, v106, v122\n" \
  "v_mul_f32 v107, v107, v123\n" \
  "v_cvt_pk_bf16_f32 v3, v106, v107\n" \
  "buffer_store_dwordx4 v[0:3], v42, s[8:11], s33 offen sc1\n" \
  "v_mul_f32 v16, s36, v108\n" \
  "v_mul_f32 v17, s36, v109\n" \
  "v_exp_f32 v16, v16\n" \
  "v_exp_f32 v17, v17\n" \
  "v_add_f32 v16, 1.0, v16\n" \
  "v_add_f32 v17, 1.0, v17\n" \
  "v_rcp_f32 v16, v16\n" \
  "v_rcp_f32 v17, v17\n" \
  "v_mul_f32 v108, v108, v16\n" \
  "v_mul_f32 v109, v109, v17\n" \
  "v_mul_f32 v108, v108, v124\n" \
  "v_mul_f32 v109, v109, v125\n" \
  "v_cvt_pk_bf16_f32 v4, v108, v109\n" \
  "v_mul_f32 v18, s36, v110\n" \
  "v_mul_f32 v19, s36, v111\n" \
  "v_exp_f32 v18, v18\n" \
  "v_exp_f32 v19, v19\n" \
  "v_add_f32 v18, 1.0, v18\n" \
  "v_add_f32 v19, 1.0, v19\n" \
  "v_rcp_f32 v18, v18\n" \
  "v_rcp_f32 v19, v19\n" \
  "v_mul_f32 v110, v110, v18\n" \
  "v_mul_f32 v111, v111, v19\n" \
  "v_mul_f32 v110, v110, v126\n" \
  "v_mul_f32 v111, v111, v127\n" \
  "v_cvt_pk_bf16_f32 v5, v110, v111\n" \
  "v_mul_f32 v16, s36, v112\n" \
  "v_mul_f32 v17, s36, v113\n" \
  "v_exp_f32 v16, v16\n" \
  "v_exp_f32 v17, v17\n" \
  "v_add_f32 v16, 1.0, v16\n" \
  "v_add_f32 v17, 1.0, v17\n" \
  "v_rcp_f32 v16, v16\n" \
  "v_rcp_f32 v17, v17\n" \
  "v_mul_f32 v112, v112, v16\n" \
  "v_mul_f32 v113, v113, v17\n" \
  "v_mul_f32 v112, v112, v128\n" \
  "v_mul_f32 v113, v113, v129\n" \
  "v_cvt_pk_bf16_f32 v6, v112, v113\n" \
  "v_mul_f32 v18, s36, v114\n" \
  "v_mul_f32 v19, s36, v115\n" \
  "v_exp_f32 v18, v18\n" \
  "v_exp_f32 v19, v19\n" \
  "v_add_f32 v18, 1.0, v18\n" \
  "v_add_f32 v19, 1.0, v19\n" \
  "v_rcp_f32 v18, v18\n" \
  "v_rcp_f32 v19, v19\n" \
  "v_mul_f32 v114, v114, v18\n" \
  "v_mul_f32 v115, v115, v19\n" \
  "v_mul_f32 v114, v114, v130\n" \
  "v_mul_f32 v115, v115, v131\n" \
  "v_cvt_pk_bf16_f32 v7, v114, v115\n" \
  "buffer_store_dwordx4 v[4:7], v42, s[8:11], s33 offen offset:1024 sc1\n" \
  "s_waitcnt vmcnt(0)\n" \
  "s_waitcnt lgkmcnt(0)\n" \
  "s_mov_b32 exec_hi, 0\n" \
  "buffer_store_dword v44, v132, s[8:11], s35 offen sc1\n" \
  "s_mov_b32 exec_hi, -1\n" \
  "s_waitcnt vmcnt(0)\n"

#define ASM_G2 \
  "s_add_u32 m0, s24, 0\n" \
  "s_nop 0\n" \
  "buffer_load_dwordx4 v146, s[12:15], s25 offen lds\n" \
  "buffer_load_dwordx4 v147, s[12:15], s25 offen offset:1024 lds\n" \
  "s_add_u32 s25, s25, s26\n" \
  "s_cmp_eq_u32 s25, s36\n" \
  "s_cselect_b32 s25, 0, s25\n" \
  "s_add_u32 m0, s24, 2048\n" \
  "s_nop 0\n" \
  "buffer_load_dwordx4 v146, s[12:15], s25 offen lds\n" \
  "buffer_load_dwordx4 v147, s[12:15], s25 offen offset:1024 lds\n" \
  "s_add_u32 s25, s25, s26\n" \
  "s_cmp_eq_u32 s25, s36\n" \
  "s_cselect_b32 s25, 0, s25\n" \
  "s_add_u32 m0, s24, 4096\n" \
  "s_nop 0\n" \
  "buffer_load_dwordx4 v146, s[12:15], s25 offen lds\n" \
  "buffer_load_dwordx4 v147, s[12:15], s25 offen offset:1024 lds\n" \
  "s_add_u32 s25, s25, s26\n" \
  "s_cmp_eq_u32 s25, s36\n" \
  "s_cselect_b32 s25, 0, s25\n" \
  "s_add_u32 m0, s24, 6144\n" \
  "s_nop 0\n" \
  "buffer_load_dwordx4 v146, s[12:15], s25 offen lds\n" \
  "buffer_load_dwordx4 v147, s[12:15], s25 offen offset:1024 lds\n" \
  "s_add_u32 s25, s25, s26\n" \
  "s_cmp_eq_u32 s25, s36\n" \
  "s_cselect_b32 s25, 0, s25\n" \
  "s_add_u32 m0, s24, 8192\n" \
  "s_nop 0\n" \
  "buffer_load_dwordx4 v146, s[12:15], s25 offen lds\n" \
  "buffer_load_dwordx4 v147, s[12:15], s25 offen offset:1024 lds\n" \
  "s_add_u32 s25, s25, s26\n" \
  "s_cmp_eq_u32 s25, s36\n" \
  "s_cselect_b32 s25, 0, s25\n" \
  "s_add_u32 m0, s24, 10240\n" \
  "s_nop 0\n" \
  "buffer_load_dwordx4 v146, s[12:15], s25 offen lds\n" \
  "buffer_load_dwordx4 v147, s[12:15], s25 offen offset:1024 lds\n" \
  "s_add_u32 s25, s25, s26\n" \
  "s_cmp_eq_u32 s25, s36\n" \
  "s_cselect_b32 s25, 0, s25\n" \
  "s_add_u32 m0, s24, 12288\n" \
  "s_nop 0\n" \
  "buffer_load_dwordx4 v146, s[12:15], s25 offen lds\n" \
  "buffer_load_dwordx4 v147, s[12:15], s25 offen offset:1024 lds\n" \
  "s_add_u32 s25, s25, s26\n" \
  "s_cmp_eq_u32 s25, s36\n" \
  "s_cselect_b32 s25, 0, s25\n" \
  "s_add_u32 m0, s24, 14336\n" \
  "s_nop 0\n" \
  "buffer_load_dwordx4 v146, s[12:15], s25 offen lds\n" \
  "buffer_load_dwordx4 v147, s[12:15], s25 offen offset:1024 lds\n" \
  "s_add_u32 s25, s25, s26\n" \
  "s_cmp_eq_u32 s25, s36\n" \
  "s_cselect_b32 s25, 0, s25\n" \
  "s_add_u32 m0, s24, 16384\n" \
  "s_nop 0\n" \
  "buffer_load_dwordx4 v146, s[12:15], s25 offen lds\n" \
  "buffer_load_dwordx4 v147, s[12:15], s25 offen offset:1024 lds\n" \
  "s_add_u32 s25, s25, s26\n" \
  "s_cmp_eq_u32 s25, s36\n" \
  "s_cselect_b32 s25, 0, s25\n" \
  "s_add_u32 m0, s24, 18432\n" \
  "s_nop 0\n" \
  "buffer_load_dwordx4 v146, s[12:15], s25 offen lds\n" \
  "buffer_load_dwordx4 v147, s[12:15], s25 offen offset:1024 lds\n" \
  "s_add_u32 s25, s25, s26\n" \
  "s_cmp_eq_u32 s25, s36\n" \
  "s_cselect_b32 s25, 0, s25\n" \
  "s_add_u32 m0, s24, 20480\n" \
  "s_nop 0\n" \
  "buffer_load_dwordx4 v146, s[12:15], s25 offen lds\n" \
  "buffer_load_dwordx4 v147, s[12:15], s25 offen offset:1024 lds\n" \
  "s_add_u32 s25, s25, s26\n" \
  "s_cmp_eq_u32 s25, s36\n" \
  "s_cselect_b32 s25, 0, s25\n" \
  "s_add_u32 m0, s24, 22528\n" \
  "s_nop 0\n" \
  "buffer_load_dwordx4 v146, s[12:15], s25 offen lds\n" \
  "buffer_load_dwordx4 v147, s[12:15], s25 offen offset:1024 lds\n" \
  "s_add_u32 s25, s25, s26\n" \
  "s_cmp_eq_u32 s25, s36\n" \
  "s_cselect_b32 s25, 0, s25\n" \
  "s_add_u32 m0, s24, 24576\n" \
  "s_nop 0\n" \
  "buffer_load_dwordx4 v146, s[12:15], s25 offen lds\n" \
  "buffer_load_dwordx4 v147, s[12:15], s25 offen offset:1024 lds\n" \
  "s_add_u32 s25, s25, s26\n" \
  "s_cmp_eq_u32 s25, s36\n" \
  "s_cselect_b32 s25, 0, s25\n" \
  "s_add_u32 m0, s24, 26624\n" \
  "s_nop 0\n" \
  "buffer_load_dwordx4 v146, s[12:15], s25 offen lds\n" \
  "buffer_load_dwordx4 v147, s[12:15], s25 offen offset:1024 lds\n" \
  "s_add_u32 s25, s25, s26\n" \
  "s_cmp_eq_u32 s25, s36\n" \
  "s_cselect_b32 s25, 0, s25\n" \
  "s_add_u32 m0, s24, 28672\n" \
  "s_nop 0\n" \
  "buffer_load_dwordx4 v146, s[12:15], s25 offen lds\n" \
  "buffer_load_dwordx4 v147, s[12:15], s25 offen offset:1024 lds\n" \
  "s_add_u32 s25, s25, s26\n" \
  "s_cmp_eq_u32 s25, s36\n" \
  "s_cselect_b32 s25, 0, s25\n" \
  "s_waitcnt vmcnt(28)\n" \
  "ds_read_b64_tr_b16 a[0:1], v148\n" \
  "ds_read_b64_tr_b16 a[2:3], v148 offset:1024\n" \
  "ds_read_b64_tr_b16 a[4:5], v148 offset:64\n" \
  "ds_read_b64_tr_b16 a[6:7], v148 offset:1088\n" \
  "s_waitcnt vmcnt(26)\n" \
  "ds_read_b64_tr_b16 a[8:9], v148 offset:2048\n" \
  "ds_read_b64_tr_b16 a[10:11], v148 offset:3072\n" \
  "ds_read_b64_tr_b16 a[12:13], v148 offset:2112\n" \
  "ds_read_b64_tr_b16 a[14:15], v148 offset:3136\n" \
  "s_waitcnt lgkmcnt(4)\n" \
  "s_add_u32 m0, s24, 0\n" \
  "s_nop 0\n" \
  "buffer_load_dwordx4 v146, s[12:15], s25 offen lds\n" \
  "buffer_load_dwordx4 v147, s[12:15], s25 offen offset:1024 lds\n" \
  "s_add_u32 s25, s25, s26\n" \
  "s_cmp_eq_u32 s25, s36\n" \
  "s_cselect_b32 s25, 0, s25\n" \
  "s_waitcnt vmcnt(26)\n" \
  "ds_read_b64_tr_b16 a[16:17], v148 offset:4096\n" \
  "ds_read_b64_tr_b16 a[18:19], v148 offset:5120\n" \
  "ds_read_b64_tr_b16 a[20:21], v148 offset:4160\n" \
  "ds_read_b64_tr_b16 a[22:23], v148 offset:5184\n" \
  "s_waitcnt lgkmcnt(4)\n" \
  "s_add_u32 m0, s24, 2048\n" \
  "s_nop 0\n" \
  "buffer_load_dwordx4 v146, s[12:15], s25 offen lds\n" \
  "buffer_load_dwordx4 v147, s[12:15], s25 offen offset:1024 lds\n" \
  "s_add_u32 s25, s25, s26\n" \
  "s_cmp_eq_u32 s25, s36\n" \
  "s_cselect_b32 s25, 0, s25\n" \
  "s_waitcnt vmcnt(26)\n" \
  "ds_read_b64_tr_b16 a[24:25], v148 offset:6144\n" \
  "ds_read_b64_tr_b16 a[26:27], v148 offset:7168\n" \
  "ds_read_b64_tr_b16 a[28:29], v148 offset:6208\n" \
  "ds_read_b64_tr_b16 a[30:31], v148 offset:7232\n" \
  "s_waitcnt lgkmcnt(4)\n" \
  "s_add_u32 m0, s24, 4096\n" \
  "s_nop 0\n" \
  "buffer_load_dwordx4 v146, s[12:15], s25 offen lds\n" \
  "buffer_load_dwordx4 v147, s[12:15], s25 offen offset:1024 lds\n" \
  "s_add_u32 s25, s25, s26\n" \
  "s_cmp_eq_u32 s25, s36\n" \
  "s_cselect_b32 s25, 0, s25\n" \
  "s_waitcnt vmcnt(26)\n" \
  "ds_read_b64_tr_b16 a[32:33], v148 offset:8192\n" \
  "ds_read_b64_tr_b16 a[34:35], v148 offset:9216\n" \
  "ds_read_b64_tr_b16 a[36:37], v148 offset:8256\n" \
  "ds_read_b64_tr_b16 a[38:39], v148 offset:9280\n" \
  "s_waitcnt lgkmcnt(4)\n" \
  "s_add_u32 m0, s24, 6144\n" \
  "s_nop 0\n" \
  "buffer_load_dwordx4 v146, s[12:15], s25 offen lds\n" \
  "buffer_load_dwordx4 v147, s[12:15], s25 offen offset:1024 lds\n" \
  "s_add_u32 s25, s25, s26\n" \
  "s_cmp_eq_u32 s25, s36\n" \
  "s_cselect_b32 s25, 0, s25\n" \
  "s_waitcnt vmcnt(26)\n" \
  "ds_read_b64_tr_b16 a[40:41], v148 offset:10240\n" \
  "ds_read_b64_tr_b16 a[42:43], v148 offset:11264\n" \
  "ds_read_b64_tr_b16 a[44:45], v148 offset:10304\n" \
  "ds_read_b64_tr_b16 a[46:47], v148 offset:11328\n" \
  "s_waitcnt lgkmcnt(4)\n" \
  "s_add_u32 m0, s24, 8192\n" \
  "s_nop 0\n" \
  "buffer_load_dwordx4 v146, s[12:15], s25 offen lds\n" \
  "buffer_load_dwordx4 v147, s[12:15], s25 offen offset:1024 lds\n" \
  "s_add_u32 s25, s25, s26\n" \
  "s_cmp_eq_u32 s25, s36\n" \
  "s_cselect_b32 s25, 0, s25\n" \
  "s_waitcnt vmcnt(26)\n" \
  "ds_read_b64_tr_b16 a[48:49], v148 offset:12288\n" \
  "ds_read_b64_tr_b16 a[50:51], v148 offset:13312\n" \
  "ds_read_b64_tr_b16 a[52:53], v148 offset:12352\n" \
  "ds_read_b64_tr_b16 a[54:55], v148 offset:13376\n" \
  "s_waitcnt lgkmcnt(4)\n" \
  "s_add_u32 m0, s24, 10240\n" \
  "s_nop 0\n" \
  "buffer_load_dwordx4 v146, s[12:15], s25 offen lds\n" \
  "buffer_load_dwordx4 v147, s[12:15], s25 offen offset:1024 lds\n" \
  "s_add_u32 s25, s25, s26\n" \
  "s_cmp_eq_u32 s25, s36\n" \
  "s_cselect_b32 s25, 0, s25\n" \
  "s_waitcnt vmcnt(26)\n" \
  "ds_read_b64_tr_b16 a[56:57], v148 offset:14336\n" \
  "ds_read_b64_tr_b16 a[58:59], v148 offset:15360\n" \
  "ds_read_b64_tr_b16 a[60:61], v148 offset:14400\n" \
  "ds_read_b64_tr_b16 a[62:63], v148 offset:15424\n" \
  "s_waitcnt lgkmcnt(4)\n" \
  "s_add_u32 m0, s24, 12288\n" \
  "s_nop 0\n" \
  "buffer_load_dwordx4 v146, s[12:15], s25 offen lds\n" \
  "buffer_load_dwordx4 v147, s[12:15], s25 offen offset:1024 lds\n" \
  "s_add_u32 s25, s25, s26\n" \
  "s_cmp_eq_u32 s25, s36\n" \
  "s_cselect_b32 s25, 0, s25\n" \
  "s_waitcnt vmcnt(26)\n" \
  "ds_read_b64_tr_b16 a[64:65], v148 offset:16384\n" \
  "ds_read_b64_tr_b16 a[66:67], v148 offset:17408\n" \
  "ds_read_b64_tr_b16 a[68:69], v148 offset:16448\n" \
  "ds_read_b64_tr_b16 a[70:71], v148 offset:17472\n" \
  "s_waitcnt lgkmcnt(4)\n" \
  "s_add_u32 m0, s24, 14336\n" \
  "s_nop 0\n" \
  "buffer_load_dwordx4 v146, s[12:15], s25 offen lds\n" \
  "buffer_load_dwordx4 v147, s[12:15], s25 offen offset:1024 lds\n" \
  "s_add_u32 s25, s25, s26\n" \
  "s_cmp_eq_u32 s25, s36\n" \
  "s_cselect_b32 s25, 0, s25\n" \
  "s_waitcnt vmcnt(26)\n" \
  "ds_read_b64_tr_b16 a[72:73], v148 offset:18432\n" \
  "ds_read_b64_tr_b16 a[74:75], v148 offset:19456\n" \
  "ds_read_b64_tr_b16 a[76:77], v148 offset:18496\n" \
  "ds_read_b64_tr_b16 a[78:79], v148 offset:19520\n" \
  "s_waitcnt lgkmcnt(4)\n" \
  "s_add_u32 m0, s24, 16384\n" \
  "s_nop 0\n" \
  "buffer_load_dwordx4 v146, s[12:15], s25 offen lds\n" \
  "buffer_load_dwordx4 v147, s[12:15], s25 offen offset:1024 lds\n" \
  "s_add_u32 s25, s25, s26\n" \
  "s_cmp_eq_u32 s25, s36\n" \
  "s_cselect_b32 s25, 0, s25\n" \
  "s_waitcnt vmcnt(26)\n" \
  "ds_read_b64_tr_b16 a[80:81], v148 offset:20480\n" \
  "ds_read_b64_tr_b16 a[82:83], v148 offset:21504\n" \
  "ds_read_b64_tr_b16 a[84:85], v148 offset:20544\n" \
  "ds_read_b64_tr_b16 a[86:87], v148 offset:21568\n" \
  "s_waitcnt lgkmcnt(4)\n" \
  "s_add_u32 m0, s24, 18432\n" \
  "s_nop 0\n" \
  "buffer_load_dwordx4 v146, s[12:15], s25 offen lds\n" \
  "buffer_load_dwordx4 v147, s[12:15], s25 offen offset:1024 lds\n" \
  "s_add_u32 s25, s25, s26\n" \
  "s_cmp_eq_u32 s25, s36\n" \
  "s_cselect_b32 s25, 0, s25\n" \
  "s_waitcnt vmcnt(26)\n" \
  "ds_read_b64_tr_b16 a[88:89], v148 offset:22528\n" \
  "ds_read_b64_tr_b16 a[90:91], v148 offset:23552\n" \
  "ds_read_b64_tr_b16 a[92:93], v148 offset:22592\n" \
  "ds_read_b64_tr_b16 a[94:95], v148 offset:23616\n" \
  "s_waitcnt lgkmcnt(4)\n" \
  "s_add_u32 m0, s24, 20480\n" \
  "s_nop 0\n" \
  "buffer_load_dwordx4 v146, s[12:15], s25 offen lds\n" \
  "buffer_load_dwordx4 v147, s[12:15], s25 offen offset:1024 lds\n" \
  "s_add_u32 s25, s25, s26\n" \
  "s_cmp_eq_u32 s25, s36\n" \
  "s_cselect_b32 s25, 0, s25\n" \
  "s_waitcnt vmcnt(26)\n" \
  "ds_read_b64_tr_b16 a[96:97], v148 offset:24576\n" \
  "ds_read_b64_tr_b16 a[98:99], v148 offset:25600\n" \
  "ds_read_b64_tr_b16 a[100:101], v148 offset:24640\n" \
  "ds_read_b64_tr_b16 a[102:103], v148 offset:25664\n" \
  "s_waitcnt lgkmcnt(4)\n" \
  "s_add_u32 m0, s24, 22528\n" \
  "s_nop 0\n" \
  "buffer_load_dwordx4 v146, s[12:15], s25 offen lds\n" \
  "buffer_load_dwordx4 v147, s[12:15], s25 offen offset:1024 lds\n" \
  "s_add_u32 s25, s25, s26\n" \
  "s_cmp_eq_u32 s25, s36\n" \
  "s_cselect_b32 s25, 0, s25\n" \
  "s_waitcnt vmcnt(26)\n" \
  "ds_read_b64_tr_b16 a[104:105], v148 offset:26624\n" \
  "ds_read_b64_tr_b16 a[106:107], v148 offset:27648\n" \
  "ds_read_b64_tr_b16 a[108:109], v148 offset:26688\n" \
  "ds_read_b64_tr_b16 a[110:111], v148 offset:27712\n" \
  "s_waitcnt vmcnt(24)\n" \
  "ds_read_b64_tr_b16 a[112:113], v148 offset:28672\n" \
  "ds_read_b64_tr_b16 a[114:115], v148 offset:29696\n" \
  "ds_read_b64_tr_b16 a[116:117], v148 offset:28736\n" \
  "ds_read_b64_tr_b16 a[118:119], v148 offset:29760\n" \
  "s_waitcnt vmcnt(22)\n" \
  "ds_read_b64_tr_b16 a[120:121], v148\n" \
  "ds_read_b64_tr_b16 a[122:123], v148 offset:1024\n" \
  "ds_read_b64_tr_b16 a[124:125], v148 offset:64\n" \
  "ds_read_b64_tr_b16 a[126:127], v148 offset:1088\n" \
  "s_waitcnt vmcnt(20)\n" \
  "ds_read_b64_tr_b16 a[128:129], v148 offset:2048\n" \
  "ds_read_b64_tr_b16 a[130:131], v148 offset:3072\n" \
  "ds_read_b64_tr_b16 a[132:133], v148 offset:2112\n" \
  "ds_read_b64_tr_b16 a[134:135], v148 offset:3136\n" \
  "s_waitcnt vmcnt(18)\n" \
  "ds_read_b64_tr_b16 a[136:137], v148 offset:4096\n" \
  "ds_read_b64_tr_b16 a[138:139], v148 offset:5120\n" \
  "ds_read_b64_tr_b16 a[140:141], v148 offset:4160\n" \
  "ds_read_b64_tr_b16 a[142:143], v148 offset:5184\n" \
  "s_waitcnt vmcnt(16)\n" \
  "ds_read_b64_tr_b16 a[144:145], v148 offset:6144\n" \
  "ds_read_b64_tr_b16 a[146:147], v148 offset:7168\n" \
  "ds_read_b64_tr_b16 a[148:149], v148 offset:6208\n" \
  "ds_read_b64_tr_b16 a[150:151], v148 offset:7232\n" \
  "s_waitcnt vmcnt(14)\n" \
  "ds_read_b64_tr_b16 a[152:153], v148 offset:8192\n" \
  "ds_read_b64_tr_b16 a[154:155], v148 offset:9216\n" \
  "ds_read_b64_tr_b16 a[156:157], v148 offset:8256\n" \
  "ds_read_b64_tr_b16 a[158:159], v148 offset:9280\n" \
  "s_waitcnt vmcnt(12)\n" \
  "ds_read_b64_tr_b16 a[160:161], v148 offset:10240\n" \
  "ds_read_b64_tr_b16 a[162:163], v148 offset:11264\n" \
  "ds_read_b64_tr_b16 a[164:165], v148 offset:10304\n" \
  "ds_read_b64_tr_b16 a[166:167], v148 offset:11328\n" \
  "s_waitcnt vmcnt(10)\n" \
  "ds_read_b64_tr_b16 a[168:169], v148 offset:12288\n" \
  "ds_read_b64_tr_b16 a[170:171], v148 offset:13312\n" \
  "ds_read_b64_tr_b16 a[172:173], v148 offset:12352\n" \
  "ds_read_b64_tr_b16 a[174:175], v148 offset:13376\n" \
  "s_waitcnt vmcnt(8)\n" \
  "ds_read_b64_tr_b16 a[176:177], v148 offset:14336\n" \
  "ds_read_b64_tr_b16 a[178:179], v148 offset:15360\n" \
  "ds_read_b64_tr_b16 a[180:181], v148 offset:14400\n" \
  "ds_read_b64_tr_b16 a[182:183], v148 offset:15424\n" \
  "s_waitcnt vmcnt(6)\n" \
  "ds_read_b64_tr_b16 a[184:185], v148 offset:16384\n" \
  "ds_read_b64_tr_b16 a[186:187], v148 offset:17408\n" \
  "ds_read_b64_tr_b16 a[188:189], v148 offset:16448\n" \
  "ds_read_b64_tr_b16 a[190:191], v148 offset:17472\n" \
  "s_waitcnt vmcnt(4)\n" \
  "ds_read_b64_tr_b16 a[192:193], v148 offset:18432\n" \
  "ds_read_b64_tr_b16 a[194:195], v148 offset:19456\n" \
  "ds_read_b64_tr_b16 a[196:197], v148 offset:18496\n" \
  "ds_read_b64_tr_b16 a[198:199], v148 offset:19520\n" \
  "s_waitcnt vmcnt(2)\n" \
  "ds_read_b64_tr_b16 a[200:201], v148 offset:20480\n" \
  "ds_read_b64_tr_b16 a[202:203], v148 offset:21504\n" \
  "ds_read_b64_tr_b16 a[204:205], v148 offset:20544\n" \
  "ds_read_b64_tr_b16 a[206:207], v148 offset:21568\n" \
  "s_waitcnt vmcnt(0)\n" \
  "ds_read_b64_tr_b16 a[208:209], v148 offset:22528\n" \
  "ds_read_b64_tr_b16 a[210:211], v148 offset:23552\n" \
  "ds_read_b64_tr_b16 a[212:213], v148 offset:22592\n" \
  "ds_read_b64_tr_b16 a[214:215], v148 offset:23616\n" \
  "s_waitcnt lgkmcnt(0)\n" \
  "s_mov_b32 s34, 647296\n" \
  "BG_%=:\n" \
  "buffer_load_dword v0, off, s[8:11], s34 sc1\n" \
  "s_waitcnt vmcnt(0)\n" \
  "v_cmp_ne_u32 vcc, s38, v0\n" \
  "s_cbranch_vccz BGG_%=\n" \
  "s_sleep 4\n" \
  "s_sub_u32 s33, s33, 1\n" \
  "s_cbranch_scc0 BG_%=\n" \
  "s_mov_b32 s33, 0\n" \
  "BGG_%=:\n" \
  "s_barrier\n" \
  "v_mov_b32 v153, -2\n" \
  "v_mbcnt_lo_u32_b32 v154, -1, 0\n" \
  "v_lshlrev_b32 v154, 2, v154\n" \
  "v_mov_b32 v157, 0x80000000\n" \
  "v_mov_b32 v158, -1\n" \
  "BA_%=:\n" \
  "buffer_load_dword v150, v141, s[8:11], s32 offen sc1\n" \
  "buffer_load_dword v152, v142, s[8:11], s29 offen sc1\n" \
  "s_waitcnt vmcnt(1)\n" \
  "v_max_i32 v158, v158, v150\n" \
  "v_cmp_gt_i32 vcc, s27, v158\n" \
  "s_waitcnt vmcnt(0)\n" \
  "s_nop 3\n" \
  "s_cmp_lg_u64 vcc, 0\n" \
  "s_cbranch_scc0 BAG_%=\n" \
  "s_sleep 4\n" \
  "s_sub_u32 s33, s33, 1\n" \
  "s_cbranch_scc0 BA_%=\n" \
  "s_mov_b32 s33, 0\n" \
  "BAG_%=:\n" \
  "v_mov_b32 v149, v152\n" \
  "s_add_u32 s29, s29, 128\n" \
  "s_add_u32 s28, s28, 110592\n" \
  "s_add_u32 s43, s27, 1\n" \
  "v_cmp_gt_i32 vcc, s43, v158\n" \
  "s_nop 3\n" \
  "s_cmp_eq_u64 vcc, 0\n" \
  "s_cselect_b32 s43, 0x80000000, 0\n" \
  "v_or_b32 v159, s43, v141\n" \
  "buffer_load_dword v150, v159, s[8:11], s32 offen sc1\n" \
  "buffer_load_dword v152, v142, s[8:11], s29 offen sc1\n" \
  "s_add_u32 s34, s28, s37\n" \
  "s_add_u32 s42, s28, 27648\n" \
  "buffer_load_dwordx4 v[0:3], v140, s[8:11], s34 offen sc1\n" \
  "s_add_u32 s34, s34, 1024\n" \
  "s_cmp_eq_u32 s34, s42\n" \
  "s_cselect_b32 s34, s28, s34\n" \
  "buffer_load_dwordx4 v[4:7], v140, s[8:11], s34 offen sc1\n" \
  "s_add_u32 s34, s34, 1024\n" \
  "s_cmp_eq_u32 s34, s42\n" \
  "s_cselect_b32 s34, s28, s34\n" \
  "buffer_load_dwordx4 v[8:11], v140, s[8:11], s34 offen sc1\n" \
  "s_add_u32 s34, s34, 1024\n" \
  "s_cmp_eq_u32 s34, s42\n" \
  "s_cselect_b32 s34, s28, s34\n" \
  "buffer_load_dwordx4 v[12:15], v140, s[8:11], s34 offen sc1\n" \
  "s_add_u32 s34, s34, 1024\n" \
  "s_cmp_eq_u32 s34, s42\n" \
  "s_cselect_b32 s34, s28, s34\n" \
  "buffer_load_dwordx4 v[16:19], v140, s[8:11], s34 offen sc1\n" \
  "s_add_u32 s34, s34, 1024\n" \
  "s_cmp_eq_u32 s34, s42\n" \
  "s_cselect_b32 s34, s28, s34\n" \
  "buffer_load_dwordx4 v[20:23], v140, s[8:11], s34 offen sc1\n" \
  "s_add_u32 s34, s34, 1024\n" \
  "s_cmp_eq_u32 s34, s42\n" \
  "s_cselect_b32 s34, s28, s34\n" \
  "buffer_load_dwordx4 v[24:27], v140, s[8:11], s34 offen sc1\n" \
  "s_add_u32 s34, s34, 1024\n" \
  "s_cmp_eq_u32 s34, s42\n" \
  "s_cselect_b32 s34, s28, s34\n" \
  "buffer_load_dwordx4 v[28:31], v140, s[8:11], s34 offen sc1\n" \
  "s_add_u32 s34, s34, 1024\n" \
  "s_cmp_eq_u32 s34, s42\n" \
  "s_cselect_b32 s34, s28, s34\n" \
  "buffer_load_dwordx4 v[32:35], v140, s[8:11], s34 offen sc1\n" \
  "s_add_u32 s34, s34, 1024\n" \
  "s_cmp_eq_u32 s34, s42\n" \
  "s_cselect_b32 s34, s28, s34\n" \
  "buffer_load_dwordx4 v[36:39], v140, s[8:11], s34 offen sc1\n" \
  "s_add_u32 s34, s34, 1024\n" \
  "s_cmp_eq_u32 s34, s42\n" \
  "s_cselect_b32 s34, s28, s34\n" \
  "buffer_load_dwordx4 v[40:43], v140, s[8:11], s34 offen sc1\n" \
  "s_add_u32 s34, s34, 1024\n" \
  "s_cmp_eq_u32 s34, s42\n" \
  "s_cselect_b32 s34, s28, s34\n" \
  "buffer_load_dwordx4 v[44:47], v140, s[8:11], s34 offen sc1\n" \
  "s_add_u32 s34, s34, 1024\n" \
  "s_cmp_eq_u32 s34, s42\n" \
  "s_cselect_b32 s34, s28, s34\n" \
  "buffer_load_dwordx4 v[48:51], v140, s[8:11], s34 offen sc1\n" \
  "s_add_u32 s34, s34, 1024\n" \
  "s_cmp_eq_u32 s34, s42\n" \
  "s_cselect_b32 s34, s28, s34\n" \
  "buffer_load_dwordx4 v[52:55], v140, s[8:11], s34 offen sc1\n" \
  "s_add_u32 s34, s34, 1024\n" \
  "s_cmp_eq_u32 s34, s42\n" \
  "s_cselect_b32 s34, s28, s34\n" \
  "buffer_load_dwordx4 v[56:59], v140, s[8:11], s34 offen sc1\n" \
  "s_add_u32 s34, s34, 1024\n" \
  "s_cmp_eq_u32 s34, s42\n" \
  "s_cselect_b32 s34, s28, s34\n" \
  "buffer_load_dwordx4 v[60:63], v140, s[8:11], s34 offen sc1\n" \
  "s_add_u32 s34, s34, 1024\n" \
  "s_cmp_eq_u32 s34, s42\n" \
  "s_cselect_b32 s34, s28, s34\n" \
  "buffer_load_dwordx4 v[64:67], v140, s[8:11], s34 offen sc1\n" \
  "s_add_u32 s34, s34, 1024\n" \
  "s_cmp_eq_u32 s34, s42\n" \
  "s_cselect_b32 s34, s28, s34\n" \
  "buffer_load_dwordx4 v[68:71], v140, s[8:11], s34 offen sc1\n" \
  "s_add_u32 s34, s34, 1024\n" \
  "s_cmp_eq_u32 s34, s42\n" \
  "s_cselect_b32 s34, s28, s34\n" \
  "buffer_load_dwordx4 v[72:75], v140, s[8:11], s34 offen sc1\n" \
  "s_add_u32 s34, s34, 1024\n" \
  "s_cmp_eq_u32 s34, s42\n" \
  "s_cselect_b32 s34, s28, s34\n" \
  "buffer_load_dwordx4 v[76:79], v140, s[8:11], s34 offen sc1\n" \
  "s_add_u32 s34, s34, 1024\n" \
  "s_cmp_eq_u32 s34, s42\n" \
  "s_cselect_b32 s34, s28, s34\n" \
  "buffer_load_dwordx4 v[80:83], v140, s[8:11], s34 offen sc1\n" \
  "s_add_u32 s34, s34, 1024\n" \
  "s_cmp_eq_u32 s34, s42\n" \
  "s_cselect_b32 s34, s28, s34\n" \
  "buffer_load_dwordx4 v[84:87], v140, s[8:11], s34 offen sc1\n" \
  "s_add_u32 s34, s34, 1024\n" \
  "s_cmp_eq_u32 s34, s42\n" \
  "s_cselect_b32 s34, s28, s34\n" \
  "buffer_load_dwordx4 v[88:91], v140, s[8:11], s34 offen sc1\n" \
  "s_add_u32 s34, s34, 1024\n" \
  "s_cmp_eq_u32 s34, s42\n" \
  "s_cselect_b32 s34, s28, s34\n" \
  "buffer_load_dwordx4 v[92:95], v140, s[8:11], s34 offen sc1\n" \
  "s_add_u32 s34, s34, 1024\n" \
  "s_cmp_eq_u32 s34, s42\n" \
  "s_cselect_b32 s34, s28, s34\n" \
  "buffer_load_dwordx4 v[96:99], v140, s[8:11], s34 offen sc1\n" \
  "s_add_u32 s34, s34, 1024\n" \
  "s_cmp_eq_u32 s34, s42\n" \
  "s_cselect_b32 s34, s28, s34\n" \
  "buffer_load_dwordx4 v[100:103], v140, s[8:11], s34 offen sc1\n" \
  "s_add_u32 s34, s34, 1024\n" \
  "s_cmp_eq_u32 s34, s42\n" \
  "s_cselect_b32 s34, s28, s34\n" \
  "buffer_load_dwordx4 v[104:107], v140, s[8:11], s34 offen sc1\n" \
  "BL_%=:\n" \
  "v_mov_b32 v156, v149\n" \
  "s_waitcnt vmcnt(28)\n" \
  "v_max_i32 v158, v158, v150\n" \
  "v_cmp_ge_i32 vcc, s27, v158\n" \
  "s_waitcnt vmcnt(27)\n" \
  "s_nop 3\n" \
  "s_cmp_lg_u64 vcc, 0\n" \
  "s_cselect_b32 s35, 0x80000000, 0\n" \
  "s_cmp_eq_u32 s27, 63\n" \
  "s_cselect_b32 s35, 0x80000000, s35\n" \
  "v_or_b32 v155, s35, v140\n" \
  "s_cmp_lg_u32 s35, 0\n" \
  "s_cbranch_scc1 BC_%=\n" \
  "v_mov_b32 v149, v152\n" \
  "s_add_u32 s29, s29, 128\n" \
  "s_add_u32 s28, s28, 110592\n" \
  "BC_%=:\n" \
  "s_nop 0\n" \
  "s_add_u32 s43, s27, 2\n" \
  "v_cmp_gt_i32 vcc, s43, v158\n" \
  "s_nop 3\n" \
  "s_cmp_eq_u64 vcc, 0\n" \
  "s_cselect_b32 s43, 0x80000000, 0\n" \
  "v_or_b32 v159, s43, v141\n" \
  "buffer_load_dword v150, v159, s[8:11], s32 offen sc1\n" \
  "buffer_load_dword v152, v142, s[8:11], s29 offen sc1\n" \
  "s_waitcnt vmcnt(28)\n" \
  "v_mfma_f32_32x32x16_bf16 v[108:123], a[0:3], v[0:3], 0\n" \
  "v_mfma_f32_32x32x16_bf16 v[124:139], a[4:7], v[0:3], 0\n" \
  "s_barrier\n" \
  "s_waitcnt vmcnt(27)\n" \
  "v_mfma_f32_32x32x16_bf16 v[108:123], a[8:11], v[4:7], v[108:123]\n" \
  "ds_read_b128 v[160:163], v144\n" \
  "ds_read_b128 v[164:167], v144 offset:1024\n" \
  "ds_read_b128 v[168:171], v144 offset:2048\n" \
  "v_mfma_f32_32x32x16_bf16 v[124:139], a[12:15], v[4:7], v[124:139]\n" \
  "ds_read_b128 v[172:175], v144 offset:3072\n" \
  "ds_read_b128 v[176:179], v144 offset:4096\n" \
  "ds_read_b128 v[180:183], v144 offset:5120\n" \
  "s_add_u32 s34, s28, s37\n" \
  "s_add_u32 s42, s28, 27648\n" \
  "buffer_load_dwordx4 v[0:3], v155, s[8:11], s34 offen sc1\n" \
  "s_waitcnt vmcnt(27)\n" \
  "v_mfma_f32_32x32x16_bf16 v[108:123], a[16:19], v[8:11], v[108:123]\n" \
  "ds_read_b128 v[184:187], v144 offset:6144\n" \
  "ds_read_b128 v[188:191], v144 offset:7168\n" \
  "v_mfma_f32_32x32x16_bf16 v[124:139], a[20:23], v[8:11], v[124:139]\n" \
  "s_add_u32 s34, s34, 1024\n" \
  "s_cmp_eq_u32 s34, s42\n" \
  "s_cselect_b32 s34, s28, s34\n" \
  "buffer_load_dwordx4 v[4:7], v155, s[8:11], s34 offen sc1\n" \
  "s_waitcnt vmcnt(27)\n" \
  "v_mfma_f32_32x32x16_bf16 v[108:123], a[24:27], v[12:15], v[108:123]\n" \
  "s_waitcnt lgkmcnt(5)\n" \
  "v_add_f32 v160, v160, v168\n" \
  "v_mfma_f32_32x32x16_bf16 v[124:139], a[28:31], v[12:15], v[124:139]\n" \
  "v_add_f32 v161, v161, v169\n" \
  "v_add_f32 v162, v162, v170\n" \
  "v_add_f32 v163, v163, v171\n" \
  "s_add_u32 s34, s34, 1024\n" \
  "s_cmp_eq_u32 s34, s42\n" \
  "s_cselect_b32 s34, s28, s34\n" \
  "buffer_load_dwordx4 v[8:11], v155, s[8:11], s34 offen sc1\n" \
  "s_waitcnt vmcnt(27)\n" \
  "v_mfma_f32_32x32x16_bf16 v[108:123], a[32:35], v[16:19], v[108:123]\n" \
  "s_waitcnt lgkmcnt(4)\n" \
  "v_add_f32 v164, v164, v172\n" \
  "v_add_f32 v165, v165, v173\n" \
  "v_add_f32 v166, v166, v174\n" \
  "v_mfma_f32_32x32x16_bf16 v[124:139], a[36:39], v[16:19], v[124:139]\n" \
  "v_add_f32 v167, v167, v175\n" \
  "s_waitcnt lgkmcnt(1)\n" \
  "v_add_f32 v176, v176, v184\n" \
  "v_add_f32 v177, v177, v185\n" \
  "s_add_u32 s34, s34, 1024\n" \
  "s_cmp_eq_u32 s34, s42\n" \
  "s_cselect_b32 s34, s28, s34\n" \
  "buffer_load_dwordx4 v[12:15], v155, s[8:11], s34 offen sc1\n" \
  "s_waitcnt vmcnt(27)\n" \
  "v_mfma_f32_32x32x16_bf16 v[108:123], a[40:43], v[20:23], v[108:123]\n" \
  "v_add_f32 v178, v178, v186\n" \
  "v_add_f32 v179, v179, v187\n" \
  "s_waitcnt lgkmcnt(0)\n" \
  "v_add_f32 v180, v180, v188\n" \
  "v_mfma_f32_32x32x16_bf16 v[124:139], a[44:47], v[20:23], v[124:139]\n" \
  "v_add_f32 v181, v181, v189\n" \
  "v_add_f32 v182, v182, v190\n" \
  "v_add_f32 v183, v183, v191\n" \
  "s_add_u32 s34, s34, 1024\n" \
  "s_cmp_eq_u32 s34, s42\n" \
  "s_cselect_b32 s34, s28, s34\n" \
  "buffer_load_dwordx4 v[16:19], v155, s[8:11], s34 offen sc1\n" \
  "s_waitcnt vmcnt(27)\n" \
  "v_mfma_f32_32x32x16_bf16 v[108:123], a[48:51], v[24:27], v[108:123]\n" \
  "v_xor_b32 v144, 0x8000, v144\n" \
  "v_add_f32 v160, v160, v176\n" \
  "v_add_f32 v161, v161, v177\n" \
  "v_mfma_f32_32x32x16_bf16 v[124:139], a[52:55], v[24:27], v[124:139]\n" \
  "v_add_f32 v162, v162, v178\n" \
  "v_add_f32 v163, v163, v179\n" \
  "v_add_f32 v164, v164, v180\n" \
  "s_add_u32 s34, s34, 1024\n" \
  "s_cmp_eq_u32 s34, s42\n" \
  "s_cselect_b32 s34, s28, s34\n" \
  "buffer_load_dwordx4 v[20:23], v155, s[8:11], s34 offen sc1\n" \
  "s_waitcnt vmcnt(27)\n" \
  "v_mfma_f32_32x32x16_bf16 v[108:123], a[56:59], v[28:31], v[108:123]\n" \
  "v_add_f32 v165, v165, v181\n" \
  "v_add_f32 v166, v166, v182\n" \
  "v_add_f32 v167, v167, v183\n" \
  "v_mfma_f32_32x32x16_bf16 v[124:139], a[60:63], v[28:31], v[124:139]\n" \
  "v_cvt_pk_bf16_f32 v168, v160, v161\n" \
  "v_cvt_pk_bf16_f32 v169, v162, v163\n" \
  "v_cvt_pk_bf16_f32 v170, v164, v165\n" \
  "s_add_u32 s34, s34, 1024\n" \
  "s_cmp_eq_u32 s34, s42\n" \
  "s_cselect_b32 s34, s28, s34\n" \
  "buffer_load_dwordx4 v[24:27], v155, s[8:11], s34 offen sc1\n" \
  "s_waitcnt vmcnt(27)\n" \
  "v_mfma_f32_32x32x16_bf16 v[108:123], a[64:67], v[32:35], v[108:123]\n" \
  "v_cvt_pk_bf16_f32 v171, v166, v167\n" \
  "v_lshlrev_b32 v160, 16, v168\n" \
  "v_and_b32 v161, 0xffff0000, v168\n" \
  "v_mfma_f32_32x32x16_bf16 v[124:139], a[68:71], v[32:35], v[124:139]\n" \
  "v_lshlrev_b32 v162, 16, v169\n" \
  "v_and_b32 v163, 0xffff0000, v169\n" \
  "v_lshlrev_b32 v164, 16, v170\n" \
  "s_add_u32 s34, s34, 1024\n" \
  "s_cmp_eq_u32 s34, s42\n" \
  "s_cselect_b32 s34, s28, s34\n" \
  "buffer_load_dwordx4 v[28:31], v155, s[8:11], s34 offen sc1\n" \
  "s_waitcnt vmcnt(27)\n" \
  "v_mfma_f32_32x32x16_bf16 v[108:123], a[72:75], v[36:39], v[108:123]\n" \
  "v_and_b32 v165, 0xffff0000, v170\n" \
  "v_lshlrev_b32 v166, 16, v171\n" \
  "v_and_b32 v167, 0xffff0000, v171\n" \
  "v_mfma_f32_32x32x16_bf16 v[124:139], a[76:79], v[36:39], v[124:139]\n" \
  "v_mul_f32 v160, v160, v151\n" \
  "v_mul_f32 v161, v161, v151\n" \
  "v_mul_f32 v162, v162, v151\n" \
  "s_add_u32 s34, s34, 1024\n" \
  "s_cmp_eq_u32 s34, s42\n" \
  "s_cselect_b32 s34, s28, s34\n" \
  "buffer_load_dwordx4 v[32:35], v155, s[8:11], s34 offen sc1\n" \
  "s_waitcnt vmcnt(27)\n" \
  "v_mfma_f32_32x32x16_bf16 v[108:123], a[80:83], v[40:43], v[108:123]\n" \
  "v_mul_f32 v163, v163, v151\n" \
  "v_mul_f32 v164, v164, v151\n" \
  "v_mul_f32 v165, v165, v151\n" \
  "v_mfma_f32_32x32x16_bf16 v[124:139], a[84:87], v[40:43], v[124:139]\n" \
  "v_mul_f32 v166, v166, v151\n" \
  "v_mul_f32 v167, v167, v151\n" \
  "v_cvt_pk_bf16_f32 v168, v160, v161\n" \
  "s_add_u32 s34, s34, 1024\n" \
  "s_cmp_eq_u32 s34, s42\n" \
  "s_cselect_b32 s34, s28, s34\n" \
  "buffer_load_dwordx4 v[36:39], v155, s[8:11], s34 offen sc1\n" \
  "s_waitcnt vmcnt(27)\n" \
  "v_mfma_f32_32x32x16_bf16 v[108:123], a[88:91], v[44:47], v[108:123]\n" \
  "v_cvt_pk_bf16_f32 v169, v162, v163\n" \
  "v_cvt_pk_bf16_f32 v170, v164, v165\n" \
  "v_cvt_pk_bf16_f32 v171, v166, v167\n" \
  "v_mfma_f32_32x32x16_bf16 v[124:139], a[92:95], v[44:47], v[124:139]\n" \
  "buffer_store_dwordx4 v[168:171], v157, s[8:11], s30 offen sc1\n" \
  "s_add_u32 s30, s30, 40960\n" \
  "v_mov_b32 v157, v145\n" \
  "s_add_u32 s34, s34, 1024\n" \
  "s_cmp_eq_u32 s34, s42\n" \
  "s_cselect_b32 s34, s28, s34\n" \
  "buffer_load_dwordx4 v[40:43], v155, s[8:11], s34 offen sc1\n" \
  "s_waitcnt vmcnt(28)\n" \
  "v_mfma_f32_32x32x16_bf16 v[108:123], a[96:99], v[48:51], v[108:123]\n" \
  "v_sub_f32 v151, 1.0, v156\n" \
  "v_cvt_pk_bf16_f32 v151, v151, v151\n" \
  "v_lshlrev_b32 v151, 16, v151\n" \
  "v_mfma_f32_32x32x16_bf16 v[124:139], a[100:103], v[48:51], v[124:139]\n" \
  "s_add_u32 s34, s34, 1024\n" \
  "s_cmp_eq_u32 s34, s42\n" \
  "s_cselect_b32 s34, s28, s34\n" \
  "buffer_load_dwordx4 v[44:47], v155, s[8:11], s34 offen sc1\n" \
  "s_waitcnt vmcnt(28)\n" \
  "v_mfma_f32_32x32x16_bf16 v[108:123], a[104:107], v[52:55], v[108:123]\n" \
  "v_mfma_f32_32x32x16_bf16 v[124:139], a[108:111], v[52:55], v[124:139]\n" \
  "s_add_u32 s34, s34, 1024\n" \
  "s_cmp_eq_u32 s34, s42\n" \
  "s_cselect_b32 s34, s28, s34\n" \
  "buffer_load_dwordx4 v[48:51], v155, s[8:11], s34 offen sc1\n" \
  "s_mov_b32 exec_hi, 0\n" \
  "buffer_store_dword v153, v154, s[8:11], s31 offen sc1\n" \
  "s_mov_b32 exec_hi, -1\n" \
  "v_add_u32 v153, 1, v153\n" \
  "s_waitcnt vmcnt(29)\n" \
  "v_mfma_f32_32x32x16_bf16 v[108:123], a[112:115], v[56:59], v[108:123]\n" \
  "v_mfma_f32_32x32x16_bf16 v[124:139], a[116:119], v[56:59], v[124:139]\n" \
  "s_add_u32 s34, s34, 1024\n" \
  "s_cmp_eq_u32 s34, s42\n" \
  "s_cselect_b32 s34, s28, s34\n" \
  "buffer_load_dwordx4 v[52:55], v155, s[8:11], s34 offen sc1\n" \
  "s_waitcnt vmcnt(29)\n" \
  "v_mfma_f32_32x32x16_bf16 v[108:123], a[120:123], v[60:63], v[108:123]\n" \
  "v_mfma_f32_32x32x16_bf16 v[124:139], a[124:127], v[60:63], v[124:139]\n" \
  "s_add_u32 s34, s34, 1024\n" \
  "s_cmp_eq_u32 s34, s42\n" \
  "s_cselect_b32 s34, s28, s34\n" \
  "buffer_load_dwordx4 v[56:59], v155, s[8:11], s34 offen sc1\n" \
  "s_waitcnt vmcnt(29)\n" \
  "v_mfma_f32_32x32x16_bf16 v[108:123], a[128:131], v[64:67], v[108:123]\n" \
  "v_mfma_f32_32x32x16_bf16 v[124:139], a[132:135], v[64:67], v[124:139]\n" \
  "s_add_u32 s34, s34, 1024\n" \
  "s_cmp_eq_u32 s34, s42\n" \
  "s_cselect_b32 s34, s28, s34\n" \
  "buffer_load_dwordx4 v[60:63], v155, s[8:11], s34 offen sc1\n" \
  "s_waitcnt vmcnt(29)\n" \
  "v_mfma_f32_32x32x16_bf16 v[108:123], a[136:139], v[68:71], v[108:123]\n" \
  "v_mfma_f32_32x32x16_bf16 v[124:139], a[140:143], v[68:71], v[124:139]\n" \
  "s_add_u32 s34, s34, 1024\n" \
  "s_cmp_eq_u32 s34, s42\n" \
  "s_cselect_b32 s34, s28, s34\n" \
  "buffer_load_dwordx4 v[64:67], v155, s[8:11], s34 offen sc1\n" \
  "s_waitcnt vmcnt(29)\n" \
  "v_mfma_f32_32x32x16_bf16 v[108:123], a[144:147], v[72:75], v[108:123]\n" \
  "v_mfma_f32_32x32x16_bf16 v[124:139], a[148:151], v[72:75], v[124:139]\n" \
  "s_add_u32 s34, s34, 1024\n" \
  "s_cmp_eq_u32 s34, s42\n" \
  "s_cselect_b32 s34, s28, s34\n" \
  "buffer_load_dwordx4 v[68:71], v155, s[8:11], s34 offen sc1\n" \
  "s_waitcnt vmcnt(29)\n" \
  "v_mfma_f32_32x32x16_bf16 v[108:123], a[152:155], v[76:79], v[108:123]\n" \
  "v_mfma_f32_32x32x16_bf16 v[124:139], a[156:159], v[76:79], v[124:139]\n" \
  "s_add_u32 s34, s34, 1024\n" \
  "s_cmp_eq_u32 s34, s42\n" \
  "s_cselect_b32 s34, s28, s34\n" \
  "buffer_load_dwordx4 v[72:75], v155, s[8:11], s34 offen sc1\n" \
  "s_waitcnt vmcnt(29)\n" \
  "v_mfma_f32_32x32x16_bf16 v[108:123], a[160:163], v[80:83], v[108:123]\n" \
  "v_mfma_f32_32x32x16_bf16 v[124:139], a[164:167], v[80:83], v[124:139]\n" \
  "s_add_u32 s34, s34, 1024\n" \
  "s_cmp_eq_u32 s34, s42\n" \
  "s_cselect_b32 s34, s28, s34\n" \
  "buffer_load_dwordx4 v[76:79], v155, s[8:11], s34 offen sc1\n" \
  "s_waitcnt vmcnt(29)\n" \
  "v_mfma_f32_32x32x16_bf16 v[108:123], a[168:171], v[84:87], v[108:123]\n" \
  "v_mfma_f32_32x32x16_bf16 v[124:139], a[172:175], v[84:87], v[124:139]\n" \
  "s_add_u32 s34, s34, 1024\n" \
  "s_cmp_eq_u32 s34, s42\n" \
  "s_cselect_b32 s34, s28, s34\n" \
  "buffer_load_dwordx4 v[80:83], v155, s[8:11], s34 offen sc1\n" \
  "s_waitcnt vmcnt(29)\n" \
  "v_mfma_f32_32x32x16_bf16 v[108:123], a[176:179], v[88:91], v[108:123]\n" \
  "v_mfma_f32_32x32x16_bf16 v[124:139], a[180:183], v[88:91], v[124:139]\n" \
  "s_add_u32 s34, s34, 1024\n" \
  "s_cmp_eq_u32 s34, s42\n" \
  "s_cselect_b32 s34, s28, s34\n" \
  "buffer_load_dwordx4 v[84:87], v155, s[8:11], s34 offen sc1\n" \
  "s_waitcnt vmcnt(29)\n" \
  "v_mfma_f32_32x32x16_bf16 v[108:123], a[184:187], v[92:95], v[108:123]\n" \
  "v_mfma_f32_32x32x16_bf16 v[124:139], a[188:191], v[92:95], v[124:139]\n" \
  "s_add_u32 s34, s34, 1024\n" \
  "s_cmp_eq_u32 s34, s42\n" \
  "s_cselect_b32 s34, s28, s34\n" \
  "buffer_load_dwordx4 v[88:91], v155, s[8:11], s34 offen sc1\n" \
  "s_waitcnt vmcnt(29)\n" \
  "v_mfma_f32_32x32x16_bf16 v[108:123], a[192:195], v[96:99], v[108:123]\n" \
  "v_mfma_f32_32x32x16_bf16 v[124:139], a[196:199], v[96:99], v[124:139]\n" \
  "s_add_u32 s34, s34, 1024\n" \
  "s_cmp_eq_u32 s34, s42\n" \
  "s_cselect_b32 s34, s28, s34\n" \
  "buffer_load_dwordx4 v[92:95], v155, s[8:11], s34 offen sc1\n" \
  "s_waitcnt vmcnt(29)\n" \
  "v_mfma_f32_32x32x16_bf16 v[108:123], a[200:203], v[100:103], v[108:123]\n" \
  "v_mfma_f32_32x32x16_bf16 v[124:139], a[204:207], v[100:103], v[124:139]\n" \
  "s_add_u32 s34, s34, 1024\n" \
  "s_cmp_eq_u32 s34, s42\n" \
  "s_cselect_b32 s34, s28, s34\n" \
  "buffer_load_dwordx4 v[96:99], v155, s[8:11], s34 offen sc1\n" \
  "s_waitcnt vmcnt(29)\n" \
  "v_mfma_f32_32x32x16_bf16 v[108:123], a[208:211], v[104:107], v[108:123]\n" \
  "v_mfma_f32_32x32x16_bf16 v[124:139], a[212:215], v[104:107], v[124:139]\n" \
  "s_add_u32 s34, s34, 1024\n" \
  "s_cmp_eq_u32 s34, s42\n" \
  "s_cselect_b32 s34, s28, s34\n" \
  "buffer_load_dwordx4 v[100:103], v155, s[8:11], s34 offen sc1\n" \
  "s_nop 3\n" \
  "s_add_u32 s34, s34, 1024\n" \
  "s_cmp_eq_u32 s34, s42\n" \
  "s_cselect_b32 s34, s28, s34\n" \
  "buffer_load_dwordx4 v[104:107], v155, s[8:11], s34 offen sc1\n" \
  "ds_write_b128 v143, v[108:111]\n" \
  "ds_write_b128 v143, v[112:115] offset:1024\n" \
  "ds_write_b128 v143, v[116:119] offset:8192\n" \
  "ds_write_b128 v143, v[120:123] offset:9216\n" \
  "ds_write_b128 v143, v[124:127] offset:16384\n" \
  "ds_write_b128 v143, v[128:131] offset:17408\n" \
  "ds_write_b128 v143, v[132:135] offset:24576\n" \
  "ds_write_b128 v143, v[136:139] offset:25600\n" \
  "v_xor_b32 v143, 0x8000, v143\n" \
  "s_cmp_eq_u32 s27, 63\n" \
  "s_cbranch_scc1 BE_%=\n" \
  "s_cmp_eq_u32 s35, 0\n" \
  "s_cbranch_scc1 BN_%=\n" \
  "s_waitcnt vmcnt(30)\n" \
  "v_max_i32 v158, v158, v150\n" \
  "v_cmp_ge_i32 vcc, s27, v158\n" \
  "s_waitcnt vmcnt(29)\n" \
  "s_nop 3\n" \
  "s_cmp_lg_u64 vcc, 0\n" \
  "s_cbranch_scc0 BPG_%=\n" \
  "BP_%=:\n" \
  "buffer_load_dword v150, v141, s[8:11], s32 offen sc1\n" \
  "buffer_load_dword v152, v142, s[8:11], s29 offen sc1\n" \
  "s_waitcnt vmcnt(1)\n" \
  "v_max_i32 v158, v158, v150\n" \
  "v_cmp_ge_i32 vcc, s27, v158\n" \
  "s_waitcnt vmcnt(0)\n" \
  "s_nop 3\n" \
  "s_cmp_lg_u64 vcc, 0\n" \
  "s_cbranch_scc0 BPG_%=\n" \
  "s_sleep 2\n" \
  "s_sub_u32 s33, s33, 1\n" \
  "s_cbranch_scc0 BP_%=\n" \
  "s_mov_b32 s33, 0\n" \
  "BPG_%=:\n" \
  "v_mov_b32 v149, v152\n" \
  "s_add_u32 s29, s29, 128\n" \
  "s_add_u32 s28, s28, 110592\n" \
  "s_add_u32 s43, s27, 2\n" \
  "v_cmp_gt_i32 vcc, s43, v158\n" \
  "s_nop 3\n" \
  "s_cmp_eq_u64 vcc, 0\n" \
  "s_cselect_b32 s43, 0x80000000, 0\n" \
  "v_or_b32 v159, s43, v141\n" \
  "buffer_load_dword v150, v159, s[8:11], s32 offen sc1\n" \
  "buffer_load_dword v152, v142, s[8:11], s29 offen sc1\n" \
  "s_add_u32 s34, s28, s37\n" \
  "s_add_u32 s42, s28, 27648\n" \
  "buffer_load_dwordx4 v[0:3], v140, s[8:11], s34 offen sc1\n" \
  "s_add_u32 s34, s34, 1024\n" \
  "s_cmp_eq_u32 s34, s42\n" \
  "s_cselect_b32 s34, s28, s34\n" \
  "buffer_load_dwordx4 v[4:7], v140, s[8:11], s34 offen sc1\n" \
  "s_add_u32 s34, s34, 1024\n" \
  "s_cmp_eq_u32 s34, s42\n" \
  "s_cselect_b32 s34, s28, s34\n" \
  "buffer_load_dwordx4 v[8:11], v140, s[8:11], s34 offen sc1\n" \
  "s_add_u32 s34, s34, 1024\n" \
  "s_cmp_eq_u32 s34, s42\n" \
  "s_cselect_b32 s34, s28, s34\n" \
  "buffer_load_dwordx4 v[12:15], v140, s[8:11], s34 offen sc1\n" \
  "s_add_u32 s34, s34, 1024\n" \
  "s_cmp_eq_u32 s34, s42\n" \
  "s_cselect_b32 s34, s28, s34\n" \
  "buffer_load_dwordx4 v[16:19], v140, s[8:11], s34 offen sc1\n" \
  "s_add_u32 s34, s34, 1024\n" \
  "s_cmp_eq_u32 s34, s42\n" \
  "s_cselect_b32 s34, s28, s34\n" \
  "buffer_load_dwordx4 v[20:23], v140, s[8:11], s34 offen sc1\n" \
  "s_add_u32 s34, s34, 1024\n" \
  "s_cmp_eq_u32 s34, s42\n" \
  "s_cselect_b32 s34, s28, s34\n" \
  "buffer_load_dwordx4 v[24:27], v140, s[8:11], s34 offen sc1\n" \
  "s_add_u32 s34, s34, 1024\n" \
  "s_cmp_eq_u32 s34, s42\n" \
  "s_cselect_b32 s34, s28, s34\n" \
  "buffer_load_dwordx4 v[28:31], v140, s[8:11], s34 offen sc1\n" \
  "s_add_u32 s34, s34, 1024\n" \
  "s_cmp_eq_u32 s34, s42\n" \
  "s_cselect_b32 s34, s28, s34\n" \
  "buffer_load_dwordx4 v[32:35], v140, s[8:11], s34 offen sc1\n" \
  "s_add_u32 s34, s34, 1024\n" \
  "s_cmp_eq_u32 s34, s42\n" \
  "s_cselect_b32 s34, s28, s34\n" \
  "buffer_load_dwordx4 v[36:39], v140, s[8:11], s34 offen sc1\n" \
  "s_add_u32 s34, s34, 1024\n" \
  "s_cmp_eq_u32 s34, s42\n" \
  "s_cselect_b32 s34, s28, s34\n" \
  "buffer_load_dwordx4 v[40:43], v140, s[8:11], s34 offen sc1\n" \
  "s_add_u32 s34, s34, 1024\n" \
  "s_cmp_eq_u32 s34, s42\n" \
  "s_cselect_b32 s34, s28, s34\n" \
  "buffer_load_dwordx4 v[44:47], v140, s[8:11], s34 offen sc1\n" \
  "s_add_u32 s34, s34, 1024\n" \
  "s_cmp_eq_u32 s34, s42\n" \
  "s_cselect_b32 s34, s28, s34\n" \
  "buffer_load_dwordx4 v[48:51], v140, s[8:11], s34 offen sc1\n" \
  "s_add_u32 s34, s34, 1024\n" \
  "s_cmp_eq_u32 s34, s42\n" \
  "s_cselect_b32 s34, s28, s34\n" \
  "buffer_load_dwordx4 v[52:55], v140, s[8:11], s34 offen sc1\n" \
  "s_add_u32 s34, s34, 1024\n" \
  "s_cmp_eq_u32 s34, s42\n" \
  "s_cselect_b32 s34, s28, s34\n" \
  "buffer_load_dwordx4 v[56:59], v140, s[8:11], s34 offen sc1\n" \
  "s_add_u32 s34, s34, 1024\n" \
  "s_cmp_eq_u32 s34, s42\n" \
  "s_cselect_b32 s34, s28, s34\n" \
  "buffer_load_dwordx4 v[60:63], v140, s[8:11], s34 offen sc1\n" \
  "s_add_u32 s34, s34, 1024\n" \
  "s_cmp_eq_u32 s34, s42\n" \
  "s_cselect_b32 s34, s28, s34\n" \
  "buffer_load_dwordx4 v[64:67], v140, s[8:11], s34 offen sc1\n" \
  "s_add_u32 s34, s34, 1024\n" \
  "s_cmp_eq_u32 s34, s42\n" \
  "s_cselect_b32 s34, s28, s34\n" \
  "buffer_load_dwordx4 v[68:71], v140, s[8:11], s34 offen sc1\n" \
  "s_add_u32 s34, s34, 1024\n" \
  "s_cmp_eq_u32 s34, s42\n" \
  "s_cselect_b32 s34, s28, s34\n" \
  "buffer_load_dwordx4 v[72:75], v140, s[8:11], s34 offen sc1\n" \
  "s_add_u32 s34, s34, 1024\n" \
  "s_cmp_eq_u32 s34, s42\n" \
  "s_cselect_b32 s34, s28, s34\n" \
  "buffer_load_dwordx4 v[76:79], v140, s[8:11], s34 offen sc1\n" \
  "s_add_u32 s34, s34, 1024\n" \
  "s_cmp_eq_u32 s34, s42\n" \
  "s_cselect_b32 s34, s28, s34\n" \
  "buffer_load_dwordx4 v[80:83], v140, s[8:11], s34 offen sc1\n" \
  "s_add_u32 s34, s34, 1024\n" \
  "s_cmp_eq_u32 s34, s42\n" \
  "s_cselect_b32 s34, s28, s34\n" \
  "buffer_load_dwordx4 v[84:87], v140, s[8:11], s34 offen sc1\n" \
  "s_add_u32 s34, s34, 1024\n" \
  "s_cmp_eq_u32 s34, s42\n" \
  "s_cselect_b32 s34, s28, s34\n" \
  "buffer_load_dwordx4 v[88:91], v140, s[8:11], s34 offen sc1\n" \
  "s_add_u32 s34, s34, 1024\n" \
  "s_cmp_eq_u32 s34, s42\n" \
  "s_cselect_b32 s34, s28, s34\n" \
  "buffer_load_dwordx4 v[92:95], v140, s[8:11], s34 offen sc1\n" \
  "s_add_u32 s34, s34, 1024\n" \
  "s_cmp_eq_u32 s34, s42\n" \
  "s_cselect_b32 s34, s28, s34\n" \
  "buffer_load_dwordx4 v[96:99], v140, s[8:11], s34 offen sc1\n" \
  "s_add_u32 s34, s34, 1024\n" \
  "s_cmp_eq_u32 s34, s42\n" \
  "s_cselect_b32 s34, s28, s34\n" \
  "buffer_load_dwordx4 v[100:103], v140, s[8:11], s34 offen sc1\n" \
  "s_add_u32 s34, s34, 1024\n" \
  "s_cmp_eq_u32 s34, s42\n" \
  "s_cselect_b32 s34, s28, s34\n" \
  "buffer_load_dwordx4 v[104:107], v140, s[8:11], s34 offen sc1\n" \
  "BN_%=:\n" \
  "s_add_u32 s27, s27, 1\n" \
  "s_waitcnt lgkmcnt(0)\n" \
  "s_branch BL_%=\n" \
  "BE_%=:\n" \
  "s_waitcnt lgkmcnt(0)\n" \
  "s_barrier\n" \
  "ds_read_b128 v[160:163], v144\n" \
  "ds_read_b128 v[164:167], v144 offset:1024\n" \
  "ds_read_b128 v[168:171], v144 offset:2048\n" \
  "ds_read_b128 v[172:175], v144 offset:3072\n" \
  "ds_read_b128 v[176:179], v144 offset:4096\n" \
  "ds_read_b128 v[180:183], v144 offset:5120\n" \
  "ds_read_b128 v[184:187], v144 offset:6144\n" \
  "ds_read_b128 v[188:191], v144 offset:7168\n" \
  "s_waitcnt lgkmcnt(5)\n" \
  "v_add_f32 v160, v160, v168\n" \
  "v_add_f32 v161, v161, v169\n" \
  "v_add_f32 v162, v162, v170\n" \
  "v_add_f32 v163, v163, v171\n" \
  "s_waitcnt lgkmcnt(4)\n" \
  "v_add_f32 v164, v164, v172\n" \
  "v_add_f32 v165, v165, v173\n" \
  "v_add_f32 v166, v166, v174\n" \
  "v_add_f32 v167, v167, v175\n" \
  "s_waitcnt lgkmcnt(1)\n" \
  "v_add_f32 v176, v176, v184\n" \
  "v_add_f32 v177, v177, v185\n" \
  "v_add_f32 v178, v178, v186\n" \
  "v_add_f32 v179, v179, v187\n" \
  "s_waitcnt lgkmcnt(0)\n" \
  "v_add_f32 v180, v180, v188\n" \
  "v_add_f32 v181, v181, v189\n" \
  "v_add_f32 v182, v182, v190\n" \
  "v_add_f32 v183, v183, v191\n" \
  "v_xor_b32 v144, 0x8000, v144\n" \
  "v_add_f32 v160, v160, v176\n" \
  "v_add_f32 v161, v161, v177\n" \
  "v_add_f32 v162, v162, v178\n" \
  "v_add_f32 v163, v163, v179\n" \
  "v_add_f32 v164, v164, v180\n" \
  "v_add_f32 v165, v165, v181\n" \
  "v_add_f32 v166, v166, v182\n" \
  "v_add_f32 v167, v167, v183\n" \
  "v_cvt_pk_bf16_f32 v168, v160, v161\n" \
  "v_cvt_pk_bf16_f32 v169, v162, v163\n" \
  "v_cvt_pk_bf16_f32 v170, v164, v165\n" \
  "v_cvt_pk_bf16_f32 v171, v166, v167\n" \
  "v_lshlrev_b32 v160, 16, v168\n" \
  "v_and_b32 v161, 0xffff0000, v168\n" \
  "v_lshlrev_b32 v162, 16, v169\n" \
  "v_and_b32 v163, 0xffff0000, v169\n" \
  "v_lshlrev_b32 v164, 16, v170\n" \
  "v_and_b32 v165, 0xffff0000, v170\n" \
  "v_lshlrev_b32 v166, 16, v171\n" \
  "v_and_b32 v167, 0xffff0000, v171\n" \
  "v_mul_f32 v160, v160, v151\n" \
  "v_mul_f32 v161, v161, v151\n" \
  "v_mul_f32 v162, v162, v151\n" \
  "v_mul_f32 v163, v163, v151\n" \
  "v_mul_f32 v164, v164, v151\n" \
  "v_mul_f32 v165, v165, v151\n" \
  "v_mul_f32 v166, v166, v151\n" \
  "v_mul_f32 v167, v167, v151\n" \
  "v_cvt_pk_bf16_f32 v168, v160, v161\n" \
  "v_cvt_pk_bf16_f32 v169, v162, v163\n" \
  "v_cvt_pk_bf16_f32 v170, v164, v165\n" \
  "v_cvt_pk_bf16_f32 v171, v166, v167\n" \
  "buffer_store_dwordx4 v[168:171], v157, s[8:11], s30 offen sc1\n" \
  "s_add_u32 s30, s30, 40960\n" \
  "v_mov_b32 v157, v145\n" \
  "v_sub_f32 v151, 1.0, v156\n" \
  "v_cvt_pk_bf16_f32 v151, v151, v151\n" \
  "v_lshlrev_b32 v151, 16, v151\n" \
  "s_waitcnt vmcnt(0)\n" \
  "v_mov_b32 v153, 63\n" \
  "s_mov_b32 exec_hi, 0\n" \
  "buffer_store_dword v153, v154, s[8:11], s31 offen sc1\n" \
  "s_mov_b32 exec_hi, -1\n" \
  "s_waitcnt vmcnt(0)\n"

#define ASM_Z \
  "s_add_u32 m0, s24, 0\n" \
  "s_nop 0\n" \
  "buffer_load_dwordx4 v148, s[12:15], s25 offen lds\n" \
  "buffer_load_dwordx4 v149, s[12:15], s25 offen offset:1024 lds\n" \
  "s_add_u32 s25, s25, s26\n" \
  "s_add_u32 m0, s24, 2048\n" \
  "s_nop 0\n" \
  "buffer_load_dwordx4 v148, s[12:15], s25 offen lds\n" \
  "buffer_load_dwordx4 v149, s[12:15], s25 offen offset:1024 lds\n" \
  "s_add_u32 s25, s25, s26\n" \
  "s_add_u32 m0, s24, 4096\n" \
  "s_nop 0\n" \
  "buffer_load_dwordx4 v148, s[12:15], s25 offen lds\n" \
  "buffer_load_dwordx4 v149, s[12:15], s25 offen offset:1024 lds\n" \
  "s_add_u32 s25, s25, s26\n" \
  "s_add_u32 m0, s24, 6144\n" \
  "s_nop 0\n" \
  "buffer_load_dwordx4 v148, s[12:15], s25 offen lds\n" \
  "buffer_load_dwordx4 v149, s[12:15], s25 offen offset:1024 lds\n" \
  "s_add_u32 s25, s25, s26\n" \
  "s_cmp_eq_u32 s25, 1310720\n" \
  "s_cselect_b32 s25, 0, s25\n" \
  "s_add_u32 m0, s24, 8192\n" \
  "s_nop 0\n" \
  "buffer_load_dwordx4 v148, s[12:15], s25 offen lds\n" \
  "buffer_load_dwordx4 v149, s[12:15], s25 offen offset:1024 lds\n" \
  "s_add_u32 s25, s25, s26\n" \
  "s_add_u32 m0, s24, 10240\n" \
  "s_nop 0\n" \
  "buffer_load_dwordx4 v148, s[12:15], s25 offen lds\n" \
  "buffer_load_dwordx4 v149, s[12:15], s25 offen offset:1024 lds\n" \
  "s_add_u32 s25, s25, s26\n" \
  "s_add_u32 m0, s24, 12288\n" \
  "s_nop 0\n" \
  "buffer_load_dwordx4 v148, s[12:15], s25 offen lds\n" \
  "buffer_load_dwordx4 v149, s[12:15], s25 offen offset:1024 lds\n" \
  "s_add_u32 s25, s25, s26\n" \
  "s_add_u32 m0, s24, 14336\n" \
  "s_nop 0\n" \
  "buffer_load_dwordx4 v148, s[12:15], s25 offen lds\n" \
  "buffer_load_dwordx4 v149, s[12:15], s25 offen offset:1024 lds\n" \
  "s_add_u32 s25, s25, s26\n" \
  "s_cmp_eq_u32 s25, 1310720\n" \
  "s_cselect_b32 s25, 0, s25\n" \
  "s_add_u32 m0, s24, 16384\n" \
  "s_nop 0\n" \
  "buffer_load_dwordx4 v148, s[12:15], s25 offen lds\n" \
  "buffer_load_dwordx4 v149, s[12:15], s25 offen offset:1024 lds\n" \
  "s_add_u32 s25, s25, s26\n" \
  "s_add_u32 m0, s24, 18432\n" \
  "s_nop 0\n" \
  "buffer_load_dwordx4 v148, s[12:15], s25 offen lds\n" \
  "buffer_load_dwordx4 v149, s[12:15], s25 offen offset:1024 lds\n" \
  "s_add_u32 s25, s25, s26\n" \
  "s_add_u32 m0, s24, 20480\n" \
  "s_nop 0\n" \
  "buffer_load_dwordx4 v148, s[12:15], s25 offen lds\n" \
  "buffer_load_dwordx4 v149, s[12:15], s25 offen offset:1024 lds\n" \
  "s_add_u32 s25, s25, s26\n" \
  "s_add_u32 m0, s24, 22528\n" \
  "s_nop 0\n" \
  "buffer_load_dwordx4 v148, s[12:15], s25 offen lds\n" \
  "buffer_load_dwordx4 v149, s[12:15], s25 offen offset:1024 lds\n" \
  "s_add_u32 s25, s25, s26\n" \
  "s_cmp_eq_u32 s25, 1310720\n" \
  "s_cselect_b32 s25, 0, s25\n" \
  "s_add_u32 m0, s24, 24576\n" \
  "s_nop 0\n" \
  "buffer_load_dwordx4 v148, s[12:15], s25 offen lds\n" \
  "buffer_load_dwordx4 v149, s[12:15], s25 offen offset:1024 lds\n" \
  "s_add_u32 s25, s25, s26\n" \
  "s_add_u32 m0, s24, 26624\n" \
  "s_nop 0\n" \
  "buffer_load_dwordx4 v148, s[12:15], s25 offen lds\n" \
  "buffer_load_dwordx4 v149, s[12:15], s25 offen offset:1024 lds\n" \
  "s_add_u32 s25, s25, s26\n" \
  "s_add_u32 m0, s24, 28672\n" \
  "s_nop 0\n" \
  "buffer_load_dwordx4 v148, s[12:15], s25 offen lds\n" \
  "buffer_load_dwordx4 v149, s[12:15], s25 offen offset:1024 lds\n" \
  "s_add_u32 s25, s25, s26\n" \
  "s_waitcnt vmcnt(28)\n" \
  "ds_read_b64_tr_b16 a[0:1], v150\n" \
  "ds_read_b64_tr_b16 a[2:3], v150 offset:512\n" \
  "ds_read_b64_tr_b16 a[4:5], v150 offset:8\n" \
  "ds_read_b64_tr_b16 a[6:7], v150 offset:520\n" \
  "s_waitcnt vmcnt(26)\n" \
  "ds_read_b64_tr_b16 a[8:9], v150 offset:2048\n" \
  "ds_read_b64_tr_b16 a[10:11], v150 offset:2560\n" \
  "ds_read_b64_tr_b16 a[12:13], v150 offset:2056\n" \
  "ds_read_b64_tr_b16 a[14:15], v150 offset:2568\n" \
  "s_waitcnt lgkmcnt(4)\n" \
  "s_add_u32 m0, s24, 0\n" \
  "s_nop 0\n" \
  "buffer_load_dwordx4 v148, s[12:15], s25 offen lds\n" \
  "buffer_load_dwordx4 v149, s[12:15], s25 offen offset:1024 lds\n" \
  "s_add_u32 s25, s25, s26\n" \
  "s_cmp_eq_u32 s25, 1310720\n" \
  "s_cselect_b32 s25, 0, s25\n" \
  "s_waitcnt vmcnt(26)\n" \
  "ds_read_b64_tr_b16 a[16:17], v150 offset:4096\n" \
  "ds_read_b64_tr_b16 a[18:19], v150 offset:4608\n" \
  "ds_read_b64_tr_b16 a[20:21], v150 offset:4104\n" \
  "ds_read_b64_tr_b16 a[22:23], v150 offset:4616\n" \
  "s_waitcnt lgkmcnt(4)\n" \
  "s_add_u32 m0, s24, 2048\n" \
  "s_nop 0\n" \
  "buffer_load_dwordx4 v148, s[12:15], s25 offen lds\n" \
  "buffer_load_dwordx4 v149, s[12:15], s25 offen offset:1024 lds\n" \
  "s_add_u32 s25, s25, s26\n" \
  "s_waitcnt vmcnt(26)\n" \
  "ds_read_b64_tr_b16 a[24:25], v150 offset:6144\n" \
  "ds_read_b64_tr_b16 a[26:27], v150 offset:6656\n" \
  "ds_read_b64_tr_b16 a[28:29], v150 offset:6152\n" \
  "ds_read_b64_tr_b16 a[30:31], v150 offset:6664\n" \
  "s_waitcnt lgkmcnt(4)\n" \
  "s_add_u32 m0, s24, 4096\n" \
  "s_nop 0\n" \
  "buffer_load_dwordx4 v148, s[12:15], s25 offen lds\n" \
  "buffer_load_dwordx4 v149, s[12:15], s25 offen offset:1024 lds\n" \
  "s_add_u32 s25, s25, s26\n" \
  "s_waitcnt vmcnt(26)\n" \
  "ds_read_b64_tr_b16 a[32:33], v150 offset:8192\n" \
  "ds_read_b64_tr_b16 a[34:35], v150 offset:8704\n" \
  "ds_read_b64_tr_b16 a[36:37], v150 offset:8200\n" \
  "ds_read_b64_tr_b16 a[38:39], v150 offset:8712\n" \
  "s_waitcnt lgkmcnt(4)\n" \
  "s_add_u32 m0, s24, 6144\n" \
  "s_nop 0\n" \
  "buffer_load_dwordx4 v148, s[12:15], s25 offen lds\n" \
  "buffer_load_dwordx4 v149, s[12:15], s25 offen offset:1024 lds\n" \
  "s_add_u32 s25, s25, s26\n" \
  "s_waitcnt vmcnt(26)\n" \
  "ds_read_b64_tr_b16 a[40:41], v150 offset:10240\n" \
  "ds_read_b64_tr_b16 a[42:43], v150 offset:10752\n" \
  "ds_read_b64_tr_b16 a[44:45], v150 offset:10248\n" \
  "ds_read_b64_tr_b16 a[46:47], v150 offset:10760\n" \
  "s_waitcnt lgkmcnt(4)\n" \
  "s_add_u32 m0, s24, 8192\n" \
  "s_nop 0\n" \
  "buffer_load_dwordx4 v148, s[12:15], s25 offen lds\n" \
  "buffer_load_dwordx4 v149, s[12:15], s25 offen offset:1024 lds\n" \
  "s_add_u32 s25, s25, s26\n" \
  "s_cmp_eq_u32 s25, 1310720\n" \
  "s_cselect_b32 s25, 0, s25\n" \
  "s_waitcnt vmcnt(26)\n" \
  "ds_read_b64_tr_b16 a[48:49], v150 offset:12288\n" \
  "ds_read_b64_tr_b16 a[50:51], v150 offset:12800\n" \
  "ds_read_b64_tr_b16 a[52:53], v150 offset:12296\n" \
  "ds_read_b64_tr_b16 a[54:55], v150 offset:12808\n" \
  "s_waitcnt lgkmcnt(4)\n" \
  "s_add_u32 m0, s24, 10240\n" \
  "s_nop 0\n" \
  "buffer_load_dwordx4 v148, s[12:15], s25 offen lds\n" \
  "buffer_load_dwordx4 v149, s[12:15], s25 offen offset:1024 lds\n" \
  "s_add_u32 s25, s25, s26\n" \
  "s_waitcnt vmcnt(26)\n" \
  "ds_read_b64_tr_b16 a[56:57], v150 offset:14336\n" \
  "ds_read_b64_tr_b16 a[58:59], v150 offset:14848\n" \
  "ds_read_b64_tr_b16 a[60:61], v150 offset:14344\n" \
  "ds_read_b64_tr_b16 a[62:63], v150 offset:14856\n" \
  "s_waitcnt lgkmcnt(4)\n" \
  "s_add_u32 m0, s24, 12288\n" \
  "s_nop 0\n" \
  "buffer_load_dwordx4 v148, s[12:15], s25 offen lds\n" \
  "buffer_load_dwordx4 v149, s[12:15], s25 offen offset:1024 lds\n" \
  "s_add_u32 s25, s25, s26\n" \
  "s_waitcnt vmcnt(26)\n" \
  "ds_read_b64_tr_b16 a[64:65], v150 offset:16384\n" \
  "ds_read_b64_tr_b16 a[66:67], v150 offset:16896\n" \
  "ds_read_b64_tr_b16 a[68:69], v150 offset:16392\n" \
  "ds_read_b64_tr_b16 a[70:71], v150 offset:16904\n" \
  "s_waitcnt lgkmcnt(4)\n" \
  "s_add_u32 m0, s24, 14336\n" \
  "s_nop 0\n" \
  "buffer_load_dwordx4 v148, s[12:15], s25 offen lds\n" \
  "buffer_load_dwordx4 v149, s[12:15], s25 offen offset:1024 lds\n" \
  "s_add_u32 s25, s25, s26\n" \
  "s_waitcnt vmcnt(26)\n" \
  "ds_read_b64_tr_b16 a[72:73], v150 offset:18432\n" \
  "ds_read_b64_tr_b16 a[74:75], v150 offset:18944\n" \
  "ds_read_b64_tr_b16 a[76:77], v150 offset:18440\n" \
  "ds_read_b64_tr_b16 a[78:79], v150 offset:18952\n" \
  "s_waitcnt lgkmcnt(4)\n" \
  "s_add_u32 m0, s24, 16384\n" \
  "s_nop 0\n" \
  "buffer_load_dwordx4 v148, s[12:15], s25 offen lds\n" \
  "buffer_load_dwordx4 v149, s[12:15], s25 offen offset:1024 lds\n" \
  "s_add_u32 s25, s25, s26\n" \
  "s_cmp_eq_u32 s25, 1310720\n" \
  "s_cselect_b32 s25, 0, s25\n" \
  "s_waitcnt vmcnt(26)\n" \
  "ds_read_b64_tr_b16 a[80:81], v150 offset:20480\n" \
  "ds_read_b64_tr_b16 a[82:83], v150 offset:20992\n" \
  "ds_read_b64_tr_b16 a[84:85], v150 offset:20488\n" \
  "ds_read_b64_tr_b16 a[86:87], v150 offset:21000\n" \
  "s_waitcnt lgkmcnt(4)\n" \
  "s_add_u32 m0, s24, 18432\n" \
  "s_nop 0\n" \
  "buffer_load_dwordx4 v148, s[12:15], s25 offen lds\n" \
  "buffer_load_dwordx4 v149, s[12:15], s25 offen offset:1024 lds\n" \
  "s_add_u32 s25, s25, s26\n" \
  "s_waitcnt vmcnt(26)\n" \
  "ds_read_b64_tr_b16 a[88:89], v150 offset:22528\n" \
  "ds_read_b64_tr_b16 a[90:91], v150 offset:23040\n" \
  "ds_read_b64_tr_b16 a[92:93], v150 offset:22536\n" \
  "ds_read_b64_tr_b16 a[94:95], v150 offset:23048\n" \
  "s_waitcnt lgkmcnt(4)\n" \
  "s_add_u32 m0, s24, 20480\n" \
  "s_nop 0\n" \
  "buffer_load_dwordx4 v148, s[12:15], s25 offen lds\n" \
  "buffer_load_dwordx4 v149, s[12:15], s25 offen offset:1024 lds\n" \
  "s_add_u32 s25, s25, s26\n" \
  "s_waitcnt vmcnt(26)\n" \
  "ds_read_b64_tr_b16 a[96:97], v150 offset:24576\n" \
  "ds_read_b64_tr_b16 a[98:99], v150 offset:25088\n" \
  "ds_read_b64_tr_b16 a[100:101], v150 offset:24584\n" \
  "ds_read_b64_tr_b16 a[102:103], v150 offset:25096\n" \
  "s_waitcnt lgkmcnt(4)\n" \
  "s_add_u32 m0, s24, 22528\n" \
  "s_nop 0\n" \
  "buffer_load_dwordx4 v148, s[12:15], s25 offen lds\n" \
  "buffer_load_dwordx4 v149, s[12:15], s25 offen offset:1024 lds\n" \
  "s_add_u32 s25, s25, s26\n" \
  "s_waitcnt vmcnt(26)\n" \
  "ds_read_b64_tr_b16 a[104:105], v150 offset:26624\n" \
  "ds_read_b64_tr_b16 a[106:107], v150 offset:27136\n" \
  "ds_read_b64_tr_b16 a[108:109], v150 offset:26632\n" \
  "ds_read_b64_tr_b16 a[110:111], v150 offset:27144\n" \
  "s_waitcnt lgkmcnt(4)\n" \
  "s_add_u32 m0, s24, 24576\n" \
  "s_nop 0\n" \
  "buffer_load_dwordx4 v148, s[12:15], s25 offen lds\n" \
  "buffer_load_dwordx4 v149, s[12:15], s25 offen offset:1024 lds\n" \
  "s_add_u32 s25, s25, s26\n" \
  "s_cmp_eq_u32 s25, 1310720\n" \
  "s_cselect_b32 s25, 0, s25\n" \
  "s_waitcnt vmcnt(26)\n" \
  "ds_read_b64_tr_b16 a[112:113], v150 offset:28672\n" \
  "ds_read_b64_tr_b16 a[114:115], v150 offset:29184\n" \
  "ds_read_b64_tr_b16 a[116:117], v150 offset:28680\n" \
  "ds_read_b64_tr_b16 a[118:119], v150 offset:29192\n" \
  "s_waitcnt lgkmcnt(4)\n" \
  "s_add_u32 m0, s24, 26624\n" \
  "s_nop 0\n" \
  "buffer_load_dwordx4 v148, s[12:15], s25 offen lds\n" \
  "buffer_load_dwordx4 v149, s[12:15], s25 offen offset:1024 lds\n" \
  "s_add_u32 s25, s25, s26\n" \
  "s_waitcnt vmcnt(26)\n" \
  "ds_read_b64_tr_b16 a[120:121], v150\n" \
  "ds_read_b64_tr_b16 a[122:123], v150 offset:512\n" \
  "ds_read_b64_tr_b16 a[124:125], v150 offset:8\n" \
  "ds_read_b64_tr_b16 a[126:127], v150 offset:520\n" \
  "s_waitcnt lgkmcnt(4)\n" \
  "s_add_u32 m0, s24, 28672\n" \
  "s_nop 0\n" \
  "buffer_load_dwordx4 v148, s[12:15], s25 offen lds\n" \
  "buffer_load_dwordx4 v149, s[12:15], s25 offen offset:1024 lds\n" \
  "s_add_u32 s25, s25, s26\n" \
  "s_waitcnt vmcnt(26)\n" \
  "ds_read_b64_tr_b16 a[128:129], v150 offset:2048\n" \
  "ds_read_b64_tr_b16 a[130:131], v150 offset:2560\n" \
  "ds_read_b64_tr_b16 a[132:133], v150 offset:2056\n" \
  "ds_read_b64_tr_b16 a[134:135], v150 offset:2568\n" \
  "s_waitcnt lgkmcnt(4)\n" \
  "s_add_u32 m0, s24, 0\n" \
  "s_nop 0\n" \
  "buffer_load_dwordx4 v148, s[12:15], s25 offen lds\n" \
  "buffer_load_dwordx4 v149, s[12:15], s25 offen offset:1024 lds\n" \
  "s_add_u32 s25, s25, s26\n" \
  "s_waitcnt vmcnt(26)\n" \
  "ds_read_b64_tr_b16 a[136:137], v150 offset:4096\n" \
  "ds_read_b64_tr_b16 a[138:139], v150 offset:4608\n" \
  "ds_read_b64_tr_b16 a[140:141], v150 offset:4104\n" \
  "ds_read_b64_tr_b16 a[142:143], v150 offset:4616\n" \
  "s_waitcnt lgkmcnt(4)\n" \
  "s_add_u32 m0, s24, 2048\n" \
  "s_nop 0\n" \
  "buffer_load_dwordx4 v148, s[12:15], s25 offen lds\n" \
  "buffer_load_dwordx4 v149, s[12:15], s25 offen offset:1024 lds\n" \
  "s_add_u32 s25, s25, s26\n" \
  "s_cmp_eq_u32 s25, 1310720\n" \
  "s_cselect_b32 s25, 0, s25\n" \
  "s_waitcnt vmcnt(26)\n" \
  "ds_read_b64_tr_b16 a[144:145], v150 offset:6144\n" \
  "ds_read_b64_tr_b16 a[146:147], v150 offset:6656\n" \
  "ds_read_b64_tr_b16 a[148:149], v150 offset:6152\n" \
  "ds_read_b64_tr_b16 a[150:151], v150 offset:6664\n" \
  "s_waitcnt vmcnt(24)\n" \
  "ds_read_b64_tr_b16 a[152:153], v150 offset:8192\n" \
  "ds_read_b64_tr_b16 a[154:155], v150 offset:8704\n" \
  "ds_read_b64_tr_b16 a[156:157], v150 offset:8200\n" \
  "ds_read_b64_tr_b16 a[158:159], v150 offset:8712\n" \
  "s_waitcnt vmcnt(22)\n" \
  "ds_read_b64_tr_b16 a[160:161], v150 offset:10240\n" \
  "ds_read_b64_tr_b16 a[162:163], v150 offset:10752\n" \
  "ds_read_b64_tr_b16 a[164:165], v150 offset:10248\n" \
  "ds_read_b64_tr_b16 a[166:167], v150 offset:10760\n" \
  "s_waitcnt vmcnt(20)\n" \
  "ds_read_b64_tr_b16 a[168:169], v150 offset:12288\n" \
  "ds_read_b64_tr_b16 a[170:171], v150 offset:12800\n" \
  "ds_read_b64_tr_b16 a[172:173], v150 offset:12296\n" \
  "ds_read_b64_tr_b16 a[174:175], v150 offset:12808\n" \
  "s_waitcnt vmcnt(18)\n" \
  "ds_read_b64_tr_b16 a[176:177], v150 offset:14336\n" \
  "ds_read_b64_tr_b16 a[178:179], v150 offset:14848\n" \
  "ds_read_b64_tr_b16 a[180:181], v150 offset:14344\n" \
  "ds_read_b64_tr_b16 a[182:183], v150 offset:14856\n" \
  "s_waitcnt vmcnt(16)\n" \
  "ds_read_b64_tr_b16 a[184:185], v150 offset:16384\n" \
  "ds_read_b64_tr_b16 a[186:187], v150 offset:16896\n" \
  "ds_read_b64_tr_b16 a[188:189], v150 offset:16392\n" \
  "ds_read_b64_tr_b16 a[190:191], v150 offset:16904\n" \
  "s_waitcnt vmcnt(14)\n" \
  "ds_read_b64_tr_b16 a[192:193], v150 offset:18432\n" \
  "ds_read_b64_tr_b16 a[194:195], v150 offset:18944\n" \
  "ds_read_b64_tr_b16 a[196:197], v150 offset:18440\n" \
  "ds_read_b64_tr_b16 a[198:199], v150 offset:18952\n" \
  "s_waitcnt vmcnt(12)\n" \
  "ds_read_b64_tr_b16 a[200:201], v150 offset:20480\n" \
  "ds_read_b64_tr_b16 a[202:203], v150 offset:20992\n" \
  "ds_read_b64_tr_b16 a[204:205], v150 offset:20488\n" \
  "ds_read_b64_tr_b16 a[206:207], v150 offset:21000\n" \
  "s_waitcnt vmcnt(10)\n" \
  "ds_read_b64_tr_b16 a[208:209], v150 offset:22528\n" \
  "ds_read_b64_tr_b16 a[210:211], v150 offset:23040\n" \
  "ds_read_b64_tr_b16 a[212:213], v150 offset:22536\n" \
  "ds_read_b64_tr_b16 a[214:215], v150 offset:23048\n" \
  "s_waitcnt vmcnt(8)\n" \
  "ds_read_b64_tr_b16 a[216:217], v150 offset:24576\n" \
  "ds_read_b64_tr_b16 a[218:219], v150 offset:25088\n" \
  "ds_read_b64_tr_b16 a[220:221], v150 offset:24584\n" \
  "ds_read_b64_tr_b16 a[222:223], v150 offset:25096\n" \
  "s_waitcnt vmcnt(6)\n" \
  "ds_read_b64_tr_b16 a[224:225], v150 offset:26624\n" \
  "ds_read_b64_tr_b16 a[226:227], v150 offset:27136\n" \
  "ds_read_b64_tr_b16 a[228:229], v150 offset:26632\n" \
  "ds_read_b64_tr_b16 a[230:231], v150 offset:27144\n" \
  "s_waitcnt vmcnt(4)\n" \
  "ds_read_b64_tr_b16 a[232:233], v150 offset:28672\n" \
  "ds_read_b64_tr_b16 a[234:235], v150 offset:29184\n" \
  "ds_read_b64_tr_b16 a[236:237], v150 offset:28680\n" \
  "ds_read_b64_tr_b16 a[238:239], v150 offset:29192\n" \
  "s_waitcnt vmcnt(2)\n" \
  "ds_read_b64_tr_b16 a[240:241], v150\n" \
  "ds_read_b64_tr_b16 a[242:243], v150 offset:512\n" \
  "ds_read_b64_tr_b16 a[244:245], v150 offset:8\n" \
  "ds_read_b64_tr_b16 a[246:247], v150 offset:520\n" \
  "s_waitcnt vmcnt(0)\n" \
  "ds_read_b64_tr_b16 a[248:249], v150 offset:2048\n" \
  "ds_read_b64_tr_b16 a[250:251], v150 offset:2560\n" \
  "ds_read_b64_tr_b16 a[252:253], v150 offset:2056\n" \
  "ds_read_b64_tr_b16 a[254:255], v150 offset:2568\n" \
  "s_waitcnt lgkmcnt(0)\n" \
  "s_mov_b32 s35, 647296\n" \
  "ZG_%=:\n" \
  "buffer_load_dword v0, off, s[8:11], s35 sc1\n" \
  "s_waitcnt vmcnt(0)\n" \
  "v_cmp_ne_u32 vcc, s37, v0\n" \
  "s_cbranch_vccz ZGG_%=\n" \
  "s_sleep 4\n" \
  "s_sub_u32 s34, s34, 1\n" \
  "s_cbranch_scc0 ZG_%=\n" \
  "s_mov_b32 s34, 0\n" \
  "ZGG_%=:\n" \
  "s_barrier\n" \
  "ZA_%=:\n" \
  "buffer_load_dword v152, v141, s[8:11], s31 offen sc1\n" \
  "s_waitcnt vmcnt(0)\n" \
  "v_cmp_gt_i32 vcc, s27, v152\n" \
  "s_cbranch_vccz ZAG_%=\n" \
  "s_sleep 4\n" \
  "s_sub_u32 s34, s34, 1\n" \
  "s_cbranch_scc0 ZA_%=\n" \
  "s_mov_b32 s34, 0\n" \
  "ZAG_%=:\n" \
  "v_xor_b32 v141, v142, v141\n" \
  "s_add_u32 s28, s28, 262144\n" \
  "s_add_u32 s35, s28, s44\n" \
  "buffer_load_dwordx4 v[0:3], v140, s[8:11], s35 offen sc1\n" \
  "buffer_load_dwordx4 v[4:7], v140, s[8:11], s35 offen offset:1024 sc1\n" \
  "buffer_load_dwordx4 v[8:11], v140, s[8:11], s35 offen offset:2048 sc1\n" \
  "buffer_load_dwordx4 v[12:15], v140, s[8:11], s35 offen offset:3072 sc1\n" \
  "s_add_u32 s35, s28, s45\n" \
  "buffer_load_dwordx4 v[16:19], v140, s[8:11], s35 offen sc1\n" \
  "buffer_load_dwordx4 v[20:23], v140, s[8:11], s35 offen offset:1024 sc1\n" \
  "buffer_load_dwordx4 v[24:27], v140, s[8:11], s35 offen offset:2048 sc1\n" \
  "buffer_load_dwordx4 v[28:31], v140, s[8:11], s35 offen offset:3072 sc1\n" \
  "s_add_u32 s35, s28, s46\n" \
  "buffer_load_dwordx4 v[32:35], v140, s[8:11], s35 offen sc1\n" \
  "buffer_load_dwordx4 v[36:39], v140, s[8:11], s35 offen offset:1024 sc1\n" \
  "buffer_load_dwordx4 v[40:43], v140, s[8:11], s35 offen offset:2048 sc1\n" \
  "buffer_load_dwordx4 v[44:47], v140, s[8:11], s35 offen offset:3072 sc1\n" \
  "s_add_u32 s35, s28, s47\n" \
  "buffer_load_dwordx4 v[48:51], v140, s[8:11], s35 offen sc1\n" \
  "buffer_load_dwordx4 v[52:55], v140, s[8:11], s35 offen offset:1024 sc1\n" \
  "buffer_load_dwordx4 v[56:59], v140, s[8:11], s35 offen offset:2048 sc1\n" \
  "buffer_load_dwordx4 v[60:63], v140, s[8:11], s35 offen offset:3072 sc1\n" \
  "ZL_%=:\n" \
  "s_waitcnt vmcnt(15)\n" \
  "v_mfma_f32_16x16x32_bf16 v[128:131], a[0:3], v[0:3], 0\n" \
  "v_mfma_f32_16x16x32_bf16 v[132:135], a[4:7], v[0:3], 0\n" \
  "buffer_load_dword v154, v146, s[8:11], s30 offen sc1\n" \
  "buffer_load_dword v153, v143, s[8:11], s29 offen sc1\n" \
  "s_add_u32 s35, s28, s48\n" \
  "buffer_load_dwordx4 v[64:67], v140, s[8:11], s35 offen sc1\n" \
  "s_waitcnt vmcnt(17)\n" \
  "v_mfma_f32_16x16x32_bf16 v[128:131], a[8:11], v[4:7], v[128:131]\n" \
  "v_mfma_f32_16x16x32_bf16 v[132:135], a[12:15], v[4:7], v[132:135]\n" \
  "buffer_load_dword v152, v141, s[8:11], s31 offen sc1\n" \
  "buffer_load_dwordx4 v[68:71], v140, s[8:11], s35 offen offset:1024 sc1\n" \
  "s_waitcnt vmcnt(18)\n" \
  "v_mfma_f32_16x16x32_bf16 v[128:131], a[16:19], v[8:11], v[128:131]\n" \
  "v_mfma_f32_16x16x32_bf16 v[132:135], a[20:23], v[8:11], v[132:135]\n" \
  "buffer_load_dwordx4 v[72:75], v140, s[8:11], s35 offen offset:2048 sc1\n" \
  "s_waitcnt vmcnt(18)\n" \
  "v_mfma_f32_16x16x32_bf16 v[128:131], a[24:27], v[12:15], v[128:131]\n" \
  "v_mfma_f32_16x16x32_bf16 v[132:135], a[28:31], v[12:15], v[132:135]\n" \
  "buffer_load_dwordx4 v[76:79], v140, s[8:11], s35 offen offset:3072 sc1\n" \
  "s_waitcnt vmcnt(18)\n" \
  "v_mfma_f32_16x16x32_bf16 v[128:131], a[32:35], v[16:19], v[128:131]\n" \
  "v_mfma_f32_16x16x32_bf16 v[132:135], a[36:39], v[16:19], v[132:135]\n" \
  "s_add_u32 s35, s28, s49\n" \
  "buffer_load_dwordx4 v[80:83], v140, s[8:11], s35 offen sc1\n" \
  "s_waitcnt vmcnt(18)\n" \
  "v_mfma_f32_16x16x32_bf16 v[128:131], a[40:43], v[20:23], v[128:131]\n" \
  "v_mfma_f32_16x16x32_bf16 v[132:135], a[44:47], v[20:23], v[132:135]\n" \
  "buffer_load_dwordx4 v[84:87], v140, s[8:11], s35 offen offset:1024 sc1\n" \
  "s_waitcnt vmcnt(18)\n" \
  "v_mfma_f32_16x16x32_bf16 v[128:131], a[48:51], v[24:27], v[128:131]\n" \
  "v_mfma_f32_16x16x32_bf16 v[132:135], a[52:55], v[24:27], v[132:135]\n" \
  "buffer_load_dwordx4 v[88:91], v140, s[8:11], s35 offen offset:2048 sc1\n" \
  "s_waitcnt vmcnt(18)\n" \
  "v_mfma_f32_16x16x32_bf16 v[128:131], a[56:59], v[28:31], v[128:131]\n" \
  "v_mfma_f32_16x16x32_bf16 v[132:135], a[60:63], v[28:31], v[132:135]\n" \
  "buffer_load_dwordx4 v[92:95], v140, s[8:11], s35 offen offset:3072 sc1\n" \
  "s_waitcnt vmcnt(18)\n" \
  "v_mfma_f32_16x16x32_bf16 v[128:131], a[64:67], v[32:35], v[128:131]\n" \
  "v_mfma_f32_16x16x32_bf16 v[132:135], a[68:71], v[32:35], v[132:135]\n" \
  "s_add_u32 s35, s28, s50\n" \
  "buffer_load_dwordx4 v[96:99], v140, s[8:11], s35 offen sc1\n" \
  "s_waitcnt vmcnt(18)\n" \
  "v_mfma_f32_16x16x32_bf16 v[128:131], a[72:75], v[36:39], v[128:131]\n" \
  "v_mfma_f32_16x16x32_bf16 v[132:135], a[76:79], v[36:39], v[132:135]\n" \
  "buffer_load_dwordx4 v[100:103], v140, s[8:11], s35 offen offset:1024 sc1\n" \
  "s_waitcnt vmcnt(18)\n" \
  "v_mfma_f32_16x16x32_bf16 v[128:131], a[80:83], v[40:43], v[128:131]\n" \
  "v_mfma_f32_16x16x32_bf16 v[132:135], a[84:87], v[40:43], v[132:135]\n" \
  "buffer_load_dwordx4 v[104:107], v140, s[8:11], s35 offen offset:2048 sc1\n" \
  "s_waitcnt vmcnt(18)\n" \
  "v_mfma_f32_16x16x32_bf16 v[128:131], a[88:91], v[44:47], v[128:131]\n" \
  "v_mfma_f32_16x16x32_bf16 v[132:135], a[92:95], v[44:47], v[132:135]\n" \
  "buffer_load_dwordx4 v[108:111], v140, s[8:11], s35 offen offset:3072 sc1\n" \
  "s_waitcnt vmcnt(18)\n" \
  "v_mfma_f32_16x16x32_bf16 v[128:131], a[96:99], v[48:51], v[128:131]\n" \
  "v_mfma_f32_16x16x32_bf16 v[132:135], a[100:103], v[48:51], v[132:135]\n" \
  "s_add_u32 s35, s28, s51\n" \
  "buffer_load_dwordx4 v[112:115], v140, s[8:11], s35 offen sc1\n" \
  "s_waitcnt vmcnt(18)\n" \
  "v_mfma_f32_16x16x32_bf16 v[128:131], a[104:107], v[52:55], v[128:131]\n" \
  "v_mfma_f32_16x16x32_bf16 v[132:135], a[108:111], v[52:55], v[132:135]\n" \
  "buffer_load_dwordx4 v[116:119], v140, s[8:11], s35 offen offset:1024 sc1\n" \
  "s_waitcnt vmcnt(18)\n" \
  "v_mfma_f32_16x16x32_bf16 v[128:131], a[112:115], v[56:59], v[128:131]\n" \
  "v_mfma_f32_16x16x32_bf16 v[132:135], a[116:119], v[56:59], v[132:135]\n" \
  "buffer_load_dwordx4 v[120:123], v140, s[8:11], s35 offen offset:2048 sc1\n" \
  "s_waitcnt vmcnt(18)\n" \
  "v_mfma_f32_16x16x32_bf16 v[128:131], a[120:123], v[60:63], v[128:131]\n" \
  "v_mfma_f32_16x16x32_bf16 v[132:135], a[124:127], v[60:63], v[132:135]\n" \
  "buffer_load_dwordx4 v[124:127], v140, s[8:11], s35 offen offset:3072 sc1\n" \
  "s_waitcnt vmcnt(16)\n" \
  "v_mfma_f32_16x16x32_bf16 v[128:131], a[128:131], v[64:67], v[128:131]\n" \
  "v_mfma_f32_16x16x32_bf16 v[132:135], a[132:135], v[64:67], v[132:135]\n" \
  "s_waitcnt vmcnt(15)\n" \
  "v_cmp_ge_i32 vcc, s27, v152\n" \
  "s_nop 3\n" \
  "s_cmp_lg_u64 vcc, 0\n" \
  "s_cselect_b32 s32, 0x80000000, 0\n" \
  "s_cmp_eq_u32 s27, 63\n" \
  "s_cselect_b32 s32, 0x80000000, s32\n" \
  "v_or_b32 v151, s32, v140\n" \
  "v_mov_b32 v155, v141\n" \
  "s_cmp_lg_u32 s32, 0\n" \
  "s_cbranch_scc1 ZC_%=\n" \
  "v_xor_b32 v141, v142, v141\n" \
  "s_add_u32 s28, s28, 262144\n" \
  "v_mov_b32 v155, 0x80000000\n" \
  "ZC_%=:\n" \
  "s_add_u32 s35, s28, s44\n" \
  "buffer_load_dwordx4 v[0:3], v151, s[8:11], s35 offen sc1\n" \
  "s_waitcnt vmcnt(15)\n" \
  "v_mfma_f32_16x16x32_bf16 v[128:131], a[136:139], v[68:71], v[128:131]\n" \
  "v_mfma_f32_16x16x32_bf16 v[132:135], a[140:143], v[68:71], v[132:135]\n" \
  "buffer_load_dwordx4 v[4:7], v151, s[8:11], s35 offen offset:1024 sc1\n" \
  "s_waitcnt vmcnt(15)\n" \
  "v_mfma_f32_16x16x32_bf16 v[128:131], a[144:147], v[72:75], v[128:131]\n" \
  "v_mfma_f32_16x16x32_bf16 v[132:135], a[148:151], v[72:75], v[132:135]\n" \
  "buffer_load_dwordx4 v[8:11], v151, s[8:11], s35 offen offset:2048 sc1\n" \
  "s_waitcnt vmcnt(15)\n" \
  "v_mfma_f32_16x16x32_bf16 v[128:131], a[152:155], v[76:79], v[128:131]\n" \
  "v_mfma_f32_16x16x32_bf16 v[132:135], a[156:159], v[76:79], v[132:135]\n" \
  "buffer_load_dwordx4 v[12:15], v151, s[8:11], s35 offen offset:3072 sc1\n" \
  "s_waitcnt vmcnt(15)\n" \
  "v_mfma_f32_16x16x32_bf16 v[128:131], a[160:163], v[80:83], v[128:131]\n" \
  "v_mfma_f32_16x16x32_bf16 v[132:135], a[164:167], v[80:83], v[132:135]\n" \
  "s_add_u32 s35, s28, s45\n" \
  "buffer_load_dwordx4 v[16:19], v151, s[8:11], s35 offen sc1\n" \
  "s_waitcnt vmcnt(15)\n" \
  "v_mfma_f32_16x16x32_bf16 v[128:131], a[168:171], v[84:87], v[128:131]\n" \
  "v_mfma_f32_16x16x32_bf16 v[132:135], a[172:175], v[84:87], v[132:135]\n" \
  "buffer_load_dwordx4 v[20:23], v151, s[8:11], s35 offen offset:1024 sc1\n" \
  "s_waitcnt vmcnt(15)\n" \
  "v_mfma_f32_16x16x32_bf16 v[128:131], a[176:179], v[88:91], v[128:131]\n" \
  "v_mfma_f32_16x16x32_bf16 v[132:135], a[180:183], v[88:91], v[132:135]\n" \
  "buffer_load_dwordx4 v[24:27], v151, s[8:11], s35 offen offset:2048 sc1\n" \
  "s_waitcnt vmcnt(15)\n" \
  "v_mfma_f32_16x16x32_bf16 v[128:131], a[184:187], v[92:95], v[128:131]\n" \
  "v_mfma_f32_16x16x32_bf16 v[132:135], a[188:191], v[92:95], v[132:135]\n" \
  "buffer_load_dwordx4 v[28:31], v151, s[8:11], s35 offen offset:3072 sc1\n" \
  "s_waitcnt vmcnt(15)\n" \
  "v_mfma_f32_16x16x32_bf16 v[128:131], a[192:195], v[96:99], v[128:131]\n" \
  "v_mfma_f32_16x16x32_bf16 v[132:135], a[196:199], v[96:99], v[132:135]\n" \
  "buffer_load_dword v152, v155, s[8:11], s31 offen sc1\n" \
  "s_add_u32 s35, s28, s46\n" \
  "buffer_load_dwordx4 v[32:35], v151, s[8:11], s35 offen sc1\n" \
  "s_waitcnt vmcnt(16)\n" \
  "v_mfma_f32_16x16x32_bf16 v[128:131], a[200:203], v[100:103], v[128:131]\n" \
  "v_mfma_f32_16x16x32_bf16 v[132:135], a[204:207], v[100:103], v[132:135]\n" \
  "buffer_load_dwordx4 v[36:39], v151, s[8:11], s35 offen offset:1024 sc1\n" \
  "s_waitcnt vmcnt(16)\n" \
  "v_mfma_f32_16x16x32_bf16 v[128:131], a[208:211], v[104:107], v[128:131]\n" \
  "v_mfma_f32_16x16x32_bf16 v[132:135], a[212:215], v[104:107], v[132:135]\n" \
  "buffer_load_dwordx4 v[40:43], v151, s[8:11], s35 offen offset:2048 sc1\n" \
  "s_waitcnt vmcnt(16)\n" \
  "v_mfma_f32_16x16x32_bf16 v[128:131], a[216:219], v[108:111], v[128:131]\n" \
  "v_mfma_f32_16x16x32_bf16 v[132:135], a[220:223], v[108:111], v[132:135]\n" \
  "buffer_load_dwordx4 v[44:47], v151, s[8:11], s35 offen offset:3072 sc1\n" \
  "s_waitcnt vmcnt(16)\n" \
  "v_mfma_f32_16x16x32_bf16 v[128:131], a[224:227], v[112:115], v[128:131]\n" \
  "v_mfma_f32_16x16x32_bf16 v[132:135], a[228:231], v[112:115], v[132:135]\n" \
  "s_add_u32 s35, s28, s47\n" \
  "buffer_load_dwordx4 v[48:51], v151, s[8:11], s35 offen sc1\n" \
  "s_waitcnt vmcnt(16)\n" \
  "v_mfma_f32_16x16x32_bf16 v[128:131], a[232:235], v[116:119], v[128:131]\n" \
  "v_mfma_f32_16x16x32_bf16 v[132:135], a[236:239], v[116:119], v[132:135]\n" \
  "buffer_load_dwordx4 v[52:55], v151, s[8:11], s35 offen offset:1024 sc1\n" \
  "s_waitcnt vmcnt(16)\n" \
  "v_mfma_f32_16x16x32_bf16 v[128:131], a[240:243], v[120:123], v[128:131]\n" \
  "v_mfma_f32_16x16x32_bf16 v[132:135], a[244:247], v[120:123], v[132:135]\n" \
  "buffer_load_dwordx4 v[56:59], v151, s[8:11], s35 offen offset:2048 sc1\n" \
  "s_waitcnt vmcnt(16)\n" \
  "v_mfma_f32_16x16x32_bf16 v[128:131], a[248:251], v[124:127], v[128:131]\n" \
  "v_mfma_f32_16x16x32_bf16 v[132:135], a[252:255], v[124:127], v[132:135]\n" \
  "buffer_load_dwordx4 v[60:63], v151, s[8:11], s35 offen offset:3072 sc1\n" \
  "s_nop 2\n" \
  "ds_write_b128 v144, v[128:131]\n" \
  "s_nop 2\n" \
  "ds_write_b128 v144, v[132:135] offset:1024\n" \
  "v_xor_b32 v144, 0x2000, v144\n" \
  "s_cmp_eq_u32 s32, 0\n" \
  "s_cbranch_scc1 ZE_%=\n" \
  "s_cmp_eq_u32 s27, 63\n" \
  "s_cbranch_scc1 ZE_%=\n" \
  "s_waitcnt vmcnt(8)\n" \
  "v_cmp_ge_i32 vcc, s27, v152\n" \
  "s_cbranch_vccnz ZB_%=\n" \
  "v_xor_b32 v141, v142, v141\n" \
  "s_add_u32 s28, s28, 262144\n" \
  "s_add_u32 s35, s28, s44\n" \
  "buffer_load_dwordx4 v[0:3], v140, s[8:11], s35 offen sc1\n" \
  "buffer_load_dwordx4 v[4:7], v140, s[8:11], s35 offen offset:1024 sc1\n" \
  "buffer_load_dwordx4 v[8:11], v140, s[8:11], s35 offen offset:2048 sc1\n" \
  "buffer_load_dwordx4 v[12:15], v140, s[8:11], s35 offen offset:3072 sc1\n" \
  "s_add_u32 s35, s28, s45\n" \
  "buffer_load_dwordx4 v[16:19], v140, s[8:11], s35 offen sc1\n" \
  "buffer_load_dwordx4 v[20:23], v140, s[8:11], s35 offen offset:1024 sc1\n" \
  "buffer_load_dwordx4 v[24:27], v140, s[8:11], s35 offen offset:2048 sc1\n" \
  "buffer_load_dwordx4 v[28:31], v140, s[8:11], s35 offen offset:3072 sc1\n" \
  "s_add_u32 s35, s28, s46\n" \
  "buffer_load_dwordx4 v[32:35], v140, s[8:11], s35 offen sc1\n" \
  "buffer_load_dwordx4 v[36:39], v140, s[8:11], s35 offen offset:1024 sc1\n" \
  "buffer_load_dwordx4 v[40:43], v140, s[8:11], s35 offen offset:2048 sc1\n" \
  "buffer_load_dwordx4 v[44:47], v140, s[8:11], s35 offen offset:3072 sc1\n" \
  "s_add_u32 s35, s28, s47\n" \
  "buffer_load_dwordx4 v[48:51], v140, s[8:11], s35 offen sc1\n" \
  "buffer_load_dwordx4 v[52:55], v140, s[8:11], s35 offen offset:1024 sc1\n" \
  "buffer_load_dwordx4 v[56:59], v140, s[8:11], s35 offen offset:2048 sc1\n" \
  "buffer_load_dwordx4 v[60:63], v140, s[8:11], s35 offen offset:3072 sc1\n" \
  "ZE_%=:\n" \
  "s_waitcnt lgkmcnt(0)\n" \
  "s_barrier\n" \
  "ds_read_b64 v[160:161], v145\n" \
  "ds_read_b64 v[162:163], v145 offset:2048\n" \
  "ds_read_b64 v[164:165], v145 offset:4096\n" \
  "ds_read_b64 v[166:167], v145 offset:6144\n" \
  "v_lshlrev_b32 v170, 16, v154\n" \
  "v_and_b32 v171, 0xffff0000, v154\n" \
  "v_xor_b32 v145, 0x2000, v145\n" \
  "s_add_u32 s30, s30, 40960\n" \
  "s_add_u32 s29, s29, 128\n" \
  "s_waitcnt lgkmcnt(2)\n" \
  "v_add_f32 v160, v160, v162\n" \
  "v_add_f32 v161, v161, v163\n" \
  "s_waitcnt lgkmcnt(0)\n" \
  "v_add_f32 v164, v164, v166\n" \
  "v_add_f32 v165, v165, v167\n" \
  "v_add_f32 v160, v160, v164\n" \
  "v_add_f32 v161, v161, v165\n" \
  "v_cvt_pk_bf16_f32 v168, v160, v161\n" \
  "v_lshlrev_b32 v160, 16, v168\n" \
  "v_and_b32 v161, 0xffff0000, v168\n" \
  "v_mul_f32 v160, v160, v153\n" \
  "v_mul_f32 v161, v161, v153\n" \
  "v_cvt_pk_bf16_f32 v168, v160, v161\n" \
  "v_lshlrev_b32 v160, 16, v168\n" \
  "v_and_b32 v161, 0xffff0000, v168\n" \
  "v_add_f32 v160, v160, v170\n" \
  "v_add_f32 v161, v161, v171\n" \
  "v_cvt_pk_bf16_f32 v168, v160, v161\n" \
  "buffer_store_dword v168, v147, s[16:19], s33 offen nt\n" \
  "s_add_u32 s33, s33, 40960\n" \
  "ZM_%=:\n" \
  "s_cmp_eq_u32 s27, 1\n" \
  "s_cbranch_scc1 ZT_%=\n" \
  "ZU_%=:\n" \
  "s_add_u32 s27, s27, 1\n" \
  "s_cmp_lt_u32 s27, 64\n" \
  "s_cbranch_scc1 ZL_%=\n" \
  "s_waitcnt vmcnt(0)\n" \
  "s_branch ZX_%=\n" \
  "ZT_%=:\n" \
  "v_mov_b32 v172, s37\n" \
  "v_mbcnt_lo_u32_b32 v173, -1, 0\n" \
  "v_lshlrev_b32 v173, 2, v173\n" \
  "s_mov_b32 exec_hi, 0\n" \
  "buffer_store_dword v172, v173, s[8:11], s38 offen sc1\n" \
  "s_mov_b32 exec_hi, -1\n" \
  "s_branch ZU_%=\n" \
  "ZB_%=:\n" \
  "s_waitcnt lgkmcnt(0)\n" \
  "s_barrier\n" \
  "ds_read_b64 v[160:161], v145\n" \
  "ds_read_b64 v[162:163], v145 offset:2048\n" \
  "ds_read_b64 v[164:165], v145 offset:4096\n" \
  "ds_read_b64 v[166:167], v145 offset:6144\n" \
  "v_lshlrev_b32 v170, 16, v154\n" \
  "v_and_b32 v171, 0xffff0000, v154\n" \
  "v_xor_b32 v145, 0x2000, v145\n" \
  "s_add_u32 s30, s30, 40960\n" \
  "s_add_u32 s29, s29, 128\n" \
  "s_waitcnt lgkmcnt(2)\n" \
  "v_add_f32 v160, v160, v162\n" \
  "v_add_f32 v161, v161, v163\n" \
  "s_waitcnt lgkmcnt(0)\n" \
  "v_add_f32 v164, v164, v166\n" \
  "v_add_f32 v165, v165, v167\n" \
  "v_add_f32 v160, v160, v164\n" \
  "v_add_f32 v161, v161, v165\n" \
  "v_cvt_pk_bf16_f32 v168, v160, v161\n" \
  "v_lshlrev_b32 v160, 16, v168\n" \
  "v_and_b32 v161, 0xffff0000, v168\n" \
  "v_mul_f32 v160, v160, v153\n" \
  "v_mul_f32 v161, v161, v153\n" \
  "v_cvt_pk_bf16_f32 v168, v160, v161\n" \
  "v_lshlrev_b32 v160, 16, v168\n" \
  "v_and_b32 v161, 0xffff0000, v168\n" \
  "v_add_f32 v160, v160, v170\n" \
  "v_add_f32 v161, v161, v171\n" \
  "v_cvt_pk_bf16_f32 v168, v160, v161\n" \
  "buffer_store_dword v168, v147, s[16:19], s33 offen nt\n" \
  "s_add_u32 s33, s33, 40960\n" \
  "s_cmp_lt_u32 s27, 59\n" \
  "s_cbranch_scc1 ZP_%=\n" \
  "ZQ_%=:\n" \
  "buffer_load_dword v152, v141, s[8:11], s31 offen sc1\n" \
  "s_sleep 5\n" \
  "buffer_load_dword v180, v141, s[8:11], s31 offen sc1\n" \
  "s_sleep 5\n" \
  "buffer_load_dword v181, v141, s[8:11], s31 offen sc1\n" \
  "s_sleep 5\n" \
  "ZQL_%=:\n" \
  "s_waitcnt vmcnt(2)\n" \
  "v_cmp_ge_i32 vcc, s27, v152\n" \
  "s_cbranch_vccz ZPG_%=\n" \
  "buffer_load_dword v152, v141, s[8:11], s31 offen sc1\n" \
  "s_waitcnt vmcnt(2)\n" \
  "v_cmp_ge_i32 vcc, s27, v180\n" \
  "s_cbranch_vccz ZPG_%=\n" \
  "buffer_load_dword v180, v141, s[8:11], s31 offen sc1\n" \
  "s_waitcnt vmcnt(2)\n" \
  "v_cmp_ge_i32 vcc, s27, v181\n" \
  "s_cbranch_vccz ZPG_%=\n" \
  "buffer_load_dword v181, v141, s[8:11], s31 offen sc1\n" \
  "s_sub_u32 s34, s34, 1\n" \
  "s_cbranch_scc0 ZQL_%=\n" \
  "s_mov_b32 s34, 0\n" \
  "s_branch ZPG_%=\n" \
  "ZP_%=:\n" \
  "buffer_load_dword v152, v141, s[8:11], s31 offen sc1\n" \
  "s_waitcnt vmcnt(0)\n" \
  "v_cmp_ge_i32 vcc, s27, v152\n" \
  "s_cbranch_vccz ZPG_%=\n" \
  "s_sleep 1\n" \
  "s_sub_u32 s34, s34, 1\n" \
  "s_cbranch_scc0 ZP_%=\n" \
  "s_mov_b32 s34, 0\n" \
  "ZPG_%=:\n" \
  "v_xor_b32 v141, v142, v141\n" \
  "s_add_u32 s28, s28, 262144\n" \
  "s_add_u32 s35, s28, s44\n" \
  "buffer_load_dwordx4 v[0:3], v140, s[8:11], s35 offen sc1\n" \
  "buffer_load_dwordx4 v[4:7], v140, s[8:11], s35 offen offset:1024 sc1\n" \
  "buffer_load_dwordx4 v[8:11], v140, s[8:11], s35 offen offset:2048 sc1\n" \
  "buffer_load_dwordx4 v[12:15], v140, s[8:11], s35 offen offset:3072 sc1\n" \
  "s_add_u32 s35, s28, s45\n" \
  "buffer_load_dwordx4 v[16:19], v140, s[8:11], s35 offen sc1\n" \
  "buffer_load_dwordx4 v[20:23], v140, s[8:11], s35 offen offset:1024 sc1\n" \
  "buffer_load_dwordx4 v[24:27], v140, s[8:11], s35 offen offset:2048 sc1\n" \
  "buffer_load_dwordx4 v[28:31], v140, s[8:11], s35 offen offset:3072 sc1\n" \
  "s_add_u32 s35, s28, s46\n" \
  "buffer_load_dwordx4 v[32:35], v140, s[8:11], s35 offen sc1\n" \
  "buffer_load_dwordx4 v[36:39], v140, s[8:11], s35 offen offset:1024 sc1\n" \
  "buffer_load_dwordx4 v[40:43], v140, s[8:11], s35 offen offset:2048 sc1\n" \
  "buffer_load_dwordx4 v[44:47], v140, s[8:11], s35 offen offset:3072 sc1\n" \
  "s_add_u32 s35, s28, s47\n" \
  "buffer_load_dwordx4 v[48:51], v140, s[8:11], s35 offen sc1\n" \
  "buffer_load_dwordx4 v[52:55], v140, s[8:11], s35 offen offset:1024 sc1\n" \
  "buffer_load_dwordx4 v[56:59], v140, s[8:11], s35 offen offset:2048 sc1\n" \
  "buffer_load_dwordx4 v[60:63], v140, s[8:11], s35 offen offset:3072 sc1\n" \
  "s_branch ZM_%=\n" \
  "ZX_%=:\n"

#define ASM_X \
  "v_cmp_le_i32 s[40:41], 0, v255\n" \
  "v_cmp_le_i32 s[42:43], 1, v255\n" \
  "v_cmp_le_i32 s[44:45], 2, v255\n" \
  "v_cmp_le_i32 s[46:47], 3, v255\n" \
  "v_cmp_le_i32 s[48:49], 8, v255\n" \
  "v_cmp_le_i32 s[50:51], 9, v255\n" \
  "v_cmp_le_i32 s[52:53], 10, v255\n" \
  "v_cmp_le_i32 s[54:55], 11, v255\n" \
  "v_cmp_le_i32 s[56:57], 16, v255\n" \
  "v_cmp_le_i32 s[58:59], 17, v255\n" \
  "v_cmp_le_i32 s[60:61], 18, v255\n" \
  "v_cmp_le_i32 s[62:63], 19, v255\n" \
  "v_cmp_le_i32 s[64:65], 24, v255\n" \
  "v_cmp_le_i32 s[66:67], 25, v255\n" \
  "v_cmp_le_i32 s[68:69], 26, v255\n" \
  "v_cmp_le_i32 s[70:71], 27, v255\n" \
  "v_mov_b32 v246, 0xceabdc98\n" \
  "XA_%=:\n" \
  "buffer_load_dword v247, v253, s[8:11], s37 offen sc1\n" \
  "s_waitcnt vmcnt(0)\n" \
  "v_cmp_gt_i32 vcc, s24, v247\n" \
  "s_cbranch_vccz XAG_%=\n" \
  "s_sleep 4\n" \
  "s_sub_u32 s33, s33, 1\n" \
  "s_cbranch_scc0 XA_%=\n" \
  "s_mov_b32 s33, 0\n" \
  "XAG_%=:\n" \
  "s_add_u32 s31, s24, 1\n" \
  "s_lshl_b32 s31, s31, 24\n" \
  "s_cmp_ge_u32 s24, 60\n" \
  "s_cselect_b32 s39, s79, s39\n" \
  "s_add_u32 s38, s27, 4096\n" \
  "buffer_load_dwordx4 v[32:35], v248, s[8:11], s27 offen sc1\n" \
  "buffer_load_dwordx4 v[36:39], v248, s[8:11], s27 offen offset:512 sc1\n" \
  "buffer_load_dwordx4 v[40:43], v248, s[8:11], s27 offen offset:2048 sc1\n" \
  "buffer_load_dwordx4 v[44:47], v248, s[8:11], s27 offen offset:2560 sc1\n" \
  "buffer_load_dwordx4 v[48:51], v248, s[8:11], s38 offen sc1\n" \
  "buffer_load_dwordx4 v[52:55], v248, s[8:11], s38 offen offset:512 sc1\n" \
  "buffer_load_dwordx4 v[56:59], v248, s[8:11], s38 offen offset:2048 sc1\n" \
  "buffer_load_dwordx4 v[60:63], v248, s[8:11], s38 offen offset:2560 sc1\n" \
  "s_add_u32 s38, s26, 4096\n" \
  "buffer_load_dwordx4 v[0:3], v248, s[8:11], s26 offen sc1\n" \
  "buffer_load_dwordx4 v[4:7], v248, s[8:11], s26 offen offset:512 sc1\n" \
  "buffer_load_dwordx4 v[8:11], v248, s[8:11], s26 offen offset:2048 sc1\n" \
  "buffer_load_dwordx4 v[12:15], v248, s[8:11], s26 offen offset:2560 sc1\n" \
  "buffer_load_dwordx4 v[16:19], v248, s[8:11], s38 offen sc1\n" \
  "buffer_load_dwordx4 v[20:23], v248, s[8:11], s38 offen offset:512 sc1\n" \
  "buffer_load_dwordx4 v[24:27], v248, s[8:11], s38 offen offset:2048 sc1\n" \
  "buffer_load_dwordx4 v[28:31], v248, s[8:11], s38 offen offset:2560 sc1\n" \
  "s_add_u32 s38, s28, 4096\n" \
  "buffer_load_dwordx4 v[64:67], v249, s[8:11], s28 offen sc1\n" \
  "buffer_load_dwordx4 v[68:71], v249, s[8:11], s28 offen offset:512 sc1\n" \
  "buffer_load_dwordx4 v[72:75], v249, s[8:11], s28 offen offset:2048 sc1\n" \
  "buffer_load_dwordx4 v[76:79], v249, s[8:11], s28 offen offset:2560 sc1\n" \
  "buffer_load_dwordx4 v[80:83], v249, s[8:11], s38 offen sc1\n" \
  "buffer_load_dwordx4 v[84:87], v249, s[8:11], s38 offen offset:512 sc1\n" \
  "buffer_load_dwordx4 v[88:91], v249, s[8:11], s38 offen offset:2048 sc1\n" \
  "buffer_load_dwordx4 v[92:95], v249, s[8:11], s38 offen offset:2560 sc1\n" \
  "buffer_load_dword v242, v254, s[8:11], s39 offen sc1\n" \
  "s_waitcnt vmcnt(0)\n" \
  "XL_%=:\n" \
  "v_cmp_gt_i32 vcc, s31, v242\n" \
  "s_cbranch_vccnz XN_%=\n" \
  "s_mov_b64 exec, s[76:77]\n" \
  "buffer_load_dwordx4 v[96:99], v250, s[8:11], s29 offen sc1\n" \
  "buffer_load_dwordx4 v[100:103], v250, s[8:11], s29 offen offset:256 sc1\n" \
  "buffer_load_dwordx4 v[104:107], v250, s[8:11], s29 offen offset:512 sc1\n" \
  "buffer_load_dwordx4 v[108:111], v250, s[8:11], s29 offen offset:768 sc1\n" \
  "buffer_load_dwordx4 v[112:115], v250, s[8:11], s29 offen offset:1024 sc1\n" \
  "buffer_load_dwordx4 v[116:119], v250, s[8:11], s29 offen offset:1280 sc1\n" \
  "buffer_load_dwordx4 v[120:123], v250, s[8:11], s29 offen offset:1536 sc1\n" \
  "buffer_load_dwordx4 v[124:127], v250, s[8:11], s29 offen offset:1792 sc1\n" \
  "s_mov_b64 exec, -1\n" \
  "buffer_load_dwordx2 v[128:129], v251, s[8:11], s29 offen sc1\n" \
  "buffer_load_dwordx2 v[130:131], v251, s[8:11], s29 offen offset:512 sc1\n" \
  "buffer_load_dwordx2 v[132:133], v251, s[8:11], s29 offen offset:1024 sc1\n" \
  "buffer_load_dwordx2 v[134:135], v251, s[8:11], s29 offen offset:1536 sc1\n" \
  "v_mfma_f32_32x32x16_bf16 v[136:151], v[32:35], v[0:3], 0\n" \
  "v_mfma_f32_32x32x16_bf16 v[136:151], v[36:39], v[4:7], v[136:151]\n" \
  "v_mfma_f32_32x32x16_bf16 v[136:151], v[40:43], v[8:11], v[136:151]\n" \
  "v_mfma_f32_32x32x16_bf16 v[136:151], v[44:47], v[12:15], v[136:151]\n" \
  "v_mfma_f32_32x32x16_bf16 v[136:151], v[48:51], v[16:19], v[136:151]\n" \
  "v_mfma_f32_32x32x16_bf16 v[136:151], v[52:55], v[20:23], v[136:151]\n" \
  "v_mfma_f32_32x32x16_bf16 v[136:151], v[56:59], v[24:27], v[136:151]\n" \
  "v_mfma_f32_32x32x16_bf16 v[136:151], v[60:63], v[28:31], v[136:151]\n" \
  "s_nop 7\n" \
  "s_nop 3\n" \
  "v_cndmask_b32 v136, v246, v136, s[40:41]\n" \
  "v_cndmask_b32 v137, v246, v137, s[42:43]\n" \
  "v_cndmask_b32 v138, v246, v138, s[44:45]\n" \
  "v_cndmask_b32 v139, v246, v139, s[46:47]\n" \
  "v_cndmask_b32 v140, v246, v140, s[48:49]\n" \
  "v_cndmask_b32 v141, v246, v141, s[50:51]\n" \
  "v_cndmask_b32 v142, v246, v142, s[52:53]\n" \
  "v_cndmask_b32 v143, v246, v143, s[54:55]\n" \
  "v_cndmask_b32 v144, v246, v144, s[56:57]\n" \
  "v_cndmask_b32 v145, v246, v145, s[58:59]\n" \
  "v_cndmask_b32 v146, v246, v146, s[60:61]\n" \
  "v_cndmask_b32 v147, v246, v147, s[62:63]\n" \
  "v_cndmask_b32 v148, v246, v148, s[64:65]\n" \
  "v_cndmask_b32 v149, v246, v149, s[66:67]\n" \
  "v_cndmask_b32 v150, v246, v150, s[68:69]\n" \
  "v_cndmask_b32 v151, v246, v151, s[70:71]\n" \
  "v_max_f32 v243, v136, v137\n" \
  "v_max3_f32 v243, v243, v138, v139\n" \
  "v_max3_f32 v243, v243, v140, v141\n" \
  "v_max3_f32 v243, v243, v142, v143\n" \
  "v_max3_f32 v243, v243, v144, v145\n" \
  "v_max3_f32 v243, v243, v146, v147\n" \
  "v_max3_f32 v243, v243, v148, v149\n" \
  "v_max3_f32 v243, v243, v150, v151\n" \
  "XC_%=:\n" \
  "s_add_u32 s26, s26, s34\n" \
  "s_add_u32 s27, s27, s34\n" \
  "s_add_u32 s28, s28, s34\n" \
  "s_add_u32 s24, s24, s25\n" \
  "s_cmp_lt_u32 s24, 64\n" \
  "s_cbranch_scc0 XE_%=\n" \
  "v_cmp_gt_i32 vcc, s24, v247\n" \
  "s_cbranch_vccnz XQ_%=\n" \
  "XQG_%=:\n" \
  "s_add_u32 s38, s27, 4096\n" \
  "buffer_load_dwordx4 v[32:35], v248, s[8:11], s27 offen sc1\n" \
  "buffer_load_dwordx4 v[36:39], v248, s[8:11], s27 offen offset:512 sc1\n" \
  "buffer_load_dwordx4 v[40:43], v248, s[8:11], s27 offen offset:2048 sc1\n" \
  "buffer_load_dwordx4 v[44:47], v248, s[8:11], s27 offen offset:2560 sc1\n" \
  "buffer_load_dwordx4 v[48:51], v248, s[8:11], s38 offen sc1\n" \
  "buffer_load_dwordx4 v[52:55], v248, s[8:11], s38 offen offset:512 sc1\n" \
  "buffer_load_dwordx4 v[56:59], v248, s[8:11], s38 offen offset:2048 sc1\n" \
  "buffer_load_dwordx4 v[60:63], v248, s[8:11], s38 offen offset:2560 sc1\n" \
  "s_waitcnt vmcnt(19)\n" \
  "v_mfma_f32_32x32x16_bf16 v[152:167], v[96:99], v[0:3], 0\n" \
  "s_waitcnt vmcnt(18)\n" \
  "v_mfma_f32_32x32x16_bf16 v[152:167], v[100:103], v[4:7], v[152:167]\n" \
  "s_waitcnt vmcnt(17)\n" \
  "v_mfma_f32_32x32x16_bf16 v[152:167], v[104:107], v[8:11], v[152:167]\n" \
  "s_waitcnt vmcnt(16)\n" \
  "v_mfma_f32_32x32x16_bf16 v[152:167], v[108:111], v[12:15], v[152:167]\n" \
  "s_waitcnt vmcnt(15)\n" \
  "v_mfma_f32_32x32x16_bf16 v[152:167], v[112:115], v[16:19], v[152:167]\n" \
  "s_waitcnt vmcnt(14)\n" \
  "v_mfma_f32_32x32x16_bf16 v[152:167], v[116:119], v[20:23], v[152:167]\n" \
  "s_waitcnt vmcnt(13)\n" \
  "v_mfma_f32_32x32x16_bf16 v[152:167], v[120:123], v[24:27], v[152:167]\n" \
  "s_waitcnt vmcnt(12)\n" \
  "v_mfma_f32_32x32x16_bf16 v[152:167], v[124:127], v[28:31], v[152:167]\n" \
  "s_add_u32 s38, s26, 4096\n" \
  "buffer_load_dwordx4 v[0:3], v248, s[8:11], s26 offen sc1\n" \
  "buffer_load_dwordx4 v[4:7], v248, s[8:11], s26 offen offset:512 sc1\n" \
  "buffer_load_dwordx4 v[8:11], v248, s[8:11], s26 offen offset:2048 sc1\n" \
  "buffer_load_dwordx4 v[12:15], v248, s[8:11], s26 offen offset:2560 sc1\n" \
  "buffer_load_dwordx4 v[16:19], v248, s[8:11], s38 offen sc1\n" \
  "buffer_load_dwordx4 v[20:23], v248, s[8:11], s38 offen offset:512 sc1\n" \
  "buffer_load_dwordx4 v[24:27], v248, s[8:11], s38 offen offset:2048 sc1\n" \
  "buffer_load_dwordx4 v[28:31], v248, s[8:11], s38 offen offset:2560 sc1\n" \
  "s_nop 2\n" \
  "v_max3_f32 v243, v243, v152, v153\n" \
  "v_max3_f32 v243, v243, v154, v155\n" \
  "v_mov_b32 v244, v243\n" \
  "s_nop 1\n" \
  "v_permlane32_swap_b32 v243, v244\n" \
  "v_max_f32 v243, v243, v244\n" \
  "v_sub_f32 v136, v136, v243\n" \
  "v_sub_f32 v137, v137, v243\n" \
  "v_sub_f32 v138, v138, v243\n" \
  "v_sub_f32 v139, v139, v243\n" \
  "v_sub_f32 v140, v140, v243\n" \
  "v_sub_f32 v141, v141, v243\n" \
  "v_sub_f32 v142, v142, v243\n" \
  "v_sub_f32 v143, v143, v243\n" \
  "v_sub_f32 v144, v144, v243\n" \
  "v_sub_f32 v145, v145, v243\n" \
  "v_sub_f32 v146, v146, v243\n" \
  "v_sub_f32 v147, v147, v243\n" \
  "v_sub_f32 v148, v148, v243\n" \
  "v_sub_f32 v149, v149, v243\n" \
  "v_sub_f32 v150, v150, v243\n" \
  "v_sub_f32 v151, v151, v243\n" \
  "v_sub_f32 v152, v152, v243\n" \
  "v_sub_f32 v153, v153, v243\n" \
  "v_sub_f32 v154, v154, v243\n" \
  "v_sub_f32 v155, v155, v243\n" \
  "v_exp_f32 v136, v136\n" \
  "v_exp_f32 v137, v137\n" \
  "v_exp_f32 v138, v138\n" \
  "v_exp_f32 v139, v139\n" \
  "v_exp_f32 v140, v140\n" \
  "v_exp_f32 v141, v141\n" \
  "v_exp_f32 v142, v142\n" \
  "v_exp_f32 v143, v143\n" \
  "v_exp_f32 v144, v144\n" \
  "v_exp_f32 v145, v145\n" \
  "v_exp_f32 v146, v146\n" \
  "v_exp_f32 v147, v147\n" \
  "v_exp_f32 v148, v148\n" \
  "v_exp_f32 v149, v149\n" \
  "v_exp_f32 v150, v150\n" \
  "v_exp_f32 v151, v151\n" \
  "v_exp_f32 v152, v152\n" \
  "v_exp_f32 v153, v153\n" \
  "v_exp_f32 v154, v154\n" \
  "v_exp_f32 v155, v155\n" \
  "v_add_f32 v245, v136, v137\n" \
  "v_add_f32 v242, v138, v139\n" \
  "v_add_f32 v245, v245, v140\n" \
  "v_add_f32 v242, v242, v141\n" \
  "v_add_f32 v245, v245, v142\n" \
  "v_add_f32 v242, v242, v143\n" \
  "v_add_f32 v245, v245, v144\n" \
  "v_add_f32 v242, v242, v145\n" \
  "v_add_f32 v245, v245, v146\n" \
  "v_add_f32 v242, v242, v147\n" \
  "v_add_f32 v245, v245, v148\n" \
  "v_add_f32 v242, v242, v149\n" \
  "v_add_f32 v245, v245, v150\n" \
  "v_add_f32 v242, v242, v151\n" \
  "v_add_f32 v245, v245, v152\n" \
  "v_add_f32 v242, v242, v153\n" \
  "v_add_f32 v245, v245, v154\n" \
  "v_add_f32 v242, v242, v155\n" \
  "v_add_f32 v245, v245, v242\n" \
  "v_mov_b32 v244, v245\n" \
  "s_nop 1\n" \
  "v_permlane32_swap_b32 v245, v244\n" \
  "v_add_f32 v245, v245, v244\n" \
  "v_rcp_f32 v245, v245\n" \
  "s_nop 0\n" \
  "v_mul_f32 v136, v136, v245\n" \
  "v_mul_f32 v137, v137, v245\n" \
  "v_mul_f32 v138, v138, v245\n" \
  "v_mul_f32 v139, v139, v245\n" \
  "v_mul_f32 v140, v140, v245\n" \
  "v_mul_f32 v141, v141, v245\n" \
  "v_mul_f32 v142, v142, v245\n" \
  "v_mul_f32 v143, v143, v245\n" \
  "v_mul_f32 v144, v144, v245\n" \
  "v_mul_f32 v145, v145, v245\n" \
  "v_mul_f32 v146, v146, v245\n" \
  "v_mul_f32 v147, v147, v245\n" \
  "v_mul_f32 v148, v148, v245\n" \
  "v_mul_f32 v149, v149, v245\n" \
  "v_mul_f32 v150, v150, v245\n" \
  "v_mul_f32 v151, v151, v245\n" \
  "v_mul_f32 v152, v152, v245\n" \
  "v_mul_f32 v153, v153, v245\n" \
  "v_mul_f32 v154, v154, v245\n" \
  "v_mul_f32 v155, v155, v245\n" \
  "v_cvt_pk_bf16_f32 v232, v136, v137\n" \
  "v_cvt_pk_bf16_f32 v233, v138, v139\n" \
  "v_cvt_pk_bf16_f32 v234, v140, v141\n" \
  "v_cvt_pk_bf16_f32 v235, v142, v143\n" \
  "v_cvt_pk_bf16_f32 v236, v144, v145\n" \
  "v_cvt_pk_bf16_f32 v237, v146, v147\n" \
  "v_cvt_pk_bf16_f32 v238, v148, v149\n" \
  "v_cvt_pk_bf16_f32 v239, v150, v151\n" \
  "v_cvt_pk_bf16_f32 v240, v152, v153\n" \
  "v_cvt_pk_bf16_f32 v241, v154, v155\n" \
  "v_mfma_f32_32x32x16_bf16 v[168:183], v[64:67], v[232:235], 0\n" \
  "v_mfma_f32_32x32x16_bf16 v[168:183], v[68:71], v[236:239], v[168:183]\n" \
  "s_waitcnt vmcnt(19)\n" \
  "s_nop 7\n" \
  "s_nop 0\n" \
  "v_mfma_f32_32x32x8_bf16 v[168:183], v[128:129], v[240:241], v[168:183]\n" \
  "v_mfma_f32_32x32x16_bf16 v[184:199], v[72:75], v[232:235], 0\n" \
  "v_mfma_f32_32x32x16_bf16 v[184:199], v[76:79], v[236:239], v[184:199]\n" \
  "v_cvt_pk_bf16_f32 v168, v168, v169\n" \
  "v_cvt_pk_bf16_f32 v169, v170, v171\n" \
  "v_cvt_pk_bf16_f32 v170, v172, v173\n" \
  "v_cvt_pk_bf16_f32 v171, v174, v175\n" \
  "buffer_store_dwordx4 v[168:171], v252, s[8:11], s30 offen sc1\n" \
  "s_waitcnt vmcnt(19)\n" \
  "s_nop 3\n" \
  "v_mfma_f32_32x32x8_bf16 v[184:199], v[130:131], v[240:241], v[184:199]\n" \
  "v_cvt_pk_bf16_f32 v172, v176, v177\n" \
  "v_cvt_pk_bf16_f32 v173, v178, v179\n" \
  "v_cvt_pk_bf16_f32 v174, v180, v181\n" \
  "v_cvt_pk_bf16_f32 v175, v182, v183\n" \
  "buffer_store_dwordx4 v[172:175], v252, s[8:11], s30 offen offset:512 sc1\n" \
  "v_mfma_f32_32x32x16_bf16 v[200:215], v[80:83], v[232:235], 0\n" \
  "v_mfma_f32_32x32x16_bf16 v[200:215], v[84:87], v[236:239], v[200:215]\n" \
  "v_cvt_pk_bf16_f32 v184, v184, v185\n" \
  "v_cvt_pk_bf16_f32 v185, v186, v187\n" \
  "v_cvt_pk_bf16_f32 v186, v188, v189\n" \
  "v_cvt_pk_bf16_f32 v187, v190, v191\n" \
  "buffer_store_dwordx4 v[184:187], v252, s[8:11], s30 offen offset:1024 sc1\n" \
  "s_waitcnt vmcnt(20)\n" \
  "s_nop 3\n" \
  "v_mfma_f32_32x32x8_bf16 v[200:215], v[132:133], v[240:241], v[200:215]\n" \
  "v_cvt_pk_bf16_f32 v188, v192, v193\n" \
  "v_cvt_pk_bf16_f32 v189, v194, v195\n" \
  "v_cvt_pk_bf16_f32 v190, v196, v197\n" \
  "v_cvt_pk_bf16_f32 v191, v198, v199\n" \
  "buffer_store_dwordx4 v[188:191], v252, s[8:11], s30 offen offset:1536 sc1\n" \
  "v_mfma_f32_32x32x16_bf16 v[216:231], v[88:91], v[232:235], 0\n" \
  "v_mfma_f32_32x32x16_bf16 v[216:231], v[92:95], v[236:239], v[216:231]\n" \
  "v_cvt_pk_bf16_f32 v200, v200, v201\n" \
  "v_cvt_pk_bf16_f32 v201, v202, v203\n" \
  "v_cvt_pk_bf16_f32 v202, v204, v205\n" \
  "v_cvt_pk_bf16_f32 v203, v206, v207\n" \
  "buffer_store_dwordx4 v[200:203], v252, s[8:11], s30 offen offset:2048 sc1\n" \
  "s_waitcnt vmcnt(21)\n" \
  "s_nop 3\n" \
  "v_mfma_f32_32x32x8_bf16 v[216:231], v[134:135], v[240:241], v[216:231]\n" \
  "v_cvt_pk_bf16_f32 v204, v208, v209\n" \
  "v_cvt_pk_bf16_f32 v205, v210, v211\n" \
  "v_cvt_pk_bf16_f32 v206, v212, v213\n" \
  "v_cvt_pk_bf16_f32 v207, v214, v215\n" \
  "buffer_store_dwordx4 v[204:207], v252, s[8:11], s30 offen offset:2560 sc1\n" \
  "s_nop 6\n" \
  "v_cvt_pk_bf16_f32 v216, v216, v217\n" \
  "v_cvt_pk_bf16_f32 v217, v218, v219\n" \
  "v_cvt_pk_bf16_f32 v218, v220, v221\n" \
  "v_cvt_pk_bf16_f32 v219, v222, v223\n" \
  "buffer_store_dwordx4 v[216:219], v252, s[8:11], s30 offen offset:3072 sc1\n" \
  "v_cvt_pk_bf16_f32 v220, v224, v225\n" \
  "v_cvt_pk_bf16_f32 v221, v226, v227\n" \
  "v_cvt_pk_bf16_f32 v222, v228, v229\n" \
  "v_cvt_pk_bf16_f32 v223, v230, v231\n" \
  "buffer_store_dwordx4 v[220:223], v252, s[8:11], s30 offen offset:3584 sc1\n" \
  "s_add_u32 s31, s24, 1\n" \
  "s_lshl_b32 s31, s31, 24\n" \
  "s_cmp_ge_u32 s24, 60\n" \
  "s_cselect_b32 s39, s79, s39\n" \
  "buffer_load_dword v242, v254, s[8:11], s39 offen sc1\n" \
  "s_add_u32 s38, s28, 4096\n" \
  "buffer_load_dwordx4 v[64:67], v249, s[8:11], s28 offen sc1\n" \
  "buffer_load_dwordx4 v[68:71], v249, s[8:11], s28 offen offset:512 sc1\n" \
  "buffer_load_dwordx4 v[72:75], v249, s[8:11], s28 offen offset:2048 sc1\n" \
  "buffer_load_dwordx4 v[76:79], v249, s[8:11], s28 offen offset:2560 sc1\n" \
  "buffer_load_dwordx4 v[80:83], v249, s[8:11], s38 offen sc1\n" \
  "buffer_load_dwordx4 v[84:87], v249, s[8:11], s38 offen offset:512 sc1\n" \
  "buffer_load_dwordx4 v[88:91], v249, s[8:11], s38 offen offset:2048 sc1\n" \
  "buffer_load_dwordx4 v[92:95], v249, s[8:11], s38 offen offset:2560 sc1\n" \
  "s_add_u32 s30, s30, s34\n" \
  "s_add_u32 s29, s29, s35\n" \
  "s_sub_u32 s38, s24, s25\n" \
  "v_mov_b32 v244, s38\n" \
  "v_mbcnt_lo_u32_b32 v245, -1, 0\n" \
  "v_lshlrev_b32 v245, 2, v245\n" \
  "s_waitcnt vmcnt(9)\n" \
  "s_mov_b32 exec_hi, 0\n" \
  "buffer_store_dword v244, v245, s[8:11], s32 offen sc1\n" \
  "s_mov_b32 exec_hi, -1\n" \
  "s_waitcnt vmcnt(9)\n" \
  "s_cmp_lt_u32 s24, 64\n" \
  "s_cbranch_scc1 XL_%=\n" \
  "s_waitcnt vmcnt(0)\n" \
  "s_branch XX_%=\n" \
  "XN_%=:\n" \
  "v_mfma_f32_32x32x16_bf16 v[136:151], v[32:35], v[0:3], 0\n" \
  "v_mfma_f32_32x32x16_bf16 v[136:151], v[36:39], v[4:7], v[136:151]\n" \
  "v_mfma_f32_32x32x16_bf16 v[136:151], v[40:43], v[8:11], v[136:151]\n" \
  "v_mfma_f32_32x32x16_bf16 v[136:151], v[44:47], v[12:15], v[136:151]\n" \
  "v_mfma_f32_32x32x16_bf16 v[136:151], v[48:51], v[16:19], v[136:151]\n" \
  "v_mfma_f32_32x32x16_bf16 v[136:151], v[52:55], v[20:23], v[136:151]\n" \
  "v_mfma_f32_32x32x16_bf16 v[136:151], v[56:59], v[24:27], v[136:151]\n" \
  "v_mfma_f32_32x32x16_bf16 v[136:151], v[60:63], v[28:31], v[136:151]\n" \
  "s_nop 7\n" \
  "s_nop 3\n" \
  "v_cndmask_b32 v136, v246, v136, s[40:41]\n" \
  "v_cndmask_b32 v137, v246, v137, s[42:43]\n" \
  "v_cndmask_b32 v138, v246, v138, s[44:45]\n" \
  "v_cndmask_b32 v139, v246, v139, s[46:47]\n" \
  "v_cndmask_b32 v140, v246, v140, s[48:49]\n" \
  "v_cndmask_b32 v141, v246, v141, s[50:51]\n" \
  "v_cndmask_b32 v142, v246, v142, s[52:53]\n" \
  "v_cndmask_b32 v143, v246, v143, s[54:55]\n" \
  "v_cndmask_b32 v144, v246, v144, s[56:57]\n" \
  "v_cndmask_b32 v145, v246, v145, s[58:59]\n" \
  "v_cndmask_b32 v146, v246, v146, s[60:61]\n" \
  "v_cndmask_b32 v147, v246, v147, s[62:63]\n" \
  "v_cndmask_b32 v148, v246, v148, s[64:65]\n" \
  "v_cndmask_b32 v149, v246, v149, s[66:67]\n" \
  "v_cndmask_b32 v150, v246, v150, s[68:69]\n" \
  "v_cndmask_b32 v151, v246, v151, s[70:71]\n" \
  "v_max_f32 v243, v136, v137\n" \
  "v_max3_f32 v243, v243, v138, v139\n" \
  "v_max3_f32 v243, v243, v140, v141\n" \
  "v_max3_f32 v243, v243, v142, v143\n" \
  "v_max3_f32 v243, v243, v144, v145\n" \
  "v_max3_f32 v243, v243, v146, v147\n" \
  "v_max3_f32 v243, v243, v148, v149\n" \
  "v_max3_f32 v243, v243, v150, v151\n" \
  "s_cmp_lt_u32 s24, 60\n" \
  "s_cbranch_scc1 XP_%=\n" \
  "XR_%=:\n" \
  "buffer_load_dword v242, v254, s[8:11], s39 offen sc1\n" \
  "s_sleep 5\n" \
  "buffer_load_dword v240, v254, s[8:11], s39 offen sc1\n" \
  "s_sleep 5\n" \
  "buffer_load_dword v241, v254, s[8:11], s39 offen sc1\n" \
  "s_sleep 5\n" \
  "XRL_%=:\n" \
  "s_waitcnt vmcnt(2)\n" \
  "v_cmp_gt_i32 vcc, s31, v242\n" \
  "s_cbranch_vccz XB_%=\n" \
  "buffer_load_dword v242, v254, s[8:11], s39 offen sc1\n" \
  "s_waitcnt vmcnt(2)\n" \
  "v_cmp_gt_i32 vcc, s31, v240\n" \
  "s_cbranch_vccz XB_%=\n" \
  "buffer_load_dword v240, v254, s[8:11], s39 offen sc1\n" \
  "s_waitcnt vmcnt(2)\n" \
  "v_cmp_gt_i32 vcc, s31, v241\n" \
  "s_cbranch_vccz XB_%=\n" \
  "buffer_load_dword v241, v254, s[8:11], s39 offen sc1\n" \
  "s_sub_u32 s33, s33, 1\n" \
  "s_cbranch_scc0 XRL_%=\n" \
  "s_mov_b32 s33, 0\n" \
  "s_branch XB_%=\n" \
  "XP_%=:\n" \
  "buffer_load_dword v242, v254, s[8:11], s39 offen sc1\n" \
  "s_waitcnt vmcnt(0)\n" \
  "v_cmp_gt_i32 vcc, s31, v242\n" \
  "s_cbranch_vccz XB_%=\n" \
  "s_sleep 1\n" \
  "s_sub_u32 s33, s33, 1\n" \
  "s_cbranch_scc0 XP_%=\n" \
  "s_mov_b32 s33, 0\n" \
  "XB_%=:\n" \
  "s_mov_b64 exec, s[76:77]\n" \
  "buffer_load_dwordx4 v[96:99], v250, s[8:11], s29 offen sc1\n" \
  "buffer_load_dwordx4 v[100:103], v250, s[8:11], s29 offen offset:256 sc1\n" \
  "buffer_load_dwordx4 v[104:107], v250, s[8:11], s29 offen offset:512 sc1\n" \
  "buffer_load_dwordx4 v[108:111], v250, s[8:11], s29 offen offset:768 sc1\n" \
  "buffer_load_dwordx4 v[112:115], v250, s[8:11], s29 offen offset:1024 sc1\n" \
  "buffer_load_dwordx4 v[116:119], v250, s[8:11], s29 offen offset:1280 sc1\n" \
  "buffer_load_dwordx4 v[120:123], v250, s[8:11], s29 offen offset:1536 sc1\n" \
  "buffer_load_dwordx4 v[124:127], v250, s[8:11], s29 offen offset:1792 sc1\n" \
  "s_mov_b64 exec, -1\n" \
  "buffer_load_dwordx2 v[128:129], v251, s[8:11], s29 offen sc1\n" \
  "buffer_load_dwordx2 v[130:131], v251, s[8:11], s29 offen offset:512 sc1\n" \
  "buffer_load_dwordx2 v[132:133], v251, s[8:11], s29 offen offset:1024 sc1\n" \
  "buffer_load_dwordx2 v[134:135], v251, s[8:11], s29 offen offset:1536 sc1\n" \
  "s_branch XC_%=\n" \
  "XE_%=:\n" \
  "v_mov_b32 v248, 0x80000000\n" \
  "v_mov_b32 v249, 0x80000000\n" \
  "s_branch XQG_%=\n" \
  "XQ_%=:\n" \
  "buffer_load_dword v247, v253, s[8:11], s37 offen sc1\n" \
  "s_waitcnt vmcnt(0)\n" \
  "v_cmp_gt_i32 vcc, s24, v247\n" \
  "s_cbranch_vccz XQG_%=\n" \
  "s_sleep 4\n" \
  "s_sub_u32 s33, s33, 1\n" \
  "s_cbranch_scc0 XQ_%=\n" \
  "s_mov_b32 s33, 0\n" \
  "s_branch XQG_%=\n" \
  "XX_%=:\n"

#define CLOB_A "a0", "a1", "a2", "a3", "a4", "a5", "a6", "a7", "a8", "a9", "a10", "a11", "a12", "a13", "a14", "a15", "a16", "a17", "a18", "a19", "a20", "a21", "a22", "a23", "a24", "a25", "a26", "a27", "a28", "a29", "a30", "a31", "a32", "a33", "a34", "a35", "a36", "a37", "a38", "a39", "a40", "a41", "a42", "a43", "a44", "a45", "a46", "a47", "a48", "a49", "a50", "a51", "a52", "a53", "a54", "a55", "a56", "a57", "a58", "a59", "a60", "a61", "a62", "a63", "a64", "a65", "a66", "a67", "a68", "a69", "a70", "a71", "a72", "a73", "a74", "a75", "a76", "a77", "a78", "a79", "a80", "a81", "a82", "a83", "a84", "a85", "a86", "a87", "a88", "a89", "a90", "a91", "a92", "a93", "a94", "a95", "a96", "a97", "a98", "a99", "a100", "a101", "a102", "a103", "a104", "a105", "a106", "a107", "a108", "a109", "a110", "a111", "a112", "a113", "a114", "a115", "a116", "a117", "a118", "a119", "a120", "a121", "a122", "a123", "a124", "a125", "a126", "a127", "a128", "a129", "a130", "a131", "a132", "a133", "a134", "a135", "a136", "a137", "a138", "a139", "a140", "a141", "a142", "a143", "a144", "a145", "a146", "a147", "a148", "a149", "a150", "a151", "a152", "a153", "a154", "a155", "a156", "a157", "a158", "a159", "a160", "a161", "a162", "a163", "a164", "a165", "a166", "a167", "a168", "a169", "a170", "a171", "a172", "a173", "a174", "a175", "a176", "a177", "a178", "a179", "a180", "a181", "a182", "a183", "a184", "a185", "a186", "a187", "a188", "a189", "a190", "a191", "a192", "a193", "a194", "a195", "a196", "a197", "a198", "a199", "a200", "a201", "a202", "a203", "a204", "a205", "a206", "a207", "a208", "a209", "a210", "a211", "a212", "a213", "a214", "a215", "a216", "a217", "a218", "a219", "a220", "a221", "a222", "a223", "a224", "a225", "a226", "a227", "a228", "a229", "a230", "a231", "a232", "a233", "a234", "a235", "a236", "a237", "a238", "a239", "a240", "a241", "a242", "a243", "a244", "a245", "a246", "a247", "a248", "a249", "a250", "a251", "a252", "a253", "a254", "a255"

#define CLOB_H "v0", "v1", "v2", "v3", "v4", "v5", "v6", "v7", "v8", "v9", "v10", "v11", "v12", "v13", "v14", "v15", "v16", "v17", "v18", "v19", "v20", "v21", "v22", "v23", "v24", "v25", "v26", "v27", "v28", "v29", "v30", "v31", "v32", "v33", "v34", "v35", "v36", "v37", "v38", "v39", "v40", "v41", "v42", "v43", "v44", "v45", "v46", "v47", "v48", "v49", "v50", "v51", "v52", "v53", "v54", "v55", "v56", "v57", "v58", "v59", "v60", "v61", "v62", "v63", "v64", "v65", "v66", "v67", "v68", "v69", "v70", "v71", "v72", "v73", "v74", "v75", "v76", "v77", "v78", "v79", "v80", "v81", "v82", "v83", "v120", "v121", "v122", "v123", "v124", "v125", "v126", "v127", "v128", "v129", "v130", "v131", "v132", "v133", "v134", "v135", "v136", "v137", "v138", "v139", "v140", "v141", "v142", "v143", "v144", "v145", "v146", "v147", "v148", "v149", "v150", "v151", "v152", "v153", "v154", "v155", "v156", "v157", "v158", "v159", "v160", "v161", "v162", "v163", "v164", "v165", "v166", "v167", "v168", "v169", "v170", "v171", "v172", "v173", "v174", "v175", "v176", "v177", "v178", "v179", "v180", "v181", "v182", "v183", "v184", "v185", "v186", "v187", "v188", "v189", "v190", "v191", "v192", "v193", "v194", "v195", "v196", "v197", "v198", "v199", "v200", "v201", "v202", "v203", "v204", "v205", "v206", "v207", "v208", "v209", "v210", "v211", "v212", "v213", "v214", "v215", "v216", "v217", "v218", "v219", "v220", "v221", "v222", "v223", "v224", "v225", "v226", "v227", "v228", "v229", "v230", "v231", "v232", "v233", "v234", "v235", "v236", "v237", "v238", "v239", "v240", "v241", "v242", "v243", "v244", "v245", "v246", "v247", "v248", "v249", "v250", "v251", "v252", "v253", "v254", "v255", "s64", "s65", "s66", "s67", "s68", "s69", "s70", "s71", "s72", "s73", "s74", "s75", "s76", "s77", "s78", "s79"

#define CLOB_Y "v0", "v1", "v2", "v3", "v4", "v5", "v6", "v7", "v8", "v9", "v10", "v11", "v12", "v13", "v14", "v15", "v16", "v17", "v18", "v19", "v20", "v21", "v22", "v23", "v24", "v25", "v26", "v27", "v28", "v29", "v30", "v31", "v32", "v33", "v34", "v35", "v36", "v37", "v38", "v39", "v40", "v41", "v42", "v43", "v44", "v45", "v46", "v47", "s60", "s61", "s62", "s63", "s64", "s65", "s66", "s67", "s68", "s69", "s70", "s71", "s72", "s73", "s74", "s75", "s76", "s77", "s78", "s79"

#define CLOB_G1 "v0", "v1", "v2", "v3", "v4", "v5", "v6", "v7", "v8", "v9", "v10", "v11", "v12", "v13", "v14", "v15", "v16", "v17", "v18", "v19", "v20", "v21", "v22", "v23", "v48", "v49", "v50", "v51", "v52", "v53", "v54", "v55", "v56", "v57", "v58", "v59", "v60", "v61", "v62", "v63", "v64", "v65", "v66", "v67", "v68", "v69", "v70", "v71", "v72", "v73", "v74", "v75", "v76", "v77", "v78", "v79", "v80", "v81", "v82", "v83", "v84", "v85", "v86", "v87", "v88", "v89", "v90", "v91", "v92", "v93", "v94", "v95", "v96", "v97", "v98", "v99", "v100", "v101", "v102", "v103", "v104", "v105", "v106", "v107", "v108", "v109", "v110", "v111", "v112", "v113", "v114", "v115", "v116", "v117", "v118", "v119", "v120", "v121", "v122", "v123", "v124", "v125", "v126", "v127", "v128", "v129", "v130", "v131", "v132", "v133", "v134", "v135", "v136", "v137", "v138", "v139", "v140", "v141", "v142", "v143", "v144", "v145", "v146", "v147", "v148", "v149", "v150", "v151", "v152", "v153", "v154", "v155", "v156", "v157", "v158", "v159", "v160", "v161", "v162", "v163", "v164", "v165", "v166", "v167", "v168", "v169", "v170", "v171", "v172", "v173", "v174", "v175", "v176", "v177", "v178", "v179", "v180", "v181", "v182", "v183", "v184", "v185", "v186", "v187", "v188", "v189", "v190", "v191", "v192", "v193", "v194", "v195", "v196", "v197", "v198", "v199", "v200", "v201", "v202", "v203", "v204", "v205", "v206", "v207", "v208", "v209", "v210", "v211", "v212", "v213", "v214", "v215", "v216", "v217", "v218", "v219", "v220", "v221", "v222", "v223", "v224", "v225", "v226", "v227", "v228", "v229", "v230", "v231", "v232", "v233", "v234", "v235", "v236", "v237", "v238", "v239", "v240", "v241", "v242", "v243", "v244", "v245", "v246", "v247", "v248", "v249", "v250", "v251", "v252", "v253", "v254", "v255", "s76", "s77"

#define CLOB_G2 "v0", "v1", "v2", "v3", "v4", "v5", "v6", "v7", "v8", "v9", "v10", "v11", "v12", "v13", "v14", "v15", "v16", "v17", "v18", "v19", "v20", "v21", "v22", "v23", "v24", "v25", "v26", "v27", "v28", "v29", "v30", "v31", "v32", "v33", "v34", "v35", "v36", "v37", "v38", "v39", "v40", "v41", "v42", "v43", "v44", "v45", "v46", "v47", "v48", "v49", "v50", "v51", "v52", "v53", "v54", "v55", "v56", "v57", "v58", "v59", "v60", "v61", "v62", "v63", "v64", "v65", "v66", "v67", "v68", "v69", "v70", "v71", "v72", "v73", "v74", "v75", "v76", "v77", "v78", "v79", "v80", "v81", "v82", "v83", "v84", "v85", "v86", "v87", "v88", "v89", "v90", "v91", "v92", "v93", "v94", "v95", "v96", "v97", "v98", "v99", "v100", "v101", "v102", "v103", "v104", "v105", "v106", "v107", "v108", "v109", "v110", "v111", "v112", "v113", "v114", "v115", "v116", "v117", "v118", "v119", "v120", "v121", "v122", "v123", "v124", "v125", "v126", "v127", "v128", "v129", "v130", "v131", "v132", "v133", "v134", "v135", "v136", "v137", "v138", "v139", "v156", "v157", "v158", "v159", "v160", "v161", "v162", "v163", "v164", "v165", "v166", "v167", "v168", "v169", "v170", "v171", "v172", "v173", "v174", "v175", "v176", "v177", "v178", "v179", "v180", "v181", "v182", "v183", "v184", "v185", "v186", "v187", "v188", "v189", "v190", "v191", "v192", "v193", "v194", "v195", "v196", "v197", "v198", "v199", "v200", "v201", "v202", "v203", "v204", "v205", "v206", "v207", "s40", "s41", "s42", "s43", "s76", "s77", "s56", "s57", "s58", "s59", "s60", "s61", "s62", "s63", "s64", "s65", "s66", "s67"

#define CLOB_X "v0", "v1", "v2", "v3", "v4", "v5", "v6", "v7", "v8", "v9", "v10", "v11", "v12", "v13", "v14", "v15", "v16", "v17", "v18", "v19", "v20", "v21", "v22", "v23", "v24", "v25", "v26", "v27", "v28", "v29", "v30", "v31", "v32", "v33", "v34", "v35", "v36", "v37", "v38", "v39", "v40", "v41", "v42", "v43", "v44", "v45", "v46", "v47", "v48", "v49", "v50", "v51", "v52", "v53", "v54", "v55", "v56", "v57", "v58", "v59", "v60", "v61", "v62", "v63", "v64", "v65", "v66", "v67", "v68", "v69", "v70", "v71", "v72", "v73", "v74", "v75", "v76", "v77", "v78", "v79", "v80", "v81", "v82", "v83", "v84", "v85", "v86", "v87", "v88", "v89", "v90", "v91", "v92", "v93", "v94", "v95", "v96", "v97", "v98", "v99", "v100", "v101", "v102", "v103", "v104", "v105", "v106", "v107", "v108", "v109", "v110", "v111", "v112", "v113", "v114", "v115", "v116", "v117", "v118", "v119", "v120", "v121", "v122", "v123", "v124", "v125", "v126", "v127", "v128", "v129", "v130", "v131", "v132", "v133", "v134", "v135", "v136", "v137", "v138", "v139", "v140", "v141", "v142", "v143", "v144", "v145", "v146", "v147", "v148", "v149", "v150", "v151", "v152", "v153", "v154", "v155", "v156", "v157", "v158", "v159", "v160", "v161", "v162", "v163", "v164", "v165", "v166", "v167", "v168", "v169", "v170", "v171", "v172", "v173", "v174", "v175", "v176", "v177", "v178", "v179", "v180", "v181", "v182", "v183", "v184", "v185", "v186", "v187", "v188", "v189", "v190", "v191", "v192", "v193", "v194", "v195", "v196", "v197", "v198", "v199", "v200", "v201", "v202", "v203", "v204", "v205", "v206", "v207", "v208", "v209", "v210", "v211", "v212", "v213", "v214", "v215", "v216", "v217", "v218", "v219", "v220", "v221", "v222", "v223", "v224", "v225", "v226", "v227", "v228", "v229", "v230", "v231", "v232", "v233", "v234", "v235", "v236", "v237", "v238", "v239", "v240", "v241", "v242", "v243", "v244", "v245", "v246", "v247", "s40", "s41", "s42", "s43", "s44", "s45", "s46", "s47", "s48", "s49", "s50", "s51", "s52", "s53", "s54", "s55", "s56", "s57", "s58", "s59", "s60", "s61", "s62", "s63", "s64", "s65", "s66", "s67", "s68", "s69", "s70", "s71", "s72", "s73", "s74", "s75"

typedef unsigned u32x2 __attribute__((ext_vector_type(2)));
typedef unsigned u32x4 __attribute__((ext_vector_type(4)));
typedef unsigned u32x8 __attribute__((ext_vector_type(8)));
typedef unsigned u32x16 __attribute__((ext_vector_type(16)));
typedef float f32x16 __attribute__((ext_vector_type(16)));
typedef float f32x2 __attribute__((ext_vector_type(2)));
typedef __bf16 bf16x8 __attribute__((ext_vector_type(8)));
typedef __bf16 bf16x2 __attribute__((ext_vector_type(2)));
typedef short s16x4 __attribute__((ext_vector_type(4)));
#define RST (WS_DBG + 49152 + blockIdx.x * 16)
#define DBGH(i) (WS_DBG + (i) * 32)
#define DBGY(i, j) (WS_DBG + 4096 + (i) * 32)
#define WS_XTRA 0u
#define UNI(x) ((unsigned)__builtin_amdgcn_readfirstlane(x))
#define DEVI __device__ __forceinline__
constexpr int SEQ = 2048, DM = 640, NH = 32, NSEG = 64, HD = 4096, FF = 1728;
constexpr unsigned SENT = 0xFFFFFFFFu, DROP = 0x80000000u;
constexpr int SLOT_LOG = 81920, SLOT_PHYS = 2 * SLOT_LOG;
constexpr unsigned SPIN_MAX = 1u << 18;
constexpr unsigned WS_COMM = 0, F_ST = 0x80000, F_PROG = 0x90000, F_X = 0x98000, F_G2 = 0x9A000, F_G = 0x9C000, F_PAR = 0x9E000, WS_INIT = 0x9C000, F_CHK = 0xD0000,
                   WS_DBG = 0xA0000, WS_PROBE = 0xC0000, WS_DUMP = 0x100000, WS_KX = 2u << 20, WS_VX = 18u << 20, WS_QX = 34u << 20, WS_XO = 50u << 20, WS_KVM = 66u << 20,
                   WS_HH = 74u << 20, WS_T2 = 81u << 20, WS_END = (84u << 20) + WS_XTRA;
DEVI unsigned spread(unsigned o) { return ((o >> 13) << 14) + (o & 8191u); }
struct Args { const char* x; const char* mem0; const char* wq; const char* wk; const char* wv; const char* wo; const char* w1; const char* w3; const char* w2; const char* gw;
              char* out; char* ws; int dH; int dY; int dstep; int force; int mask; int place; int fH; int fY; int dF; int dR; };
DEVI Args kargs() {
  const __attribute__((address_space(4))) Args* p = (const __attribute__((address_space(4))) Args*)__builtin_amdgcn_kernarg_segment_ptr();
  asm volatile("" : "+s"(p));
  return Args{p->x, p->mem0, p->wq, p->wk, p->wv, p->wo, p->w1, p->w3, p->w2, p->gw, p->out, p->ws, p->dH, p->dY, p->dstep, p->force, p->mask, p->place, p->fH, p->fY, p->dF, p->dR};
}
DEVI unsigned lane_id() { unsigned l; asm volatile("v_mbcnt_lo_u32_b32 %0, -1, 0\n v_mbcnt_hi_u32_b32 %0, -1, %0" : "=v"(l)); return l; }
DEVI unsigned lo32(const void* p) { return (unsigned)(size_t)p; }
DEVI unsigned hi32(const void* p) { return (unsigned)((size_t)p >> 32) & 0xffffu; }

#define PD_K 8
#define PD_F 8
#define P_DP 0
#define P_EG 0
#define P_TW 0
#define P_HX 0
#define P_CL 0
#define P_SK 0
#define P_YP 1
#define P_YL 0
#define P_W3 0
#define P_IL 0
#define P_KVF 1
#define KBEG "_Z5k_all4Args"
#define KEND "k_all_last"
#define FIN __builtin_amdgcn_endpgm()
#define BEG
#define TARG
#define TPAS

DEVI void role_H(const Args& a, unsigned lds, unsigned w, unsigned h TARG) {
  unsigned lane = lane_id();
  u32x4 T4;
  {
    const unsigned row = ((lane >> 5) & 1) * 16 + ((lane >> 3) & 1) * 8 + ((lane >> 4) & 1) * 4 + ((lane >> 2) & 1);
    const unsigned vv = row * 8192u + h * 256 + w * 64 + ((lane & 3) ^ ((lane >> 3) & 2)) * 16;
    const u32x4 Pv = {vv, vv + 2 * 8192 - 1024, vv + 32 * 8192 - 2048, vv + 34 * 8192 - 3072}, Sv = {lo32(a.wv), hi32(a.wv), DM * HD * 2, 0x20000u};
    const unsigned e = (w & 2) << 20, o = e ^ (2u << 20);
    const u32x8 K8 = {e, e + (1u << 19), (8u << 19) + (e >> 2), e + (2u << 19), e + (3u << 19), o, o + (1u << 19), (8u << 19) + (o >> 2)};
    asm volatile(ASM_H_0 : "+v"(lane), "+s"(w), "+s"(h), "={s[60:63]}"(T4) : "{v[108:111]}"(Pv), "{s[20:23]}"(Sv), "{s[24:31]}"(K8), "{s34}"(lds + w * 40960) : "scc", "memory");
  }
  BEG;
  const unsigned c16 = lane & 15, g = lane >> 4;
  const unsigned kb[10] = {2 * w, 2 * w + 1, 8 + (w >> 1), 2 * (w ^ 1), 2 * (w ^ 1) + 1, 2 * (w ^ 2), 2 * (w ^ 2) + 1, 8 + ((w ^ 2) >> 1), 2 * (w ^ 3), 2 * (w ^ 3) + 1};
  const unsigned m0o = (lane & 7) * 1280 + (g * 16 + ((lane >> 3) & 1) * 8) * 2;
  const unsigned wl = lds + w * 40960, img = lds + 36864;
  const unsigned row = ((lane >> 5) & 1) * 16 + ((lane >> 3) & 1) * 8 + ((lane >> 4) & 1) * 4 + ((lane >> 2) & 1);
  const unsigned vb = row * 8192u + h * 256 + w * 64, g1 = g & 1;
  const unsigned vq = vb + ((lane & 3) ^ g1) * 16, vv = vb + ((lane & 3) ^ (g1 * 2)) * 16;
  const unsigned rd = wl + ((lane >> 2) & 3) * 64 + g * 256, rdq = rd + ((lane >> 1) & 1) * 32 + (lane & 1) * 8, rdv = rd + (lane & 3) * 8;
  u32x16 P0 = {vq, vq + 2 * 8192 - 1024, vq + 32 * 8192 - 2048, vq + 34 * 8192 - 3072, vv, vv + 2 * 8192 - 1024, vv + 32 * 8192 - 2048, vv + 34 * 8192 - 3072,
               rdq + g1 * 16, rdq + (g1 ^ 1) * 16, rdv + g1 * 32, rdv + (g1 ^ 1) * 32, m0o + kb[0] * 128, m0o + kb[2] * 128, 0, 0};
  const unsigned pb = WS_COMM;
  const unsigned o16 = g < 2 ? 0x3F803F80u : 0u;
  const unsigned one = ((c16 >> 2) & 1) == g1 ? ((c16 & 1) ? 0x3F800000u : 0x3F80u) : 0u, pos = (c16 >> 3) * 2 + ((c16 >> 1) & 1);
  u32x16 P1 = {(lane & 8) ? 0xEEEEu : 0x4444u, wl + lane * 16, lds + (w ^ 1) * 40960 + lane * 16, lds + (w ^ 2) * 40960 + lane * 16, lds + (w ^ 3) * 40960 + lane * 16,
               img + w * 512 + g * 128 + (c16 & 7) * 16 + (c16 >> 3) * 8, img + g * 128 + (c16 & 7) * 16,
               pb + spread(((lane >> 2) & 1) * 32768 + h * 1024 + w * 256 + g * 16 + (lane & 3) * 64 + (lane & 8)),
               g * 256 + ((lane >> 2) & 1) * 128 + ((lane >> 3) & 1) * 64 + (lane & 3) * 16, F_ST + spread((h * 4 + w) * 256) + lane * 4,
               o16, o16, pos == 0 ? one : 0, pos == 1 ? one : 0, pos == 2 ? one : 0, pos == 3 ? one : 0};
  u32x4 P2 = {0x3F803F80u, 0x3F803F80u, 0x3F803F80u, 0x3F803F80u};
  u32x16 S0 = {lo32(a.ws), hi32(a.ws), WS_END, 0x20000u, lo32(a.wq), hi32(a.wq), DM * HD * 2, 0x20000u, lo32(a.wk), hi32(a.wk), DM * HD * 2, 0x20000u, lo32(a.wv), hi32(a.wv), DM * HD * 2, 0x20000u};
  const unsigned e = (w & 2) << 20, o = e ^ (2u << 20);
  u32x16 S1 = {e, e + (1u << 19), (8u << 19) + (e >> 2), e + (2u << 19), e + (3u << 19), o, o + (1u << 19), (8u << 19) + (o >> 2), o + (2u << 19), o + (3u << 19),
               wl, (unsigned)P_DH, 0, 0, SLOT_PHYS, 2 * SLOT_PHYS};
  u32x8 S2 = {WS_KX + h * 8192 + w * 2048 - wl, WS_KVM + h * 4096 - img, 0, SENT, SPIN_MAX, WS_VX + h * 8192 + w * 2048 - wl, 0, 0};
  u32x8 S3 = {WS_KVM + h * 4096 + 2048 + w * 512 - (wl >> 1), 0x19000000u, lo32(a.mem0), (unsigned)((size_t)a.mem0 >> 32), 0xFF00FF00u, 0xFF00FF00u, (unsigned)P_FH, 0};
  u32x4 S4 = {0, F_ST + w * 256 + 128, DBGH(h * 4 + w), 0};
  u32x4 S7 = {(unsigned)(P_DH - (P_AD >> 24)), (unsigned)(P_DH + 240), (unsigned)(P_AD & 255), (unsigned)-((P_AD >> 8) & 255)};
  unsigned S8 = ((P_AD >> 16) & 255) * 2;
  const unsigned pr = F_PROG + (2 * h + (w >> 1)) * 128;
  u32x4 S5 = {pr + 8192, pr + 16384, F_PAR, RST};
  u32x8 S6 = {w & 1, pb + spread(65536 + kb[0] * 1024), pb + spread(65536 + kb[1] * 1024) - 1024, pb + spread(65536 + kb[2] * 1024) - 2048, 0, 0, 0, WS_DUMP};
  asm volatile(ASM_H
               : "+{s[60:63]}"(T4), "+{v[104:119]}"(P0), "+{v[84:99]}"(P1), "+{v[100:103]}"(P2),
                 "+{s[8:23]}"(S0), "+{s[24:39]}"(S1), "+{s[40:47]}"(S2), "+{s[48:55]}"(S3), "+{s[56:59]}"(S4), "+{s[80:83]}"(S5), "+{s[84:91]}"(S6), "+{s[92:95]}"(S7), "+{s96}"(S8)
               :: CLOB_H, CLOB_A, "vcc", "scc", "memory");
}

DEVI void role_Y(const Args& a, unsigned lds, unsigned w, int th, int cc) {
  const unsigned lane = lane_id(), g = lane >> 4;
  const unsigned wl = lds + w * 32768;
  const unsigned row = ((lane >> 5) & 1) * 32 + ((lane >> 4) & 1) * 16 + ((lane >> 3) & 1) * 4 + ((lane >> 2) & 1);
  const unsigned voff = (row + w * 512) * 1280u + ((((lane >> 1) ^ (lane >> 4)) & 1) * 32) + (lane & 1) * 16 + cc * 64;
  const unsigned rd = wl + (lane & 3) * 8 + ((lane >> 2) & 3) * 64 + g * 256;
  const unsigned pb = WS_COMM;
  const unsigned cg = 2 * cc + (lane >> 5);
  const unsigned red = lds + 131072, sw = P_YS ? th : 0;
  u32x16 P0 = {(lane & 1) ? 0xEEEEu : 0x4444u, pb + spread(th * 32768 + w * 4096) + lane * 16, pb + spread(65536 + cg * 256 + th * 128 + (lane & 31) * 4) - sw * 32768,
               red + w * 512 + lane * 4, red + (lane >> 5) * 256 + (((lane >> 2) & 3) * 16 + ((lane >> 4) & 1) * 8 + (lane & 3) * 2) * 4,
               0, 0, 0, voff, voff + 2 * 1280 - 1024, voff + 64 * 1280 - 2048, voff + 66 * 1280 - 3072, rd + (g & 1) * 32, rd + ((g & 1) ^ 1) * 32, spread((((lane >> 2) & 1) * 16 + w * 4 + (lane & 3)) * 1024 + (cc & 3) * 256), 0};
  u32x8 S0 = {lo32(a.ws), hi32(a.ws), WS_END, 0x20000u, lo32(a.wo), hi32(a.wo), DM * HD * 2, 0x20000u};
  u32x8 S1 = {sw * 2048 * 1280, (sw ^ 1) * 2048 * 1280, wl, (unsigned)P_DY, 0, sw * 32768, SLOT_PHYS + sw * 32768, 2 * SLOT_PHYS + sw * 32768};
  u32x8 S2 = {(sw ^ 1) * 32768, w, 0, SENT - 1, SPIN_MAX, (unsigned)(P_DY + 160), 0, 0};
  u32x8 S3 = {F_PAR, 0, RST, 0, 0, 0, sw ? -32768u : 32768u, (unsigned)(P_DH - P_DY + P_DR)};
  u32x4 S4 = {0, F_ST, DBGY((th * 40 + cc) * 4 + w, (th * 20 + cc) * 4 + w), 0};
  asm volatile(ASM_Y2 : "+{v[48:63]}"(P0), "+{s[8:15]}"(S0), "+{s[32:39]}"(S1), "+{s[40:47]}"(S2), "+{s[48:55]}"(S3), "+{s[56:59]}"(S4) :: CLOB_Y, CLOB_A, "vcc", "scc", "memory");
}

template <int KIND>
DEVI void role_G1(const Args& a, unsigned lds, unsigned w, unsigned sl TARG) {
  unsigned lane = lane_id();
  const unsigned kind = KIND == 1 ? sl >> 4 : KIND, step = KIND < 3 ? 16 * 8192 : 16 * FF * 2;
  const char* Wa = KIND == 3 ? a.w1 : kind == 0 ? a.wq : kind == 1 ? a.wk : a.wv;
  const char* Wb = KIND == 3 ? a.w3 - 1024 : Wa;
  const u32x4 Da = {lo32(Wa), hi32(Wa), KIND < 3 ? DM * HD * 2 : DM * FF * 2, 0x20000u}, Db = {lo32(Wb), hi32(Wb), KIND < 3 ? DM * HD * 2 : DM * FF * 2 + 1024, 0x20000u};
  const unsigned f0 = (sl - 48) * 4 + w, f = f0 < 54 ? f0 : 53;
  const unsigned wa = KIND < 3 ? (lane >> 3) * 8192 + (lane & 7) * 16 + (sl & 15) * 512 + w * 128 : (lane >> 2) * (FF * 2) + (lane & 3) * 16 + f * 64;
  const unsigned wb = KIND < 3 ? wa + 8 * 8192 - 1024 : wa;
  const unsigned stage = lds + 40960 + w * 30720;
  unsigned sw = 0;
  asm volatile(ASM_GK_0 : "+v"(lane), "+{s26}"(sw) : "{v45}"(wa), "{v46}"(wb), "{s[16:19]}"(Da), "{s[20:23]}"(Db), "{s25}"(stage), "{s27}"(step) : "scc", "memory");
  BEG;
  const unsigned t = lane & 31, g2 = lane >> 5;
  unsigned gv[10], rd[8];
#pragma unroll
  for (int j = 0; j < 10; ++j) { const unsigned p = (10 * w + j) * 64 + lane, r = p / 80, c = p - r * 80; gv[j] = r * 1280 + ((c & ~15u) | ((c ^ r) & 15)) * 16 - (j & 3) * 1024; }
#pragma unroll
  for (int i = 0; i < 8; ++i) rd[i] = lds + t * 1280 + (((2 * i + g2) ^ t) & 15) * 16;
  const unsigned G = lane >> 4, ra = (lane >> 2) & 3, cb = lane & 3;
  u32x16 P0 = {gv[0], gv[1], gv[2], gv[3], gv[4], gv[5], gv[6], gv[7], gv[8], gv[9], rd[0], rd[1], rd[2], rd[3], rd[4], rd[5]};
  u32x16 S0 = {lo32(a.ws), hi32(a.ws), WS_END, 0x20000u, lo32(a.x), hi32(a.x), SEQ * DM * 2, 0x20000u, Da[0], Da[1], Da[2], Da[3], Db[0], Db[1], Db[2], Db[3]};
  if (KIND < 3) {
    const unsigned j = sl & 15, h = 2 * j + (w >> 1), wq = 2 * (w & 1);
    const unsigned so = h * 8192 + wq * 2048 + (kind == 2 ? ((lane >> 4) & 1) * 1024 + (g2 * 16 + (lane & 15)) * 16 : ((t >> 3) & 1) * 1024 + (g2 * 16 + 8 * (t >> 4) + (t & 7)) * 16);
    u32x8 P1 = {rd[6], rd[7], so, so, 0, wa, wb, stage + (8 * (G >> 1) + ra) * 128 + (G & 1) * 32 + cb * 8};
    u32x16 S1 = {lds + w * 10240, stage, sw, step, 0, 0, 0, 0, 0, kind == 0 ? WS_QX : kind == 1 ? WS_KX : WS_VX, 0x40000u, F_PROG + (sl * 4 + w) * 128, kind == 0 ? 0x3e0293eeu : 0x3f800000u, 0, SPIN_MAX, RST};
    if (KIND < 2) { asm volatile(ASM_GK : "+{v[24:39]}"(P0), "+{v[40:47]}"(P1), "+{s[8:23]}"(S0), "+{s[24:39]}"(S1) :: CLOB_G1, CLOB_A, "vcc", "scc", "memory"); }
    else { asm volatile(ASM_GV : "+{v[24:39]}"(P0), "+{v[40:47]}"(P1), "+{s[8:23]}"(S0), "+{s[24:39]}"(S1) :: CLOB_G1, CLOB_A, "vcc", "scc", "memory"); }
  } else {
    const unsigned so = f0 < 54 ? f * 2048 + lane * 16 : DROP;
    u32x8 P1 = {rd[6], rd[7], so, so, 0, wa, wb, stage + (8 * (G >> 1) + ra) * 64 + (G & 1) * 32 + cb * 8};
    u32x16 S1 = {lds + w * 10240, stage, sw, step, 0, 0, 0, 0, 0, WS_HH, 110592u, F_PROG + (sl * 4 + w) * 128, 0xbfb8aa3bu, 0, SPIN_MAX, RST};
    asm volatile(ASM_GF : "+{v[24:39]}"(P0), "+{v[40:47]}"(P1), "+{s[8:23]}"(S0), "+{s[24:39]}"(S1) :: CLOB_G1, CLOB_A, "vcc", "scc", "memory");
  }
}

DEVI void role_G2(const Args& a, unsigned lds, unsigned w, int cs, unsigned tag) {
  const unsigned lane = lane_id(), t = lane & 31, g2 = lane >> 5;
  const unsigned stage = lds + w * 30720, G = lane >> 4, ra = (lane >> 2) & 3, cb = lane & 3;
  const unsigned wa = (w * 432 + (lane >> 3)) * 1280 + (lane & 7) * 16 + cs * 128;
  unsigned ci = (27 * w) / 2 + (lane < 15 ? lane : 0); ci = ci < 54 ? ci : 53;
  const unsigned T = w >> 1, hf = w & 1;
  u32x16 P0 = {w * 27 * 1024 + lane * 16, ci * 128, t * 4, lds + w * 2048 + lane * 16, (lds + w * 8192 + lane * 16) ^ 0x8000u,
               (t >> 4) * 20480 + (2 * cs + T) * 1024 + (2 * hf + g2) * 256 + (t & 15) * 16, wa, wa + 8 * 1280 - 1024,
               stage + (4 * (G >> 1) + ra) * 128 + (16 * (G & 1) + 8 * (cb & 1) + 4 * (cb >> 1)) * 2, 0, 0, 0, 0, 0, 0, 0};
  u32x8 S0 = {lo32(a.ws), hi32(a.ws), WS_END, 0x20000u, lo32(a.w2), hi32(a.w2), DM * FF * 2, 0x20000u};
  const unsigned rot = cs * 27 / 10;
  u32x16 S1 = {stage, rot * 16 * 1280, 16 * 1280, 0, WS_HH - 110592u, F_G, WS_T2 - 40960u, F_G2 + ((2 * cs + T) * 2 + hf) * 128, F_PROG + 48 * 512, SPIN_MAX, 0, 0, 27 * 16 * 1280, rot * 1024, tag, RST};
  asm volatile(ASM_G2 KEND ":\n" : "+{v[140:155]}"(P0), "+{s[8:15]}"(S0), "+{s[24:39]}"(S1) :: CLOB_G2, CLOB_A, "vcc", "scc", "memory");
}

DEVI void role_Z(const Args& a, unsigned lds, unsigned w, int th, int sl, unsigned tag) {
  const unsigned lane = lane_id(), g = lane >> 4, r16 = lane & 15;
  const unsigned stage = lds + w * 30720, ra = (lane >> 2) & 3, cb = lane & 3, row = 4 * w + g;
  const unsigned wa = (w * 1024 + (lane >> 2)) * 1280 + (lane & 3) * 16 + sl * 64;
  u32x16 P0 = {th * 131072 + w * 32768 + lane * 16, (lane & 8) ? F_G2 + (sl * 2 + (lane & 1)) * 128 : F_X + (8 * w + (lane & 7)) * 128, (lane & 8) ? 0u : 4096u, (th * 16 + row) * 4, lds + w * 2048 + lane * 16, lds + ((r16 >> 1) & 1) * 1024 + ((r16 >> 2) * 16 + row) * 16 + (r16 & 1) * 8,
               th * 20480 + sl * 1024 + ((r16 >> 2) * 16 + row) * 16 + (r16 & 3) * 4, (th * 16 + row) * 1280 + sl * 64 + r16 * 4, wa, wa + 16 * 1280 - 1024,
               stage + (16 * (g >> 1) + 4 * (g & 1) + ra) * 64 + cb * 16, 0, 0, 0, 0, 0};
  u32x16 S0 = {lo32(a.ws), hi32(a.ws), WS_END, 0x20000u, lo32(a.wo), hi32(a.wo), DM * HD * 2, 0x20000u, lo32(a.out), hi32(a.out), SEQ * DM * 2, 0x20000u, 0, 0, 0, 0};
  const unsigned rot = (sl % 10) * 8 / 10;
  u32x16 S1 = {stage, rot * 4 * 32 * 1280, 32 * 1280, 0, WS_XO - 0x40000u, F_G, WS_T2, 0, 0, 0, SPIN_MAX, 0, w, tag, F_CHK + 0x8000 + (th * 20 + sl) * 128, RST};
  u32x8 S2 = {rot * 4096, ((rot + 1) & 7) * 4096, ((rot + 2) & 7) * 4096, ((rot + 3) & 7) * 4096, ((rot + 4) & 7) * 4096, ((rot + 5) & 7) * 4096, ((rot + 6) & 7) * 4096, ((rot + 7) & 7) * 4096};
  asm volatile(ASM_Z : "+{v[140:155]}"(P0), "+{s[8:23]}"(S0), "+{s[24:39]}"(S1), "+{s[44:51]}"(S2) :: CLOB_G2, CLOB_A, "vcc", "scc", "memory");
}

DEVI float rbf(float v) { return (float)(__bf16)v; }
DEVI void gates(const Args& a, unsigned ww) {
  const unsigned lane = lane_id();
  const __amdgpu_buffer_rsrc_t rs = __builtin_amdgcn_make_buffer_rsrc(a.ws, 0, WS_END, 0x20000);
  bf16x8 xv[2][5], gw[5];
#pragma unroll
  for (int n = 0; n < 5; ++n) {
    gw[n] = *(const bf16x8*)(a.gw + ((lane & 15) + 16 * n) * 16);
#pragma unroll
    for (int p = 0; p < 2; ++p) xv[p][n] = *(const bf16x8*)(a.x + (ww * 8 + p * 4 + (lane >> 4)) * 1280 + ((lane & 15) + 16 * n) * 16);
  }
#pragma unroll
  for (int p = 0; p < 2; ++p) {
    float acc = 0.f;
#pragma unroll
    for (int n = 0; n < 5; ++n)
#pragma unroll
      for (int j = 0; j < 8; ++j) acc += rbf((float)xv[p][n][j] * (float)gw[n][j]);
    for (int m = 1; m < 16; m <<= 1) acc += __shfl_xor(acc, m);
    const float gate = rbf(1.f / (1.f + __expf(-rbf(acc))));
    __builtin_amdgcn_raw_buffer_store_b32(__builtin_bit_cast(unsigned, gate), rs, (lane & 15) == 0 ? (ww * 8 + p * 4 + (lane >> 4)) * 4 : DROP, F_G, 16);
  }
}

DEVI void role_X(const Args& a, unsigned w, int xi, int NX, unsigned tag) {
  const unsigned lane = lane_id(), tok = lane & 31, g2 = lane >> 5;
  const unsigned ww = xi * 4 + w, h = ww & 31;
  const __amdgpu_buffer_rsrc_t rs = __builtin_amdgcn_make_buffer_rsrc(a.ws, 0, WS_END, 0x20000);
  unsigned par = ~tag;
  for (unsigned i = 0; i < SPIN_MAX && (par & ~0x2000u) != tag; ++i) { asm volatile("s_sleep 4" ::: "memory"); par = UNI(__builtin_amdgcn_raw_buffer_load_b32(rs, 0, F_PAR + 8, 16)); }
  const unsigned l3 = lane % 3, s0 = UNI(ww >> 5), st = UNI(NX / 8);
  u32x8 P0 = {h * 8192 + ((tok >> 3) & 1) * 1024 + (g2 * 16 + 8 * (tok >> 4) + (tok & 7)) * 16, h * 8192 + (tok >> 4) * 1024 + (g2 * 16 + (tok & 15)) * 16,
              h * 4096 + g2 * 128 + tok * 16, h * 4096 + 2048 + ((2 * (tok >> 4) + g2) * 16 + (tok & 15)) * 8,
              (tok >> 4) * 131072 + h * 4096 + g2 * 256 + (tok & 15) * 16, (l3 * 64 + 2 * h + ((lane / 3) & 1)) * 128, spread(h * 1024 + (lane & 3) * 256 + 128), tok - 4 * g2};
  u32x4 S0 = {UNI(lo32(a.ws)), UNI(hi32(a.ws)), WS_END, 0x20000u};
  u32x16 S1 = {s0, st, WS_QX + s0 * 0x40000, WS_KX + s0 * 0x40000, WS_VX + s0 * 0x40000, WS_KVM + s0 * 0x20000, WS_XO + s0 * 0x40000, 0, F_X + UNI(ww) * 128, SPIN_MAX,
               st * 0x40000, st * 0x20000, st * 128, F_PROG, 0, F_ST + (par & 0x2000u)};
  u32x4 S2 = {0xFFu, 0xFFu, RST, F_ST + (~par & 0x2000u) - 128};
  asm volatile(ASM_X : "+{v[248:255]}"(P0), "+{s[8:11]}"(S0), "+{s[24:39]}"(S1), "+{s[76:79]}"(S2) :: CLOB_X, "vcc", "scc", "memory");
}

DEVI void collect(__amdgpu_buffer_rsrc_t rs, unsigned base, unsigned n, unsigned tag) {
  const unsigned lane = lane_id(), vo = (lane < n ? lane : 0) * 128;
  for (unsigned i = 0; i < SPIN_MAX; ++i) {
    asm volatile("s_sleep 4" ::: "memory");
    if (__builtin_amdgcn_ballot_w64(__builtin_amdgcn_raw_buffer_load_b32(rs, vo, base, 16) != tag) == 0) break;
  }
}
DEVI void role_P(const Args& a, unsigned w, unsigned tag, unsigned t0) {
  if (w > 1) return;
  const __amdgpu_buffer_rsrc_t rs = __builtin_amdgcn_make_buffer_rsrc(a.ws, 0, WS_END, 0x20000);
  const unsigned lane = lane_id(), vo = lane == 0 ? 0 : DROP, vl = lane < 32 ? lane * 4 : DROP;
  if (w == 1) {
    collect(rs, F_CHK, 52, tag);
    __builtin_amdgcn_raw_buffer_store_b32(tag, rs, vo, F_PAR, 16);
    __builtin_amdgcn_s_barrier();
    collect(rs, F_CHK + 0x4000, 64, tag);
    __builtin_amdgcn_raw_buffer_store_b32(tag, rs, vl, F_PAR + 128, 16);
    while ((unsigned)__builtin_amdgcn_s_memrealtime() - t0 < 3000u) __builtin_amdgcn_s_sleep(64);
    collect(rs, F_CHK + 0x8000, 40, tag);
    __builtin_amdgcn_raw_buffer_store_b32(tag, rs, vo, F_PAR + 4, 16);
    return;
  }
  unsigned best[2] = {~0u, ~0u};
  for (int i = 0; i < 4 || (i < 12 && best[0] > 600 && best[1] > 600); ++i)
#pragma unroll
    for (int p = 0; p < 2; ++p) {
      const unsigned t1 = (unsigned)__builtin_amdgcn_s_memtime();
      asm volatile("s_waitcnt lgkmcnt(0)" ::: "memory");
      __builtin_amdgcn_raw_buffer_store_b32(0u, rs, vl, WS_PROBE + p * 8192 + (i & 7) * 256, 16);
      asm volatile("s_waitcnt vmcnt(0)" ::: "memory");
      const unsigned t = (unsigned)__builtin_amdgcn_s_memtime() - t1;
      best[p] = t < best[p] ? t : best[p];
    }
  unsigned r = best[0] <= best[1] ? 0u : 0x2000u;
  if (P_FORCE >= 0) r = P_FORCE ? 0x2000u : 0u;
  __builtin_amdgcn_s_barrier();
  __builtin_amdgcn_raw_buffer_store_b32(tag | r, rs, vo, F_PAR + 8, 16);
}
template <int N> DEVI void check_in(const Args& a, unsigned w, unsigned line, unsigned tag) {
  const __amdgpu_buffer_rsrc_t rs = __builtin_amdgcn_make_buffer_rsrc(a.ws, 0, WS_END, 0x20000);
  asm volatile("s_waitcnt vmcnt(%0)\n s_barrier" :: "n"(N) : "memory");
  const unsigned lane = lane_id();
  __builtin_amdgcn_raw_buffer_store_b32(tag, rs, w == 0 && lane < 32 ? lane * 4 : DROP, line, 16);
}

template <int D> DEVI void pull1(u32x4 d, unsigned vo, unsigned so) {
  asm volatile("buffer_load_dwordx4 v[228:231], %0, %1, %2 offen\n s_waitcnt vmcnt(%3)" :: "v"(vo), "s"(d), "s"(UNI(so)), "n"(D) : "v228", "v229", "v230", "v231", "memory");
}
DEVI u32x4 desc(const char* m, unsigned bytes) { return u32x4{UNI(lo32(m)), UNI(hi32(m)), bytes, 0x20000u}; }
template <int D> DEVI unsigned pull(const char* m, unsigned bytes, unsigned vo, unsigned step, unsigned n, unsigned i, unsigned ni) {
  const u32x4 d = desc(m, bytes);
  for (; i < n; i += ni) pull1<D>(d, vo, i * step);
  return i - n;
}
DEVI unsigned wo_piece(unsigned l, unsigned x) {
  const unsigned q = l * 52429u >> 18, row = ((q ^ (P_YS ? x * 1024 : 0)) & 2048) + (q & 3) * 512 + ((q >> 2) & 511);
  return row * 1280 + (l - q * 5) * 128 + (x & 1) * 640;
}
DEVI unsigned pullb(const char* m, unsigned vo, unsigned long long tab, unsigned nb, unsigned fl, unsigned i, unsigned ni, unsigned tb, const char* wo, unsigned x, unsigned& l) {
  const u32x4 d = desc(m, DM * HD * 2), e = desc(wo, DM * HD * 2);
  for (; i < nb * 64; i += ni) {
    if (P_PC) while ((int)((unsigned)__builtin_amdgcn_s_memrealtime() - tb - (i * P_PC >> 6)) < 0) {
      if (P_IL && UNI(l) < 4096 * 5) {
        const unsigned o = wo_piece(l, x);
        pull1<PD_H>(e, ((((o + lo32(wo)) ^ (x * 2048 + lo32(m))) >> 11) & 3) ? o + (vo & 127) : DROP, 0);
        l += 88 * 8;
      } else __builtin_amdgcn_s_sleep(1);
    } const unsigned k = (unsigned)(tab >> (i >> 6 << 2)) & 15; pull1<PD_H>(d, vo, ((k ^ (k < 8 ? fl * 4 : fl)) * 64 + (i & 63)) * 8192); }
  return i - nb * 64;
}

DEVI unsigned pullc(const char* m, unsigned vo, bool l1, unsigned long long tab, unsigned nb, unsigned i) {
  const u32x4 d = desc(m, DM * HD * 2);
  for (; i < nb * 32; i += 22) { const unsigned k = (unsigned)(tab >> (i >> 5 << 2)) & 15; pull1<PD_H>(d, vo + (l1 ? k ^ (k < 8 ? 4 : 1) : k) * (64 * 8192), (i & 31) * 16384); }
  return i - nb * 32;
}

enum { R_NONE, R_H, R_Y, R_P, R_G1, R_G2, R_Z, R_X };
__host__ DEVI void place(int v, int x, int r, int& role, int& id) {
  role = R_NONE; id = 0;
  if (x < 4) {
    if (r < 8) { role = R_H; id = x * 8 + r; }
    else if (r < 18) { role = R_Y; id = x * 10 + r - 8; }
    else if (x == 2 && r == 31) role = R_P;
  } else {
    if (r < 8) { role = R_G1; id = 16 + 4 * r + x - 4; }
    else if (r < 18) {
      if (x == 4) { role = R_G2; id = r - 8; }
      else { role = R_G1; id = (r - 8) * 3 + x - 5; if (id >= 16) id += 32; }
    }
    else if (r < 28) { role = R_Z; id = ((x - 4) >> 1) * 20 + ((x - 4) & 1) * 10 + r - 18; }
    else { role = R_X; id = (x - 4) * 4 + r - 28; }
  }
}

__global__ __launch_bounds__(256, 1) void k_all(Args) {
  extern __shared__ __attribute__((aligned(16))) char smem[];
  const unsigned lds = (unsigned)(size_t)(__attribute__((address_space(3))) char*)smem;
  const unsigned b = blockIdx.x, x = b & 7, r = b >> 3;
  const unsigned w = __builtin_amdgcn_readfirstlane(threadIdx.x >> 6);
  if (__builtin_expect(r < 8 && x < 4, 1)) { role_H(kargs(), lds, w, x * 8 + (r ^ P_HX) TPAS); FIN; }
  unsigned sl = 16 + 4 * r + x - 4;
  if (r >= 8) {
    const Args a = kargs();
    const unsigned t0 = (unsigned)__builtin_amdgcn_s_memrealtime();
    const unsigned lane = lane_id();
    if (r == 31 && !(P_MASK & 2048) && (x != 2 || w >= 2 || !P_EG)) {
      const bool h = x == 2 && P_EG;
      asm volatile("s_getpc_b64 s[8:9]\n LPC_%=:\n s_sub_u32 s8, s8, LPC_%=-" KBEG "\n s_subb_u32 s9, s9, 0\n s_and_b32 s9, s9, 0xffff\n s_mov_b32 s10, " KEND "-" KBEG "\n s_mov_b32 s11, 0x20000\n"
                   "s_mov_b32 s12, 0\n LPL_%=:\n buffer_load_dwordx4 v[228:231], %0, s[8:11], s12 offen\n s_add_u32 s12, s12, %1\n s_cmp_lt_u32 s12, s10\n s_cbranch_scc1 LPL_%=\n"
                   :: "v"(lane * 16 + (h ? w - 2 : w) * 1024), "s"(UNI(h ? 2048u : 4096u)) : "s8", "s9", "s10", "s11", "s12", "v228", "v229", "v230", "v231", "scc", "memory");
    }
    if (P_TW && r >= 18 && r < 18 + P_TW) {
      const char* m = w == 0 ? a.wq : w == 1 ? a.wk : w == 2 ? a.wv : a.wo;
      asm volatile("buffer_load_dword v228, %0, %1, 0 offen" :: "v"(lane * 81920 + (r - 18) * (81920 / (P_TW ? P_TW : 1))), "s"(desc(m, DM * HD * 2)) : "v228", "memory");
    }
    BEG;
    int role, id;
    place(1, x, r, role, id);
    role = UNI(role); id = UNI(id);
    const __amdgpu_buffer_rsrc_t rs = __builtin_amdgcn_make_buffer_rsrc(a.ws, 0, WS_END, 0x20000);
    unsigned tag = 0;
    if (r >= 18 || role >= R_G2) tag = __builtin_amdgcn_raw_buffer_load_b32(rs, 0, F_PAR + 4, 16);
    if (x < 4) {
      const unsigned j = (r - 18) * 4 + x;
      if (j < 52) {
        const u32x4 ff = {SENT, SENT, SENT, SENT};
#pragma unroll
        for (int i = 0; i < 3; ++i) __builtin_amdgcn_raw_buffer_store_b128(ff, rs, threadIdx.x * 16 + i * 4096, j * 12288, 16);
        if (r == 30) check_in<0>(a, w, F_CHK + j * 128, (UNI(tag) | 0x3FFFu) + 1);
      }
      if (r < 30 && P_CL) {
        const unsigned vo = x * 2048 + w * 256 + ((lane >> 3) & 1) * 128 + ((lane >> 4) & 1) * 1024 + (lane >> 5) * 8192 + (lane & 7) * 16;
        const bool l1 = lane & 8;
        unsigned i = pullc(a.wv, vo, l1, 0x7695432810ull, 10, r - 8);
        if (r >= 18) check_in<PD_H>(a, w, F_CHK + j * 128, (UNI(tag) | 0x3FFFu) + 1);
        i = pullc(a.wq, vo, l1, 0x76954, 5, i); i = pullc(a.wk, vo, l1, 0x467958, 6, i);
        i = pullc(a.wq, vo, l1, 0x80213, 5, i); pullc(a.wk, vo, l1, 0x3120, 4, i);
        if (r >= 18) {
          const u32x4 d = desc(a.wo, DM * HD * 2);
          const unsigned long long bs = x & 1 ? 0x1911100908ull : 0x2120180100ull;
          for (unsigned t = (r - 18) * 8 + (lane >> 3); t < 5120; t += 96) {
            const unsigned p = t * 52429u >> 18, e = t - p * 5, l0 = ((unsigned)(bs >> (8 * e)) & 255) + 10 * w, l = l0 >= 40 ? l0 - 40 : l0;
            pull1<PD_O>(d, (4 * ((p >> 2) & 127) + (p & 3) * 512 + (p >> 9) * 2048) * 1280 + l * 128 + (lane & 7) * 16, 0);
          }
        }
        asm volatile("s_waitcnt vmcnt(0)" ::: "memory");
        if (P_CL > 1) __builtin_amdgcn_s_barrier();
      }
      if (r < 30 && (P_YP || r >= 18) && !P_CL) {
        const unsigned fl = w & 1, vo = x * 2048 + (lane >> 3) * 256 + fl * 128 + (lane & 7) * 16;
        const unsigned NI = P_YP ? 44 : 24;
        unsigned i = ((r - (P_YP ? 8 : 18)) * 4 + w) >> 1;
        unsigned tb = t0 + P_P0, lw = ((r - 8) * 4 + w) * 8 + (lane >> 3);
        if (P_AN) {
          i = pullb(a.wv, vo, 0x95432810, 8, fl, i, NI, t0 - 8 * P_PC, a.wo, x, lw);
          asm volatile("s_waitcnt vmcnt(10)" ::: "memory");
          tb = (unsigned)__builtin_amdgcn_s_memrealtime() - 8 * P_PC + P_P0;
          i = pullb(a.wv, vo, 0x76, 2, fl, i, NI, tb + 8 * P_PC, a.wo, x, lw);
        } else i = pullb(a.wv, vo, 0x7695432810ull, 10, fl, i, NI, tb, a.wo, x, lw);
        if (r >= 18) check_in<PD_H>(a, w, F_CHK + j * 128, (UNI(tag) | 0x3FFFu) + 1);
        i = pullb(a.wq, vo, 0x76954, 5, fl, i, NI, tb + 10 * P_PC, a.wo, x, lw);
        i = pullb(a.wk, vo, 0x467958, 6, fl, i, NI, tb + 15 * P_PC, a.wo, x, lw);
        i = pullb(a.wq, vo, 0x80213, 5, fl, i, NI, tb + 21 * P_PC, a.wo, x, lw); pullb(a.wk, vo, 0x3120, 4, fl, i, NI, tb + 26 * P_PC, a.wo, x, lw);
        if (r >= 18 && !(P_MASK & 8192)) {
          const u32x4 d = desc(a.wo, DM * HD * 2);
          for (unsigned l = ((r - 18) * 4 + w) * 8 + (lane >> 3); l < 4096 * 5; l += 48 * 8) pull1<PD_O>(d, wo_piece(l, x) + (lane & 7) * 16, 0);
        }
        asm volatile("s_waitcnt vmcnt(0)" ::: "memory");
      }
      if (P_W3 && r >= 30 && role != R_P) {
        const u32x4 d = desc(a.wo, DM * HD * 2);
        for (unsigned l = 2048 * 5 + ((r - 30) * 4 + w) * 8 + (lane >> 3); l < 4096 * 5; l += x == 2 ? 32 : 64) {
          const unsigned q = l * 52429u >> 18, row = (q & 2048) + (q & 3) * 512 + ((q >> 2) & 511);
          pull1<P_W3>(d, row * 1280 + (l - q * 5) * 128 + (lane & 7) * 16 + (x & 1) * 640, 0);
          if ((unsigned)__builtin_amdgcn_s_memrealtime() - t0 > 30 * P_PC + P_P0) break;
        }
        asm volatile("s_waitcnt vmcnt(0)" ::: "memory");
      }
    } else {
      if (!(P_MASK & 512))
        pull<PD_K>(w & 2 ? a.wk : a.wv, DM * HD * 2, (lane >> 5) * 2048 + (lane & 31) * 16 + (x - 4) * 512 + (w & 1) * 4096, 8192, 640, r - 8, 24);
      const unsigned j = r >= 18 ? (r - 18) * 4 + x - 4 : x == 4 ? 48 + r : 99, hw = (r - 18) * 4 + w;
      if (P_BW) {
        const u32x4 d = desc(a.wo, DM * HD * 2);
        for (unsigned l = ((r - 8) * 4 + w) * 8 + (lane >> 3); l < P_BW * 20; l += 96 * 8) {
          const unsigned q = l * 52429u >> 18, row = (x & 2) * 1024 + (q & 3) * 512 + 511 - (q >> 2);
          pull1<PD_K>(d, row * 1280 + (l - q * 5) * 128 + (lane & 7) * 16 + (x & 1) * 640, 0);
        }
      }
      if (P_DP) while ((unsigned)__builtin_amdgcn_s_memrealtime() - t0 < P_DP) __builtin_amdgcn_s_sleep(8);
      if (r >= 18 && !(P_MASK & 1024)) {
        if (x == 4) pull<PD_F>(a.w2, DM * FF * 2, lane * 16, 1024, 2160, hw, 56);
        else {
          const char* m = w & 1 ? a.w3 : a.w1;
          const unsigned o = (x % 3) * 256 + (lane & 15) * 16;
          const unsigned i = pull<PD_F>(m, DM * FF * 2, o + (lane >> 4) * 768, FF * 2, 640, hw >> 1, 28);
          if (x != 5) pull<PD_F>(m, DM * FF * 2, o + 4 * 768 + (lane >> 4) * (FF * 2), FF * 8, 160, i, 28);
        }
      }
      if (j < 64) {
        gates(a, j * 4 + w);
        check_in<0>(a, w, F_CHK + 0x4000 + j * 128, (UNI(tag) | 0x3FFFu) + 1);
      }
      if (r >= 18 && x > 4 && !(P_MASK & 1024)) {
        const unsigned o = (x - 5) * 512 + (lane & 31) * 16 + (lane >> 5) * 1536;
        const u32x4 d = desc(a.wq, DM * HD * 2);
        for (unsigned i = hw; i < 1920; i += 56) { const unsigned row = i * 43691u >> 17, k = i - row * 3; pull1<PD_F>(d, k == 2 && x > 5 && lane >= 32 ? DROP : o, row * 8192 + k * 3072); }
      }
      asm volatile("s_waitcnt vmcnt(0)" ::: "memory");
    }
    if (role == R_G1 || role == R_Y || role == R_G2) {
      const unsigned dl = role == R_Y ? P_FY >> 16 : role == R_G1 && id < 16 ? P_FY & 0xffff : P_DF;
      while ((unsigned)__builtin_amdgcn_s_memrealtime() - t0 < dl) __builtin_amdgcn_s_sleep(8);
    }
    tag = (UNI(tag) | 0x3FFFu) + 1;
    if (role == R_Y) { role_Y(kargs(), lds, w, UNI(id / 20), UNI(id % 20)); FIN; }
    if (role == R_Z) { role_Z(kargs(), lds, w, UNI(id / 20), UNI(id % 20), tag); FIN; }
    if (role == R_G2) { role_G2(kargs(), lds, w, id, tag); FIN; }
    if (role == R_X) { role_X(kargs(), w, id, 16, tag); FIN; }
    if (role == R_P) { role_P(kargs(), w, tag, t0); FIN; }
    if (role != R_G1) FIN;
    sl = id;
  }
  if (sl < 32) { role_G1<1>(kargs(), lds, w, sl TPAS); FIN; }
  if (sl < 48) { role_G1<2>(kargs(), lds, w, sl TPAS); FIN; }
  role_G1<3>(kargs(), lds, w, sl TPAS); FIN;
}

static void launch_all(void* const* d_in, void* d_out, void* d_ws, hipStream_t stream, int dH, int dY, int dstep, int force, int mask, int plc, int fH, int fY, int dF, int dR) {
  char* ws = (char*)(((size_t)d_ws + (2u << 20) - 1) & ~(size_t)((2u << 20) - 1));
  Args a{(const char*)d_in[0], (const char*)d_in[1], (const char*)d_in[2], (const char*)d_in[3], (const char*)d_in[4], (const char*)d_in[5],
         (const char*)d_in[8], (const char*)d_in[9], (const char*)d_in[10], (const char*)d_in[11], (char*)d_out, ws, dH, dY, dstep, force, mask, plc, fH, fY, dF, dR};
  (void)hipFuncSetAttribute((const void*)k_all, hipFuncAttributeMaxDynamicSharedMemorySize, 160 * 1024);
  hipLaunchKernelGGL(k_all, dim3(256), dim3(256), 160 * 1024, stream, a);
}

extern "C" void kernel_launch(void* const* d_in, const int* in_sizes, int n_in, void* d_out, int out_size, void* d_ws, size_t ws_size, hipStream_t stream) {
  launch_all(d_in, d_out, d_ws, stream, 3065, 1525, -1, -1, 0, 1, 6, 500 + (0 << 16), 300, 1300);
}
```
